```python
import torch

def _softmax(x, axis=-1):
    return torch.softmax(x, dim=axis)
def _gelu(x):
    return torch.nn.functional.gelu(x, approximate="tanh")
def _silu(x):
    return torch.nn.functional.silu(x)
def _sigmoid(x):
    return torch.sigmoid(x)
def _layernorm(x, axis=-1, eps=1e-5):
    m = x.mean(dim=axis, keepdim=True)
    v = x.var(dim=axis, keepdim=True, unbiased=False)
    return (x - m) / torch.sqrt(v + eps)
def _scatter_add(tgt, idx, src, axis=0):
    return tgt.clone().index_add_(axis, idx, src)
def _segment_sum(src, seg, n):
    out = torch.zeros((n,) + src.shape[1:], dtype=src.dtype)
    return out.index_add_(0, seg, src)
def _pad(x, pw):
    flat = tuple(v for pair in reversed(pw) for v in pair)
    return torch.nn.functional.pad(x, flat)

kernel_name = 'gated_mix__grouped_kv_mqa__glu_gated_reglu__prompt'

def setup_inputs():

    _w = (lambda a: (lambda t: t.to(torch.bfloat16) if t.is_floating_point() else t)(torch.as_tensor(a)))
    def _A_setup():
        B, S, D, H = 1, 2048, 640, 32
        Dh = D // H
        return {
            "x": _w((lambda *s: torch.randn(*s, dtype=torch.bfloat16) * 0.05)(B, S, D)),
            "wq": _w((lambda *s: torch.randn(*s, dtype=torch.bfloat16) * 0.8)(D, H * Dh)),
            "wk": _w((lambda *s: torch.randn(*s, dtype=torch.bfloat16) * 0.8)(D, Dh)),
            "wv": _w((lambda *s: torch.randn(*s, dtype=torch.bfloat16) * 0.05)(D, Dh)),
            "wo": _w((lambda *s: torch.randn(*s, dtype=torch.bfloat16) * 0.05)(H * Dh, D)),
        }
    _A_in = _A_setup()

    def _B_setup():
        B, S, D, F = 1, 2048, 640, 1536
        return {
            "x": _w((lambda *s: torch.randn(*s, dtype=torch.bfloat16) * 0.05)(B, S, D)),
            "W_gate": _w((lambda *s: torch.randn(*s, dtype=torch.bfloat16) * 0.8)(D, F) * (2.0 / D) ** 0.5),
            "W_up": _w((lambda *s: torch.randn(*s, dtype=torch.bfloat16) * 0.8)(D, F) * (2.0 / D) ** 0.5),
            "W_down": _w((lambda *s: torch.randn(*s, dtype=torch.bfloat16) * 0.8)(F, D) * (1.0 / F) ** 0.5),
        }
    _B_in = _B_setup()

    return {
        "A_x": _A_in["x"],
        "A_wq": _A_in["wq"],
        "A_wk": _A_in["wk"],
        "A_wv": _A_in["wv"],
        "A_wo": _A_in["wo"],
        "B_x": _B_in["x"],
        "B_W_gate": _B_in["W_gate"],
        "B_W_up": _B_in["W_up"],
        "B_W_down": _B_in["W_down"],
        "gate_w": _w((lambda *s: torch.randn(*s, dtype=torch.bfloat16) * 0.05)(640) * 16.0),
    }

def reference(A_x, A_wq, A_wk, A_wv, A_wo, B_x, B_W_gate, B_W_up, B_W_down, gate_w):
    def _A_ref(x, wq, wk, wv, wo):
        B, S, D = x.shape
        H = 32
        Dh = D // H
        q = (lambda x, *d: x.permute(*d))((x @ wq).reshape(B, S, H, Dh), 0, 2, 1, 3)
        k = x @ wk
        v = x @ wv
        scores = torch.einsum("bhqd,bkd->bhqk", q, k) * (Dh ** -0.5)
        p = _softmax(scores, axis=-1)
        o = torch.einsum("bhqk,bkd->bhqd", p, v)
        return (lambda x, *d: x.permute(*d))(o, 0, 2, 1, 3).reshape(B, S, H * Dh) @ wo

    def _B_ref(x, W_gate, W_up, W_down):
        gate = torch.einsum("bsd,df->bsf", x, W_gate)
        up = torch.einsum("bsd,df->bsf", x, W_up)
        gate = gate * (lambda x, dt: x.to(dt))(gate > 0, gate.dtype)
        h = gate * up
        return torch.einsum("bsf,fd->bsd", h, W_down)

    x = A_x
    g = _sigmoid((x * gate_w).sum(-1, keepdims=True))
    a = _A_ref(x, A_wq, A_wk, A_wv, A_wo)
    b = _B_ref(x, B_W_gate, B_W_up, B_W_down)
    return g * a + (1 - g) * b
```

```cpp
#include <hip/hip_runtime.h>
#include <hip/hip_bf16.h>
#include <stdint.h>

typedef __attribute__((ext_vector_type(8))) short bf16x8;
typedef __attribute__((ext_vector_type(4))) short s16x4;
typedef __attribute__((ext_vector_type(16))) float f32x16;
typedef __attribute__((ext_vector_type(4))) float f32x4;
typedef __attribute__((ext_vector_type(2))) float f32x2;
typedef __attribute__((ext_vector_type(4))) unsigned u32x4;
typedef __attribute__((ext_vector_type(2))) unsigned u32x2;
typedef __attribute__((ext_vector_type(2))) __bf16 bf16x2_t;
typedef short v4i16_t __attribute__((ext_vector_type(4)));
typedef unsigned short u16;
#define LAS __attribute__((address_space(3)))
#define GAS __attribute__((address_space(1)))
typedef LAS const char* lds_cptr;
typedef LAS char* lds_ptr;
#define DI __device__ __forceinline__

constexpr int S_ = 2048, D_ = 640, H_ = 32, DH_ = 20, F_ = 1536;
constexpr int KV_TILE_B = 2624;
constexpr int KV_CHUNK_B = 4 * KV_TILE_B;
constexpr int KV_SLOT_B = 12288;
constexpr size_t WS_KV = 0;
constexpr size_t WS_G = 176 * 1024;
constexpr size_t WS_H = 192 * 1024;
constexpr size_t WS_O = WS_H + (size_t)64 * 48 * 2048;
constexpr int LDS_BYTES = 80 * 1024;

#define MFMA(a, b, c) __builtin_amdgcn_mfma_f32_32x32x16_bf16((a), (b), (c), 0, 0, 0)
#define SBAR() __builtin_amdgcn_sched_barrier(0)
#define PIN(x) asm volatile("" : "+v"(x))
#define WAIT_VM(N) asm volatile("s_waitcnt vmcnt(" #N ")" ::: "memory")
#define WAIT_BAR(N) asm volatile("s_waitcnt vmcnt(" #N ") lgkmcnt(0)\n\ts_barrier" ::: "memory")

DI unsigned cvtpk(float lo, float hi) {
    f32x2 v = {lo, hi};
    return __builtin_bit_cast(unsigned, __builtin_convertvector(v, bf16x2_t));
}
DI void glds16(const void* sbase, unsigned voff, unsigned lds_base) {
    unsigned sv;
    asm volatile("s_mov_b32 %0, m0\n\ts_mov_b32 m0, %3\n\ts_nop 0\n\tglobal_load_lds_dwordx4 %1, %2\n\ts_mov_b32 m0, %0"
                 : "=&s"(sv) : "v"(voff), "s"(sbase), "s"(lds_base) : "memory");
}
DI s16x4 trread(lds_cptr p) { return __builtin_bit_cast(s16x4, __builtin_amdgcn_ds_read_tr16_b64_v4i16((LAS v4i16_t*)p)); }
DI bf16x8 cat8(s16x4 lo, s16x4 hi) { return __builtin_shufflevector(lo, hi, 0, 1, 2, 3, 4, 5, 6, 7); }
DI bf16x8 ldsread16(lds_cptr p) { return *(const LAS bf16x8*)p; }
DI float xhalf_max(float m) {
    auto rr = __builtin_amdgcn_permlane32_swap(__float_as_uint(m), __float_as_uint(m), false, false);
    return __builtin_fmaxf(__uint_as_float(rr[0]), __uint_as_float(rr[1]));
}
DI f32x16 zero16() { f32x16 z; for (int i = 0; i < 16; ++i) z[i] = 0.f; return z; }

#define STAMP(kid, i) do {} while (0)
struct Params {
    const u16* x; const u16* wq; const u16* wk; const u16* wv; const u16* wo;
    const u16* wg; const u16* wu; const u16* wd; const u16* gate_w;
    u16* out; char* ws;
};

DI void kv_job(const Params& p, int job, char* lds) {
    const int tid = threadIdx.x, lane = tid & 63, w = __builtin_amdgcn_readfirstlane(tid >> 6);
    const int rt = job >> 1, isv = job & 1, r32 = lane & 31, h = lane >> 5;
    const unsigned lds0 = (unsigned)(uintptr_t)lds;
    STAMP(0, 0);
    const unsigned xoff = w * 17408, woff = xoff + 10240;
    const u16* xs = p.x + (size_t)rt * 32 * D_ + w * 160;
#pragma unroll
    for (int i = 0; i < 10; ++i) {
        const int idx = i * 64 + lane, row = idx / 20, ch = idx % 20;
        glds16(xs, (unsigned)(row * (D_ * 2) + ch * 16), lds0 + xoff + i * 1024);
    }
    const u16* wsrc = (isv ? p.wv : p.wk);
#pragma unroll
    for (int i = 0; i < 7; ++i) {
        unsigned o = (unsigned)(w * 6400 + i * 1024 + lane * 16);
        o = o > 25600u - 16u ? 25600u - 16u : o;
        glds16(wsrc, o, lds0 + woff + i * 1024);
    }
    bf16x8 gf[10];
    if (!isv) {
#pragma unroll
        for (int ks = 0; ks < 10; ++ks) gf[ks] = *(const bf16x8*)(p.gate_w + w * 160 + ks * 16 + h * 8);
    }
    WAIT_VM(0);
    const lds_cptr xb = (lds_cptr)lds + xoff + r32 * 320 + h * 16;
    const lds_cptr wb = (lds_cptr)lds + woff + (8 * h + ((lane & 15) >> 2)) * 40 + ((lane >> 4) & 1) * 32 + (lane & 3) * 8;
    f32x16 acc = zero16();
#pragma unroll
    for (int ks = 0; ks < 10; ++ks) {
        const bf16x8 xf = ldsread16(xb + ks * 32);
        bf16x8 wf = cat8(trread(wb + ks * 640), trread(wb + ks * 640 + 160));
        if (!isv) { if (r32 == 20) wf = gf[ks]; acc = MFMA(wf, xf, acc); }
        else acc = MFMA(xf, wf, acc);
    }
    STAMP(0, 1);
    LAS f32x4* red = (LAS f32x4*)((lds_ptr)lds + 69632);
    if (w > 0) {
#pragma unroll
        for (int g = 0; g < 4; ++g) red[((w - 1) * 4 + g) * 64 + lane] = (f32x4){acc[4 * g], acc[4 * g + 1], acc[4 * g + 2], acc[4 * g + 3]};
    }
    __syncthreads();
    if (w == 0) {
#pragma unroll
        for (int ww = 0; ww < 3; ++ww)
#pragma unroll
            for (int g = 0; g < 4; ++g) { const f32x4 t = red[(ww * 4 + g) * 64 + lane]; acc[4 * g] += t[0]; acc[4 * g + 1] += t[1]; acc[4 * g + 2] += t[2]; acc[4 * g + 3] += t[3]; }
        char* tile = p.ws + WS_KV + (size_t)(isv ? rt : ((rt + 63) & 63)) * KV_TILE_B;
        u32x4 lo = {cvtpk(acc[0], acc[1]), cvtpk(acc[2], acc[3]), cvtpk(acc[4], acc[5]), cvtpk(acc[6], acc[7])};
        u32x4 hi = {cvtpk(acc[8], acc[9]), cvtpk(acc[10], acc[11]), cvtpk(acc[12], acc[13]), cvtpk(acc[14], acc[15])};
        if (!isv) {
            *(u32x4*)(tile + lane * 16) = lo;
            if (h == 0) *(u32x2*)(tile + 1024 + r32 * 8) = (u32x2){hi[0], hi[1]};
            else ((float*)(p.ws + WS_G))[rt * 32 + r32] = 1.f / (1.f + __expf(-acc[8]));
        } else {
            if (r32 == 20) { lo = (u32x4){0x3f803f80u, 0x3f803f80u, 0x3f803f80u, 0x3f803f80u}; hi = lo; }
            if (r32 <= 20) {
                *(u32x4*)(tile + 1280 + h * 336 + r32 * 16) = lo;
                *(u32x4*)(tile + 1952 + h * 336 + r32 * 16) = hi;
            }
        }
    }
}

#define AE(b, i) "v_exp_f32 v[" #b "+" #i "], v[" #b "+" #i "]\n"
#define AC(pb, j, b, i) "v_cvt_pk_bf16_f32 v[" #pb "+" #j "], v[" #b "+" #i "], v[" #b "+" #i "+1]\n"
#define AG(M, X, XI, PX, PJ) M AE(X, XI) AE(X, XI+1) AE(X, XI+2) AE(X, XI+3) AC(PX, PJ, X, XI) AC(PX, PJ+1, X, XI+2)
#define AHALF(X, PX, Y, Q1, Q2, O, PR, BUF, R1, R2, R3, R4) \
    AG("v_mfma_f32_32x32x16_bf16 v[" #Y ":" #Y "+15], v[" #BUF ":" #BUF "+3], " Q1 ", 0\n" R1, X, 0, PX, 0) \
    AG("v_mfma_f32_32x32x16_bf16 v[" #Y ":" #Y "+15], v[" #BUF "+4:" #BUF "+7], " Q2 ", v[" #Y ":" #Y "+15]\n" R2, X, 4, PX, 2) \
    AG("v_mfma_f32_32x32x16_bf16 " O ", v[" #BUF "+8:" #BUF "+11], v[" #PR ":" #PR "+3], " O "\n" R3, X, 8, PX, 4) \
    AG("v_mfma_f32_32x32x16_bf16 " O ", v[" #BUF "+12:" #BUF "+15], v[" #PR "+4:" #PR "+7], " O "\n" R4, X, 12, PX, 6)
#define ARK1(NB, sl, tl) "ds_read_b128 v[" #NB ":" #NB "+3], %[k1a] offset:" #sl "*12288+" #tl "*2624\n"
#define ARK2(NB, sl, tl) "ds_read_b64 v[" #NB "+4:" #NB "+5], %[k2a] offset:" #sl "*12288+" #tl "*2624\n"
#define ARV0(NB, sl, tl) "ds_read_b128 v[" #NB "+8:" #NB "+11], %[va] offset:" #sl "*12288+" #tl "*2624\n"
#define ARV1(NB, sl, tl) "ds_read_b128 v[" #NB "+12:" #NB "+15], %[va] offset:" #sl "*12288+" #tl "*2624+672\n"
#define AH1(PREV, R1, R2, R3, R4) AHALF(128, 160, 144, "%[qb1]", "%[qb2]", "%[ob]", 168, PREV, R1, R2, R3, R4)
#define AH2(CUR, PREV, sl, tl) AHALF(144, 168, 128, "%[qa1]", "%[qa2]", "%[oa]", 160, CUR, ARK1(PREV, sl, tl), ARK2(PREV, sl, tl), ARV0(PREV, sl, tl), ARV1(PREV, sl, tl))
#define AWL "s_waitcnt lgkmcnt(0)\n"
#define ASYNC "s_waitcnt vmcnt(6) lgkmcnt(0)\ns_barrier\n"
#define ADMA0(fs) "s_mul_i32 %[of], %[ck], 10496\n s_add_u32 %[ck], %[ck], 1\n s_and_b32 %[ck], %[ck], 15\n s_add_u32 m0, %[ldst], " #fs "*12288\n" \
                  "v_add_u32 v208, %[of], %[kvo]\n"
#define ADMA1 "global_load_lds_dwordx4 v208, %[img]\n"
#define ADMA2 "global_load_lds_dwordx4 v208, %[img] offset:1024\n"
#define ADMA3 "global_load_lds_dwordx4 v208, %[img] offset:2048\n"
#define ACHUNK(sl, nsl, fs) \
    AH1(192, ADMA0(fs), ADMA1, ADMA2, ADMA3) AWL AH2(176, 192, sl, 1) \
    AH1(176, "", "", "", "") AWL AH2(192, 176, sl, 2) \
    AH1(192, "", "", "", "") AWL AH2(176, 192, sl, 3) \
    AH1(176, "", "", "", "") ASYNC AH2(192, 176, nsl, 0)
#define AMOV(d, s) "v_mov_b32 v" #d ", " s "\n"
#define ATTN_LOOP_ASM \
    "s_mov_b32 %[m0s], m0\n s_mov_b32 %[it], 4\n s_mov_b32 %[ck], 3\n" \
    "v_mov_b64 v[192:193], %[k0a]\n v_mov_b64 v[194:195], %[k0b]\n" \
    "v_mov_b64 v[196:197], %[k02]\n" AMOV(198, "%[kone]") AMOV(199, "0") AMOV(182, "%[kone]") AMOV(183, "0") \
    "v_mfma_f32_32x32x16_bf16 v[128:143], v[192:195], %[qa1], 0\n" \
    AMOV(200, "0") AMOV(201, "0") AMOV(202, "0") AMOV(203, "0") AMOV(204, "0") AMOV(205, "0") AMOV(206, "0") AMOV(207, "0") \
    AMOV(168, "0") AMOV(169, "0") AMOV(170, "0") AMOV(171, "0") AMOV(172, "0") AMOV(173, "0") AMOV(174, "0") AMOV(175, "0") \
    "v_mfma_f32_32x32x16_bf16 v[128:143], v[196:199], %[qa2], v[128:143]\n" \
    ASYNC ARK1(176, 0, 0) ARK2(176, 0, 0) ARV0(176, 0, 0) ARV1(176, 0, 0) \
    "s_nop 7\n s_nop 7\n" \
    "1:\n" \
    ACHUNK(0, 1, 3) ACHUNK(1, 2, 0) ACHUNK(2, 3, 1) ACHUNK(3, 0, 2) \
    "s_sub_u32 %[it], %[it], 1\n s_cmp_lg_u32 %[it], 0\n s_cbranch_scc1 1b\n" \
    "v_mfma_f32_32x32x16_bf16 %[ob], v[200:203], v[168:171], %[ob]\n" \
    "v_mfma_f32_32x32x16_bf16 %[ob], v[204:207], v[172:175], %[ob]\n" \
    "s_waitcnt vmcnt(0) lgkmcnt(0)\n s_mov_b32 m0, %[m0s]\n s_nop 7\n s_nop 7\n"
#define ATTN_CLOBBERS "v128","v129","v130","v131","v132","v133","v134","v135","v136","v137","v138","v139","v140","v141","v142","v143", \
    "v144","v145","v146","v147","v148","v149","v150","v151","v152","v153","v154","v155","v156","v157","v158","v159", \
    "v160","v161","v162","v163","v164","v165","v166","v167","v168","v169","v170","v171","v172","v173","v174","v175", \
    "v176","v177","v178","v179","v180","v181","v182","v183","v184","v185","v186","v187","v188","v189","v190","v191", \
    "v192","v193","v194","v195","v196","v197","v198","v199","v200","v201","v202","v203","v204","v205","v206","v207","v208"

DI void attn_wg(const Params& p, int rb, int c, char* lds) {
    const int tid = threadIdx.x, lane = tid & 63, w = __builtin_amdgcn_readfirstlane(tid >> 6);
    const int r32 = lane & 31, h = lane >> 5;
    const unsigned lds0 = (unsigned)(uintptr_t)lds;
    const char* kvimg = p.ws + WS_KV;
    const unsigned kvoff = (unsigned)(w * 3072 + lane * 16);
#define KV_DMA(chunk, slot) do { const char* s_ = kvimg + (size_t)(chunk) * KV_CHUNK_B; const unsigned d_ = lds0 + (slot) * KV_SLOT_B + w * 3072; \
        glds16(s_, kvoff, d_); glds16(s_, kvoff + 1024, d_ + 1024); glds16(s_, kvoff + 2048, d_ + 2048); } while (0)
    constexpr int QS_B = 18432, QS_0 = 2 * KV_SLOT_B;
    unsigned qoff[5]; unsigned qdst[5];
#pragma unroll
    for (int j = 0; j < 5; ++j) {
        int i = w + 4 * j; if (i >= 18) i -= 2;
        if (j < 2) { const int row = 8 * i + (lane >> 3), pos = lane & 7, ch = pos ^ ((row >> 1) & 7); qoff[j] = row * (D_ * 2) + ch * 16; }
        else { const int idx = (i - 8) * 64 + lane, kr = idx / 10, ch = idx % 10; qoff[j] = kr * (D_ * 2) + ch * 16; }
        qdst[j] = i * 1024;
    }
    const u16* xsrc = p.x + (size_t)rb * 64 * D_;
    const u16* wsrc = p.wq + c * 80;
#define Q_DMA(kt, slot) do { const unsigned d_ = lds0 + QS_0 + (slot) * QS_B; const u16* xs_ = xsrc + (kt) * 64; const u16* ws_ = wsrc + (size_t)(kt) * 64 * D_; \
        glds16(xs_, qoff[0], d_ + qdst[0]); glds16(xs_, qoff[1], d_ + qdst[1]); glds16(ws_, qoff[2], d_ + qdst[2]); glds16(ws_, qoff[3], d_ + qdst[3]); glds16(ws_, qoff[4], d_ + qdst[4]); } while (0)
    STAMP(1, 0);
    Q_DMA(0, 0); Q_DMA(1, 1);
    KV_DMA(0, 0); KV_DMA(1, 1);
    const bf16x8 k01 = *(const bf16x8*)(kvimg + 63 * KV_TILE_B + lane * 16);
    const u32x2 k02 = *(const u32x2*)(kvimg + 63 * KV_TILE_B + 1024 + r32 * 8);
    f32x16 qa = zero16(), qb = zero16();
    {
        unsigned xa[4];
#pragma unroll
        for (int ks = 0; ks < 4; ++ks) xa[ks] = r32 * 128 + (((2 * ks + h) ^ ((r32 >> 1) & 7)) << 4);
        const unsigned wa = 8192 + (8 * h + ((lane & 15) >> 2)) * 160 + w * 40 + ((lane >> 4) & 1) * 32 + (lane & 3) * 8;
        int slot = 0;
        for (int kt = 0; kt < 10; ++kt) {
            if (kt == 0) { WAIT_BAR(11); } else if (kt == 1) { WAIT_BAR(11); } else if (kt == 9) { WAIT_BAR(0); } else { WAIT_BAR(5); }
            if (kt + 2 < 10) { const int s2 = slot == 0 ? 2 : slot - 1; Q_DMA(kt + 2, s2); }
            const lds_cptr sb = (lds_cptr)lds + QS_0 + slot * QS_B;
#pragma unroll
            for (int ks = 0; ks < 4; ++ks) {
                const bf16x8 wf = cat8(trread(sb + wa + ks * 2560), trread(sb + wa + ks * 2560 + 640));
                const bf16x8 x0 = ldsread16(sb + xa[ks]), x1 = ldsread16(sb + xa[ks] + 4096);
                qa = MFMA(wf, x0, qa); qb = MFMA(wf, x1, qb);
            }
            slot = slot == 2 ? 0 : slot + 1;
        }
    }
    STAMP(1, 1);
    constexpr float C2 = 0.22360679774997896f * 1.4426950408889634f;
    u32x4 qa1, qa2, qb1, qb2;
    qa1 = (u32x4){cvtpk(qa[0] * C2, qa[1] * C2), cvtpk(qa[2] * C2, qa[3] * C2), cvtpk(qa[4] * C2, qa[5] * C2), cvtpk(qa[6] * C2, qa[7] * C2)};
    qb1 = (u32x4){cvtpk(qb[0] * C2, qb[1] * C2), cvtpk(qb[2] * C2, qb[3] * C2), cvtpk(qb[4] * C2, qb[5] * C2), cvtpk(qb[6] * C2, qb[7] * C2)};
    qa2 = (u32x4){h ? 0u : cvtpk(qa[8] * C2, qa[9] * C2), h ? 0u : cvtpk(qa[10] * C2, qa[11] * C2), 0u, 0u};
    qb2 = (u32x4){h ? 0u : cvtpk(qb[8] * C2, qb[9] * C2), h ? 0u : cvtpk(qb[10] * C2, qb[11] * C2), 0u, 0u};
    WAIT_BAR(0);
    KV_DMA(2, 2);
    const unsigned k1a = lane * 16, k2a = 1024 + r32 * 8;
    const unsigned va = 1280 + h * 336 + (r32 <= 20 ? r32 : (r32 < 28 ? 20 : 16)) * 16;
    const unsigned kone = h ? 0u : 0x3f80u;
    auto kfrag2 = [](u32x2 v, unsigned one) { return __builtin_bit_cast(bf16x8, (u32x4){v[0], v[1], one, 0u}); };
#define BC(x) __builtin_bit_cast(bf16x8, x)
    {
        f32x16 sa = MFMA(k01, BC(qa1), zero16()); sa = MFMA(kfrag2(k02, kone), BC(qa2), sa);
        f32x16 sb = MFMA(k01, BC(qb1), zero16()); sb = MFMA(kfrag2(k02, kone), BC(qb2), sb);
        float ma = sa[0], mb = sb[0];
#pragma unroll
        for (int r = 1; r < 16; ++r) { ma = __builtin_fmaxf(ma, sa[r]); mb = __builtin_fmaxf(mb, sb[r]); }
        ma = xhalf_max(ma); mb = xhalf_max(mb);
        qa2[2] = h ? 0u : (cvtpk(-ma, 0.f) & 0xffffu);
        qb2[2] = h ? 0u : (cvtpk(-mb, 0.f) & 0xffffu);
    }
    f32x16 oa = zero16(), ob = zero16();
    STAMP(1, 2);
    {
        unsigned t_m0, t_it, t_ck, t_of;
        const u32x4 k0u = __builtin_bit_cast(u32x4, k01); const u32x2 k0a = {k0u[0], k0u[1]}, k0b = {k0u[2], k0u[3]};
        asm volatile(ATTN_LOOP_ASM
            : [oa] "+v"(oa), [ob] "+v"(ob), [m0s] "=&s"(t_m0), [it] "=&s"(t_it), [ck] "=&s"(t_ck), [of] "=&s"(t_of)
            : [qa1] "v"(qa1), [qa2] "v"(qa2), [qb1] "v"(qb1), [qb2] "v"(qb2), [k1a] "v"(lds0 + k1a), [k2a] "v"(lds0 + k2a), [va] "v"(lds0 + va),
              [kvo] "v"(kvoff), [img] "s"(kvimg), [ldst] "s"(lds0 + w * 3072), [k0a] "v"(k0a), [k0b] "v"(k0b), [k02] "v"(k02), [kone] "v"(kone)
            : ATTN_CLOBBERS, "scc", "memory");
    }
    STAMP(1, 3);
    f32x16 sa, sb; u32x4 pa0, pa1, pb0, pb1;
    {
        float chk = 0.f;
#pragma unroll
        for (int r = 0; r < 8; ++r) chk += oa[r] * 0.f + ob[r] * 0.f;
        if (h == 0) { chk += (oa[8] + oa[9] + oa[10] + oa[11] + ob[8] + ob[9] + ob[10] + ob[11]) * 0.f; } else { chk += (oa[8] + ob[8]) * 0.f; }
        if (h == 1 && !(oa[8] > 1e-30f && ob[8] > 1e-30f)) chk = 1.f;
        if (__builtin_expect(__any(!(chk == 0.f)), 0)) {
            qa2[2] = 0u; qb2[2] = 0u;
            oa = zero16(); ob = zero16();
            float ma = -INFINITY, mb = -INFINITY;
            for (int t = 0; t < 64; ++t) {
                const char* tb = kvimg + (size_t)t * KV_TILE_B;
                const char* kb = kvimg + (size_t)((t + 63) & 63) * KV_TILE_B;
                const bf16x8 k1 = *(const bf16x8*)(kb + k1a), k2 = kfrag2(*(const u32x2*)(kb + k2a), kone);
                const bf16x8 v0 = *(const bf16x8*)(tb + va), v1 = *(const bf16x8*)(tb + va + 672);
                sa = MFMA(k1, BC(qa1), zero16()); sa = MFMA(k2, BC(qa2), sa);
                sb = MFMA(k1, BC(qb1), zero16()); sb = MFMA(k2, BC(qb2), sb);
                float ta = ma, tb_ = mb;
#pragma unroll
                for (int r = 0; r < 16; ++r) { ta = __builtin_fmaxf(ta, sa[r]); tb_ = __builtin_fmaxf(tb_, sb[r]); }
                ta = xhalf_max(ta); tb_ = xhalf_max(tb_);
                const float fa = __builtin_amdgcn_exp2f(ma - ta), fb = __builtin_amdgcn_exp2f(mb - tb_);
                ma = ta; mb = tb_;
#pragma unroll
                for (int r = 0; r < 16; ++r) { oa[r] *= fa; ob[r] *= fb; sa[r] = __builtin_amdgcn_exp2f(sa[r] - ma); sb[r] = __builtin_amdgcn_exp2f(sb[r] - mb); }
#pragma unroll
                for (int j = 0; j < 4; ++j) { pa0[j] = cvtpk(sa[2 * j], sa[2 * j + 1]); pa1[j] = cvtpk(sa[8 + 2 * j], sa[9 + 2 * j]); pb0[j] = cvtpk(sb[2 * j], sb[2 * j + 1]); pb1[j] = cvtpk(sb[8 + 2 * j], sb[9 + 2 * j]); }
                oa = MFMA(v0, BC(pa0), oa); oa = MFMA(v1, BC(pa1), oa);
                ob = MFMA(v0, BC(pb0), ob); ob = MFMA(v1, BC(pb1), ob);
            }
        }
    }
    const float* gv = (const float*)(p.ws + WS_G);
    const float ga = gv[rb * 64 + r32], gb = gv[rb * 64 + 32 + r32];
    const float la = __shfl(oa[8], r32 + 32), lb = __shfl(ob[8], r32 + 32);
    const float fa = ga / la, fb = gb / lb;
    const int head = 4 * c + w;
    char* oimg = p.ws + WS_O + (size_t)(rb * 2) * 40 * 1024;
#pragma unroll
    for (int qd = 0; qd < 3; ++qd) {
        const int Q = head * 5 + 2 * qd + h;
        const unsigned off = (Q >> 2) * 1024 + (((Q >> 1) & 1) * 32 + r32) * 16 + (Q & 1) * 8;
        const u32x2 ua = {cvtpk(oa[4 * qd] * fa, oa[4 * qd + 1] * fa), cvtpk(oa[4 * qd + 2] * fa, oa[4 * qd + 3] * fa)};
        const u32x2 ub = {cvtpk(ob[4 * qd] * fb, ob[4 * qd + 1] * fb), cvtpk(ob[4 * qd + 2] * fb, ob[4 * qd + 3] * fb)};
        if (qd < 2 || h == 0) { *(u32x2*)(oimg + off) = ua; *(u32x2*)(oimg + 40 * 1024 + off) = ub; }
    }
    STAMP(1, 4);
#undef KV_DMA
#undef Q_DMA
}

DI void ffn_up_wg(const Params& p, int rb2, int fs, char* lds) {
    const int tid = threadIdx.x, lane = tid & 63, w = __builtin_amdgcn_readfirstlane(tid >> 6);
    const int r32 = lane & 31, h = lane >> 5;
    const unsigned lds0 = (unsigned)(uintptr_t)lds;
    constexpr int ST_B = 20480;
    unsigned xo[2], wo_[3];
#pragma unroll
    for (int j = 0; j < 2; ++j) { const int row = 16 * (w + 4 * j) + (lane >> 2), ch = (lane & 3) ^ ((row >> 2) & 3); xo[j] = row * (D_ * 2) + ch * 16; }
#pragma unroll
    for (int j = 0; j < 3; ++j) { const int i = w + 4 * j, nt = i >> 1, kr = 16 * (i & 1) + (lane >> 2); wo_[j] = kr * (F_ * 2) + (nt % 3) * 64 + (lane & 3) * 16; }
    const u16* xsrc = p.x + (size_t)rb2 * 128 * D_;
    const u16* gsrc = p.wg + fs * 96;
    const u16* usrc = p.wu + fs * 96;
    const u16* w1src = (w + 4 < 6) ? gsrc : usrc;
#define UP_DMA(kt, slot) do { const unsigned d_ = lds0 + (slot) * ST_B; const size_t wk_ = (size_t)(kt) * 32 * F_; \
        glds16(xsrc + (kt) * 32, xo[0], d_ + w * 1024); glds16(xsrc + (kt) * 32, xo[1], d_ + (w + 4) * 1024); \
        glds16(gsrc + wk_, wo_[0], d_ + 8192 + w * 1024); glds16(w1src + wk_, wo_[1], d_ + 8192 + (w + 4) * 1024); glds16(usrc + wk_, wo_[2], d_ + 8192 + (w + 8) * 1024); } while (0)
    STAMP(1, 0);
    UP_DMA(0, 0); UP_DMA(1, 1); UP_DMA(2, 2);
    f32x16 acc[6];
#pragma unroll
    for (int i = 0; i < 6; ++i) acc[i] = zero16();
    const int row = 32 * w + r32;
    const unsigned xa0 = row * 64 + ((h ^ ((row >> 2) & 3)) << 4), xa1 = row * 64 + (((2 + h) ^ ((row >> 2) & 3)) << 4);
    const unsigned wa = 8192 + (8 * h + ((lane & 15) >> 2)) * 64 + ((lane >> 4) & 1) * 32 + (lane & 3) * 8;
    for (int kt = 0; kt < 20; ++kt) {
        if (kt < 18) { WAIT_BAR(10); } else if (kt == 18) { WAIT_BAR(5); } else { WAIT_BAR(0); }
        if (kt + 3 < 20) UP_DMA(kt + 3, (kt + 3) & 3);
        const lds_cptr sb = (lds_cptr)lds + (kt & 3) * ST_B;
#pragma unroll
        for (int ks = 0; ks < 2; ++ks) {
            const bf16x8 xf = ldsread16(sb + (ks ? xa1 : xa0));
#pragma unroll
            for (int nt = 0; nt < 6; ++nt) {
                const bf16x8 wf = cat8(trread(sb + wa + nt * 2048 + ks * 1024), trread(sb + wa + nt * 2048 + ks * 1024 + 256));
                acc[nt] = MFMA(wf, xf, acc[nt]);
            }
        }
    }
    STAMP(1, 1);
    char* himg = p.ws + WS_H + ((size_t)(rb2 * 4 + w) * 48 + fs * 3) * 2048 + lane * 16;
#pragma unroll
    for (int j = 0; j < 3; ++j) {
        float hv[16];
#pragma unroll
        for (int r = 0; r < 16; ++r) hv[r] = __builtin_fmaxf(acc[j][r], 0.f) * acc[3 + j][r];
        *(u32x4*)(himg + j * 2048) = (u32x4){cvtpk(hv[0], hv[1]), cvtpk(hv[2], hv[3]), cvtpk(hv[4], hv[5]), cvtpk(hv[6], hv[7])};
        *(u32x4*)(himg + j * 2048 + 1024) = (u32x4){cvtpk(hv[8], hv[9]), cvtpk(hv[10], hv[11]), cvtpk(hv[12], hv[13]), cvtpk(hv[14], hv[15])};
    }
    STAMP(1, 2);
#undef UP_DMA
}

DI void out_wg(const Params& p, int rb, int cn, char* lds) {
    const int tid = threadIdx.x, lane = tid & 63, w = __builtin_amdgcn_readfirstlane(tid >> 6);
    const int r32 = lane & 31, h = lane >> 5;
    const unsigned lds0 = (unsigned)(uintptr_t)lds + w * 20480;
    unsigned wo_[5];
#pragma unroll
    for (int j = 0; j < 5; ++j) { const int idx = j * 64 + lane; wo_[j] = (idx / 10) * (D_ * 2) + (idx % 10) * 16; }
    const u16* dsrc = p.wd + (size_t)w * 384 * D_ + cn * 80;
    const u16* osrc = p.wo + (size_t)w * 160 * D_ + cn * 80;
    const char* hsrc = p.ws + WS_H + ((size_t)(rb * 2) * 48 + w * 12) * 2048 + lane * 16;
    const char* asrc = p.ws + WS_O + ((size_t)(rb * 2) * 40 + w * 10) * 1024 + lane * 16;
#define W_DMA(u) do { const u16* s_ = (u) < 12 ? dsrc + (size_t)(u) * 32 * D_ : osrc + (size_t)((u) - 12) * 32 * D_; const unsigned d_ = lds0 + ((u) & 3) * 5120; \
        glds16(s_, wo_[0], d_); glds16(s_, wo_[1], d_ + 1024); glds16(s_, wo_[2], d_ + 2048); glds16(s_, wo_[3], d_ + 3072); glds16(s_, wo_[4], d_ + 4096); } while (0)
    const float* gv = (const float*)(p.ws + WS_G);
    const float g0 = 1.f - gv[rb * 64 + r32], g1 = 1.f - gv[rb * 64 + 32 + r32];
    f32x16 acc[2][3];
#pragma unroll
    for (int i = 0; i < 2; ++i)
#pragma unroll
        for (int j = 0; j < 3; ++j) acc[i][j] = zero16();
    const lds_cptr wbase = (lds_cptr)lds + w * 20480 + ((lane & 15) >> 2) * 160 + ((lane >> 4) & 1) * 32 + (lane & 3) * 8;
    STAMP(2, 0);
    W_DMA(0); W_DMA(1); W_DMA(2);
#pragma unroll
    for (int u = 0; u < 17; ++u) {
        bf16x8 af[2][2];
#pragma unroll
        for (int rt = 0; rt < 2; ++rt)
#pragma unroll
            for (int s = 0; s < 2; ++s)
                af[rt][s] = (u < 12) ? *(const bf16x8*)(hsrc + (size_t)rt * 48 * 2048 + u * 2048 + s * 1024)
                                     : *(const bf16x8*)(asrc + (size_t)rt * 40 * 1024 + ((u - 12) * 2 + s) * 1024);
        WAIT_VM(0);
        if (u + 3 < 17) W_DMA(u + 3);
        const lds_cptr ub = wbase + (u & 3) * 5120 + (u < 12 ? 4 * h : 8 * h) * 160;
        const int tstep = (u < 12 ? 8 : 4) * 160;
#pragma unroll
        for (int s = 0; s < 2; ++s)
#pragma unroll
            for (int nt = 0; nt < 3; ++nt) {
                const bf16x8 wf = cat8(trread(ub + s * 2560 + nt * 64), trread(ub + s * 2560 + nt * 64 + tstep));
                acc[0][nt] = MFMA(wf, af[0][s], acc[0][nt]);
                acc[1][nt] = MFMA(wf, af[1][s], acc[1][nt]);
            }
        if (u == 11) {
#pragma unroll
            for (int nt = 0; nt < 3; ++nt)
#pragma unroll
                for (int r = 0; r < 16; ++r) { acc[0][nt][r] *= g0; acc[1][nt][r] *= g1; }
        }
        asm volatile("s_waitcnt lgkmcnt(0)" ::: "memory");
    }
    STAMP(2, 1);
    LAS f32x4* part = (LAS f32x4*)((lds_ptr)lds + w * 20480);
#pragma unroll
    for (int rt = 0; rt < 2; ++rt)
#pragma unroll
        for (int nt = 0; nt < 3; ++nt)
#pragma unroll
            for (int g = 0; g < 4; ++g) {
                if (nt == 2 && g >= 2) continue;
                const int nq = nt * 8 + 2 * g + h;
                part[(rt * 20 + nq) * 32 + r32] = (f32x4){acc[rt][nt][4 * g], acc[rt][nt][4 * g + 1], acc[rt][nt][4 * g + 2], acc[rt][nt][4 * g + 3]};
            }
    __syncthreads();
    const LAS f32x4* pr = (const LAS f32x4*)lds;
    for (int it = tid; it < 640; it += 256) {
        const int row = it / 10, ng = it % 10, rt = row >> 5, r = row & 31;
        f32x4 a = {0.f, 0.f, 0.f, 0.f}, b = a;
#pragma unroll
        for (int ww = 0; ww < 4; ++ww) { a += pr[ww * 1280 + (rt * 20 + 2 * ng) * 32 + r]; b += pr[ww * 1280 + (rt * 20 + 2 * ng + 1) * 32 + r]; }
        *(u32x4*)(p.out + (size_t)(rb * 64 + row) * D_ + cn * 80 + ng * 8) = (u32x4){cvtpk(a[0], a[1]), cvtpk(a[2], a[3]), cvtpk(b[0], b[1]), cvtpk(b[2], b[3])};
    }
    STAMP(2, 2);
#undef W_DMA
}

__global__ __launch_bounds__(256, 2) void k_kv(Params p) {
    extern __shared__ __attribute__((aligned(16))) char lds[];
    kv_job(p, blockIdx.x, lds);
}
__global__ __launch_bounds__(256, 2) void k_main(Params p) {
    extern __shared__ __attribute__((aligned(16))) char lds[];
    const int b = blockIdx.x;
    if (b < 256) attn_wg(p, b >> 3, b & 7, lds);
    if (b >= 256) { const int u = b - 256; ffn_up_wg(p, u >> 4, u & 15, lds); }
}
__global__ __launch_bounds__(256, 2) void k_out(Params p) {
    extern __shared__ __attribute__((aligned(16))) char lds[];
    out_wg(p, blockIdx.x >> 3, blockIdx.x & 7, lds);
}

extern "C" void kernel_launch(void* const* d_in, const int* in_sizes, int n_in, void* d_out, int out_size, void* d_ws, size_t ws_size,
                              hipStream_t stream) {
    Params p;
    p.x = (const u16*)d_in[0]; p.wq = (const u16*)d_in[1]; p.wk = (const u16*)d_in[2]; p.wv = (const u16*)d_in[3]; p.wo = (const u16*)d_in[4];
    p.wg = (const u16*)d_in[6]; p.wu = (const u16*)d_in[7]; p.wd = (const u16*)d_in[8]; p.gate_w = (const u16*)d_in[9];
    p.out = (u16*)d_out; p.ws = (char*)d_ws;
    (void)hipFuncSetAttribute((const void*)k_kv, hipFuncAttributeMaxDynamicSharedMemorySize, LDS_BYTES);
    (void)hipFuncSetAttribute((const void*)k_main, hipFuncAttributeMaxDynamicSharedMemorySize, LDS_BYTES);
    (void)hipFuncSetAttribute((const void*)k_out, hipFuncAttributeMaxDynamicSharedMemorySize, LDS_BYTES);
    k_kv<<<128, 256, LDS_BYTES, stream>>>(p);
    k_main<<<512, 256, LDS_BYTES, stream>>>(p);
    k_out<<<256, 256, LDS_BYTES, stream>>>(p);
}
```

```cpp
#include <hip/hip_runtime.h>
#include <hip/hip_bf16.h>
#include <stdint.h>

typedef __attribute__((ext_vector_type(8))) short bf16x8;
typedef __attribute__((ext_vector_type(4))) short s16x4;
typedef __attribute__((ext_vector_type(16))) float f32x16;
typedef __attribute__((ext_vector_type(4))) float f32x4;
typedef __attribute__((ext_vector_type(2))) float f32x2;
typedef __attribute__((ext_vector_type(4))) unsigned u32x4;
typedef __attribute__((ext_vector_type(2))) unsigned u32x2;
typedef __attribute__((ext_vector_type(2))) __bf16 bf16x2_t;
typedef short v4i16_t __attribute__((ext_vector_type(4)));
typedef unsigned short u16;
typedef unsigned long long u64;
#define LAS __attribute__((address_space(3)))
#define GAS __attribute__((address_space(1)))
#define CAS __attribute__((address_space(4)))
typedef LAS const char* lds_cptr;
typedef LAS char* lds_ptr;
#define DI __device__ __forceinline__

constexpr int D_ = 640, F_ = 1536;
constexpr int KV_TILE_B = 2624;
constexpr int KV_CHUNK_B = 4 * KV_TILE_B;
constexpr int KV_SLOT_B = 12288;
constexpr size_t WS_EPOCH = 0, WS_SEEN = 1024, WS_HF = 8192, WS_OF = 12288, WS_KVF = 16384, WS_WF = 32768;
constexpr size_t WS_KV = 64 * 1024;
constexpr size_t WS_G = 240 * 1024;
constexpr size_t WS_H = 256 * 1024;
constexpr size_t WS_O = WS_H + (size_t)64 * 48 * 2048;
constexpr size_t WS_WT = WS_O + (size_t)64 * 40 * 1024;
constexpr size_t WS_END = WS_WT + (size_t)136 * 20480;
constexpr int LDS_BYTES = 80 * 1024;
constexpr unsigned SPIN_CAP = 1u << 18;

#define MFMA(a, b, c) __builtin_amdgcn_mfma_f32_32x32x16_bf16((a), (b), (c), 0, 0, 0)
#define SBAR() __builtin_amdgcn_sched_barrier(0)
#define WAIT_VM(N) asm volatile("s_waitcnt vmcnt(" #N ")" ::: "memory")
#define WAIT_BAR(N) asm volatile("s_waitcnt vmcnt(" #N ") lgkmcnt(0)\n\ts_barrier" ::: "memory")
#define PACE() do {} while (0)
#define RLX_AGENT __ATOMIC_RELAXED, __HIP_MEMORY_SCOPE_AGENT
#define BC(x) __builtin_bit_cast(bf16x8, x)

DI unsigned cvtpk(float lo, float hi) {
    f32x2 v = {lo, hi};
    return __builtin_bit_cast(unsigned, __builtin_convertvector(v, bf16x2_t));
}
DI void glds16(const void* sbase, unsigned voff, unsigned lds_base) {
    unsigned sv;
    asm volatile("s_mov_b32 %0, m0\n\ts_mov_b32 m0, %3\n\ts_nop 0\n\tglobal_load_lds_dwordx4 %1, %2\n\ts_mov_b32 m0, %0"
                 : "=&s"(sv) : "v"(voff), "s"(sbase), "s"(lds_base) : "memory");
}
DI void glds16v(const void* ptr, unsigned lds_base) {
    unsigned sv;
    asm volatile("s_mov_b32 %0, m0\n\ts_mov_b32 m0, %2\n\ts_nop 0\n\tglobal_load_lds_dwordx4 %1, off\n\ts_mov_b32 m0, %0"
                 : "=&s"(sv) : "v"(ptr), "s"(lds_base) : "memory");
}
DI void glds16_dev(const void* sbase, unsigned voff, unsigned lds_base) {
    unsigned sv;
    asm volatile("s_mov_b32 %0, m0\n\ts_mov_b32 m0, %3\n\ts_nop 0\n\tglobal_load_lds_dwordx4 %1, %2 sc1\n\ts_mov_b32 m0, %0"
                 : "=&s"(sv) : "v"(voff), "s"(sbase), "s"(lds_base) : "memory");
}
DI s16x4 trread(lds_cptr p) { return __builtin_bit_cast(s16x4, __builtin_amdgcn_ds_read_tr16_b64_v4i16((LAS v4i16_t*)p)); }
DI bf16x8 cat8(s16x4 lo, s16x4 hi) { return __builtin_shufflevector(lo, hi, 0, 1, 2, 3, 4, 5, 6, 7); }
DI bf16x8 ldsread16(lds_cptr p) { return *(const LAS bf16x8*)p; }
DI float xhalf_max(float m) {
    auto rr = __builtin_amdgcn_permlane32_swap(__float_as_uint(m), __float_as_uint(m), false, false);
    return __builtin_fmaxf(__uint_as_float(rr[0]), __uint_as_float(rr[1]));
}
DI f32x16 zero16() { f32x16 z; for (int i = 0; i < 16; ++i) z[i] = 0.f; return z; }
DI void st16_dev(__amdgpu_buffer_rsrc_t r, unsigned off, u32x4 v) { __builtin_amdgcn_raw_buffer_store_b128(v, r, off, 0, 16); }
DI void st8_dev(void* p, u32x2 v) { __hip_atomic_store((GAS u64*)p, (u64)v[0] | ((u64)v[1] << 32), RLX_AGENT); }
DI void st4_dev(void* p, float v) { __hip_atomic_store((GAS unsigned*)p, __float_as_uint(v), RLX_AGENT); }
DI u64 ld8_dev(const void* p) { return __hip_atomic_load((const GAS u64*)p, RLX_AGENT); }
DI float ld4_dev(const void* p) { return __uint_as_float(__hip_atomic_load((const GAS unsigned*)p, RLX_AGENT)); }
DI void flag_set(char* ws, size_t off, u64 tag) { __hip_atomic_store((GAS u64*)(ws + off), tag, RLX_AGENT); }
DI u64 epoch_load(const char* ws) { u64 e; asm volatile("s_load_dwordx2 %0, %1, 0x0" : "=s"(e) : "s"(ws) : "memory"); return e; }
DI u64 epoch_tag(u64 e) { u64 r; asm volatile("s_waitcnt lgkmcnt(0)\n\ts_mov_b64 %0, %1" : "=s"(r) : "s"(e) : "memory"); return r * 6364136223846793005ull + 1442695040888963407ull; }
template <int K, int SLEEP> DI void flags_wait(const char* f, int n, u64 tag, int lane) {
    for (unsigned spin = 0; spin < SPIN_CAP; ++spin) {
        bool ok = true;
#pragma unroll
        for (int k = 0; k < K; ++k) { const int i = lane + 64 * k; const u64 v = ld8_dev(f + 8 * (i < n ? i : 0)); ok &= (v == tag); }
        if (__all(ok)) break;
        __builtin_amdgcn_s_sleep(SLEEP);
    }
}

#define STAMP(kid, i) do {} while (0)
struct Params {
    const u16* x; const u16* wq; const u16* wk; const u16* wv; const u16* wo;
    const u16* wg; const u16* wu; const u16* wd; const u16* gate_w;
    u16* out; char* ws;
};

DI void kv_job(const Params& p, int rt, int isv, u64 ep, char* lds) {
    const int tid = threadIdx.x, lane = tid & 63, w = __builtin_amdgcn_readfirstlane(tid >> 6);
    const int r32 = lane & 31, h = lane >> 5;
    const unsigned lds0 = (unsigned)(uintptr_t)lds;
    const unsigned xoff = w * 17408, woff = xoff + 10240;
    const u16* xs = p.x + (size_t)rt * 32 * D_ + w * 160;
#pragma unroll
    for (int i = 0; i < 10; ++i) {
        const int idx = i * 64 + lane, row = idx / 20, ch = idx % 20;
        glds16(xs, (unsigned)(row * (D_ * 2) + ch * 16), lds0 + xoff + i * 1024);
    }
    const u16* wsrc = (isv ? p.wv : p.wk);
#pragma unroll
    for (int i = 0; i < 6; ++i) glds16(wsrc, (unsigned)(w * 6400 + i * 1024 + lane * 16), lds0 + woff + i * 1024);
    glds16v(lane < 16 ? (const char*)wsrc + w * 6400 + 6144 + lane * 16 : (const char*)p.gate_w + w * 320 + (lane < 36 ? lane - 16 : 0) * 16, lds0 + woff + 6144);
    const u64 tag = epoch_tag(ep);
    WAIT_VM(0);
    const lds_cptr xb = (lds_cptr)lds + xoff + r32 * 320 + h * 16;
    const lds_cptr wb = (lds_cptr)lds + woff + (8 * h + ((lane & 15) >> 2)) * 40 + ((lane >> 4) & 1) * 32 + (lane & 3) * 8;
    const lds_cptr gb = (lds_cptr)lds + woff + 6400 + h * 16;
    bf16x8 xf[10], wf[10], gf[10];
#pragma unroll
    for (int ks = 0; ks < 10; ++ks) { xf[ks] = ldsread16(xb + ks * 32); wf[ks] = cat8(trread(wb + ks * 640), trread(wb + ks * 640 + 160)); gf[ks] = ldsread16(gb + ks * 32); }
    SBAR();
    f32x16 acc = zero16();
#pragma unroll
    for (int ks = 0; ks < 10; ++ks) {
        const bf16x8 wg = (r32 == 20) ? gf[ks] : wf[ks];
        const bf16x8 fa = isv ? xf[ks] : wg, fb = isv ? wg : xf[ks];
        acc = MFMA(fa, fb, acc);
    }
    LAS f32x4* red = (LAS f32x4*)((lds_ptr)lds + 69632);
    if (w > 0) {
#pragma unroll
        for (int g = 0; g < 4; ++g) red[((w - 1) * 4 + g) * 64 + lane] = (f32x4){acc[4 * g], acc[4 * g + 1], acc[4 * g + 2], acc[4 * g + 3]};
    }
    WAIT_BAR(0);
    if (w == 0) {
#pragma unroll
        for (int ww = 0; ww < 3; ++ww)
#pragma unroll
            for (int g = 0; g < 4; ++g) { const f32x4 t = red[(ww * 4 + g) * 64 + lane]; acc[4 * g] += t[0]; acc[4 * g + 1] += t[1]; acc[4 * g + 2] += t[2]; acc[4 * g + 3] += t[3]; }
    }
    WAIT_BAR(0);
    if (w == 0) {
        const __amdgpu_buffer_rsrc_t rs = __builtin_amdgcn_make_buffer_rsrc(p.ws, 0, (int)WS_END, 0x00020000);
        const unsigned tile = (unsigned)WS_KV + (unsigned)(isv ? rt : ((rt + 63) & 63)) * KV_TILE_B;
        u32x4 lo = {cvtpk(acc[0], acc[1]), cvtpk(acc[2], acc[3]), cvtpk(acc[4], acc[5]), cvtpk(acc[6], acc[7])};
        u32x4 hi = {cvtpk(acc[8], acc[9]), cvtpk(acc[10], acc[11]), cvtpk(acc[12], acc[13]), cvtpk(acc[14], acc[15])};
        if (!isv) {
            st16_dev(rs, tile + lane * 16, lo);
            if (h == 0) st8_dev(p.ws + tile + 1024 + r32 * 8, (u32x2){hi[0], hi[1]});
            else st4_dev(p.ws + WS_G + (rt * 32 + r32) * 4, 1.f / (1.f + __expf(-acc[8])));
        } else {
            if (r32 == 20) { lo = (u32x4){0x3f803f80u, 0x3f803f80u, 0x3f803f80u, 0x3f803f80u}; hi = lo; }
            if (r32 <= 20) { st16_dev(rs, tile + 1280 + h * 336 + r32 * 16, lo); st16_dev(rs, tile + 1952 + h * 336 + r32 * 16, hi); }
        }
        WAIT_VM(0);
        if (lane < 16) flag_set(p.ws, WS_KVF + lane * 1024 + (rt * 2 + isv) * 8, tag);
    }
}

DI void wt_job(const Params& p, int ks, u64 ep, char* lds) {
    const int tid = threadIdx.x, lane = tid & 63, w = __builtin_amdgcn_readfirstlane(tid >> 6);
    const int r32 = lane & 31, h = lane >> 5;
    const unsigned lds0 = (unsigned)(uintptr_t)lds;
    const u16* src = ks < 96 ? p.wd + (size_t)ks * 16 * D_ : p.wo + (size_t)(ks - 96) * 16 * D_;
#pragma unroll
    for (int i = 0; i < 5; ++i) glds16(src, (unsigned)((w * 5 + i) * 1024 + lane * 16), lds0 + (w * 5 + i) * 1024);
    const u64 tag = epoch_tag(ep);
    WAIT_BAR(0);
    const __amdgpu_buffer_rsrc_t rs = __builtin_amdgcn_make_buffer_rsrc(p.ws, 0, (int)WS_END, 0x00020000);
    const lds_cptr tb = (lds_cptr)lds + (4 * h + ((lane & 15) >> 2)) * 1280 + ((lane >> 4) & 1) * 32 + (lane & 3) * 8 + w * 320;
    const unsigned dst = (unsigned)WS_WT + ks * 20480 + w * 5120 + (r32 >> 4) * 512 + h * 256 + (r32 & 15) * 16;
#pragma unroll
    for (int i = 0; i < 5; ++i) st16_dev(rs, dst + i * 1024, __builtin_bit_cast(u32x4, cat8(trread(tb + i * 64), trread(tb + i * 64 + 8 * 1280))));
    WAIT_BAR(0);
    if (tid < 8) flag_set(p.ws, WS_WF + tid * 2048 + ks * 8, tag);
}

#define AE(b, i) "v_exp_f32 v[" #b "+" #i "], v[" #b "+" #i "]\n"
#define AC(pb, j, b, i) "v_cvt_pk_bf16_f32 v[" #pb "+" #j "], v[" #b "+" #i "], v[" #b "+" #i "+1]\n"
#define AG(M, X, XI, PX, PJ) M AE(X, XI) AE(X, XI+1) AE(X, XI+2) AE(X, XI+3) AC(PX, PJ, X, XI) AC(PX, PJ+1, X, XI+2)
#define AHALF(X, PX, Y, Q1, Q2, O, PR, BUF, R1, R2, R3, R4) \
    AG("v_mfma_f32_32x32x16_bf16 v[" #Y ":" #Y "+15], v[" #BUF ":" #BUF "+3], " Q1 ", 0\n" R1, X, 0, PX, 0) \
    AG("v_mfma_f32_32x32x16_bf16 v[" #Y ":" #Y "+15], v[" #BUF "+4:" #BUF "+7], " Q2 ", v[" #Y ":" #Y "+15]\n" R2, X, 4, PX, 2) \
    AG("v_mfma_f32_32x32x16_bf16 " O ", v[" #BUF "+8:" #BUF "+11], v[" #PR ":" #PR "+3], " O "\n" R3, X, 8, PX, 4) \
    AG("v_mfma_f32_32x32x16_bf16 " O ", v[" #BUF "+12:" #BUF "+15], v[" #PR "+4:" #PR "+7], " O "\n" R4, X, 12, PX, 6)
#define ARK1(NB, sl, tl) "ds_read_b128 v[" #NB ":" #NB "+3], %[k1a] offset:" #sl "*12288+" #tl "*2624\n"
#define ARK2(NB, sl, tl) "ds_read_b64 v[" #NB "+4:" #NB "+5], %[k2a] offset:" #sl "*12288+" #tl "*2624\n"
#define ARV0(NB, sl, tl) "ds_read_b128 v[" #NB "+8:" #NB "+11], %[va] offset:" #sl "*12288+" #tl "*2624\n"
#define ARV1(NB, sl, tl) "ds_read_b128 v[" #NB "+12:" #NB "+15], %[va] offset:" #sl "*12288+" #tl "*2624+672\n"
#define AH1(PREV, R1, R2, R3, R4) AHALF(128, 160, 144, "%[qb1]", "%[qb2]", "%[ob]", 168, PREV, R1, R2, R3, R4)
#define AH2(CUR, PREV, sl, tl) AHALF(144, 168, 128, "%[qa1]", "%[qa2]", "%[oa]", 160, CUR, ARK1(PREV, sl, tl), ARK2(PREV, sl, tl), ARV0(PREV, sl, tl), ARV1(PREV, sl, tl))
#define AWL "s_waitcnt lgkmcnt(0)\n"
#define ASYNC "s_waitcnt vmcnt(6) lgkmcnt(0)\ns_barrier\n"
#define ADMA0(fs) "s_mul_i32 %[of], %[ck], 10496\n s_add_u32 %[ck], %[ck], 1\n s_and_b32 %[ck], %[ck], 15\n s_add_u32 m0, %[ldst], " #fs "*12288\n" \
                  "v_add_u32 v208, %[of], %[kvo]\n"
#define ADMA1 "s_setprio 3\n global_load_lds_dwordx4 v208, %[img] sc1\n s_setprio 0\n"
#define ADMA2 "s_setprio 3\n global_load_lds_dwordx4 v208, %[img] offset:1024 sc1\n s_setprio 0\n"
#define ADMA3 "s_setprio 3\n global_load_lds_dwordx4 v208, %[img] offset:2048 sc1\n s_setprio 0\n"
#define ACHUNK(sl, nsl) \
    AH1(192, "", "", "", "") AWL AH2(176, 192, sl, 2) \
    AH1(176, "", "", "", "") AWL AH2(192, 176, sl, 3) \
    AH1(192, "", "", "", "") ASYNC AH2(176, 192, nsl, 0) \
    AH1(176, ADMA0(sl), ADMA1, ADMA2, ADMA3) AWL AH2(192, 176, nsl, 1)
#define AMOV(d, s) "v_mov_b32 v" #d ", " s "\n"
#define ATTN_LOOP_ASM \
    "s_setprio 0\n s_mov_b32 %[m0s], m0\n s_mov_b32 %[it], 4\n s_mov_b32 %[ck], 4\n" \
    ARK1(176, 0, 1) ARK2(176, 0, 1) ARV0(176, 0, 1) ARV1(176, 0, 1) \
    "v_mov_b64 v[192:193], %[k0a]\n v_mov_b64 v[194:195], %[k0b]\n" \
    "v_mov_b64 v[196:197], %[k02]\n" AMOV(198, "%[kone]") AMOV(199, "0") AMOV(182, "%[kone]") AMOV(183, "0") \
    "v_mfma_f32_32x32x16_bf16 v[128:143], v[192:195], %[qa1], 0\n" \
    AMOV(200, "0") AMOV(201, "0") AMOV(202, "0") AMOV(203, "0") AMOV(204, "0") AMOV(205, "0") AMOV(206, "0") AMOV(207, "0") \
    AMOV(168, "0") AMOV(169, "0") AMOV(170, "0") AMOV(171, "0") AMOV(172, "0") AMOV(173, "0") AMOV(174, "0") AMOV(175, "0") \
    "v_mfma_f32_32x32x16_bf16 v[128:143], v[196:199], %[qa2], v[128:143]\n" \
    "s_nop 7\n s_nop 7\n" \
    "1:\n" \
    ACHUNK(0, 1) ACHUNK(1, 2) ACHUNK(2, 3) ACHUNK(3, 0) \
    "s_sub_u32 %[it], %[it], 1\n s_cmp_lg_u32 %[it], 0\n s_cbranch_scc1 1b\n" \
    "v_mfma_f32_32x32x16_bf16 %[ob], v[200:203], v[168:171], %[ob]\n" \
    "v_mfma_f32_32x32x16_bf16 %[ob], v[204:207], v[172:175], %[ob]\n" \
    "s_setprio 3\n s_waitcnt vmcnt(0) lgkmcnt(0)\n s_mov_b32 m0, %[m0s]\n s_nop 7\n s_nop 7\n"
#define ATTN_CLOBBERS "v128","v129","v130","v131","v132","v133","v134","v135","v136","v137","v138","v139","v140","v141","v142","v143", \
    "v144","v145","v146","v147","v148","v149","v150","v151","v152","v153","v154","v155","v156","v157","v158","v159", \
    "v160","v161","v162","v163","v164","v165","v166","v167","v168","v169","v170","v171","v172","v173","v174","v175", \
    "v176","v177","v178","v179","v180","v181","v182","v183","v184","v185","v186","v187","v188","v189","v190","v191", \
    "v192","v193","v194","v195","v196","v197","v198","v199","v200","v201","v202","v203","v204","v205","v206","v207","v208"

DI void attn_wg(const Params& p, int rb, int c, u64 ep, char* lds) {
    const int tid = threadIdx.x, lane = tid & 63, w = __builtin_amdgcn_readfirstlane(tid >> 6);
    const int r32 = lane & 31, h = lane >> 5;
    const unsigned lds0 = (unsigned)(uintptr_t)lds;
    const char* kvimg = p.ws + WS_KV;
    STAMP(1, 0);
    constexpr int QS_B = 18432;
    unsigned qoff[5]; unsigned qdst[5];
#pragma unroll
    for (int j = 0; j < 5; ++j) {
        int i = w + 4 * j; if (i >= 18) i -= 2;
        if (j < 2) { const int row = 8 * i + (lane >> 3), pos = lane & 7, ch = pos ^ ((row >> 1) & 7); qoff[j] = row * (D_ * 2) + ch * 16; }
        else { const int idx = (i - 8) * 64 + lane, kr = idx / 10, ch = idx % 10; qoff[j] = kr * (D_ * 2) + ch * 16; }
        qdst[j] = lds0 + i * 1024;
    }
    const u16* xsrc = p.x + (size_t)rb * 64 * D_;
    const u16* wsrc = p.wq + c * 80;
#define Q_DMA(kt) do { const unsigned d_ = ((kt) & 3) * QS_B; const u16* xs_ = xsrc + (kt) * 64; const u16* ws_ = wsrc + (size_t)(kt) * 64 * D_; \
        glds16(xs_, qoff[0], d_ + qdst[0]); glds16(xs_, qoff[1], d_ + qdst[1]); glds16(ws_, qoff[2], d_ + qdst[2]); glds16(ws_, qoff[3], d_ + qdst[3]); glds16(ws_, qoff[4], d_ + qdst[4]); } while (0)
    Q_DMA(0); Q_DMA(1); Q_DMA(2); Q_DMA(3);
    const u64 tag = epoch_tag(ep);
    f32x16 qa = zero16(), qb = zero16();
    {
        unsigned xa[4];
#pragma unroll
        for (int ks = 0; ks < 4; ++ks) xa[ks] = r32 * 128 + (((2 * ks + h) ^ ((r32 >> 1) & 7)) << 4);
        const unsigned wa = 8192 + (8 * h + ((lane & 15) >> 2)) * 160 + w * 40 + ((lane >> 4) & 1) * 32 + (lane & 3) * 8;
        bf16x8 wfA[4], x0A[4], x1A[4], wfB[4], x0B[4], x1B[4];
#define Q_RD(S, kt, ks) do { const lds_cptr sb_ = (lds_cptr)lds + ((kt) & 3) * QS_B; wf##S[ks] = cat8(trread(sb_ + wa + (ks) * 2560), trread(sb_ + wa + (ks) * 2560 + 640)); \
            x0##S[ks] = ldsread16(sb_ + xa[ks]); x1##S[ks] = ldsread16(sb_ + xa[ks] + 4096); } while (0)
#define Q_STEP(C, N, kt) do { \
            if ((kt) < 7) { WAIT_BAR(10); } else if ((kt) == 7) { WAIT_BAR(5); } else if ((kt) == 8) { WAIT_BAR(0); } \
            if ((kt) + 4 < 10) Q_DMA((kt) + 4); \
            _Pragma("unroll") for (int ks = 0; ks < 4; ++ks) { qa = MFMA(wf##C[ks], x0##C[ks], qa); qb = MFMA(wf##C[ks], x1##C[ks], qb); if ((kt) + 1 < 10) Q_RD(N, (kt) + 1, ks); SBAR(); } } while (0)
        WAIT_BAR(15);
        if (tid == 0) flag_set(p.ws, WS_SEEN + blockIdx.x * 8, tag);
#pragma unroll
        for (int ks = 0; ks < 4; ++ks) Q_RD(A, 0, ks);
#pragma unroll
        for (int kt = 0; kt < 10; kt += 2) { Q_STEP(A, B, kt); Q_STEP(B, A, kt + 1); }
    }
    STAMP(1, 1);
    constexpr float C2 = 0.22360679774997896f * 1.4426950408889634f;
    u32x4 qa1, qa2, qb1, qb2;
    qa1 = (u32x4){cvtpk(qa[0] * C2, qa[1] * C2), cvtpk(qa[2] * C2, qa[3] * C2), cvtpk(qa[4] * C2, qa[5] * C2), cvtpk(qa[6] * C2, qa[7] * C2)};
    qb1 = (u32x4){cvtpk(qb[0] * C2, qb[1] * C2), cvtpk(qb[2] * C2, qb[3] * C2), cvtpk(qb[4] * C2, qb[5] * C2), cvtpk(qb[6] * C2, qb[7] * C2)};
    qa2 = (u32x4){h ? 0u : cvtpk(qa[8] * C2, qa[9] * C2), h ? 0u : cvtpk(qa[10] * C2, qa[11] * C2), 0u, 0u};
    qb2 = (u32x4){h ? 0u : cvtpk(qb[8] * C2, qb[9] * C2), h ? 0u : cvtpk(qb[10] * C2, qb[11] * C2), 0u, 0u};
    if (w == 0) { flags_wait<2, 4>(p.ws + WS_KVF + (blockIdx.x & 15) * 1024, 128, tag, lane); STAMP(1, 5); }
    WAIT_BAR(0);
    const unsigned kvoff = (unsigned)(w * 3072 + lane * 16);
#define KV_DMA(chunk) do { const char* s_ = kvimg + (size_t)(chunk) * KV_CHUNK_B; const unsigned d_ = lds0 + (chunk) * KV_SLOT_B + w * 3072; \
        glds16_dev(s_, kvoff, d_); glds16_dev(s_, kvoff + 1024, d_ + 1024); glds16_dev(s_, kvoff + 2048, d_ + 2048); } while (0)
    KV_DMA(0); KV_DMA(1); KV_DMA(2); KV_DMA(3);
    const unsigned k1a = lane * 16, k2a = 1024 + r32 * 8;
    WAIT_BAR(9);
    STAMP(1, 6);
    const bf16x8 k01 = ldsread16((lds_cptr)lds + k1a);
    const u32x2 k02 = *(const LAS u32x2*)((lds_cptr)lds + k2a);
    const unsigned va = 1280 + h * 336 + (r32 <= 20 ? r32 : (r32 < 28 ? 20 : 16)) * 16;
    const unsigned kone = h ? 0u : 0x3f80u;
    auto kfrag2 = [](u32x2 v, unsigned one) { return __builtin_bit_cast(bf16x8, (u32x4){v[0], v[1], one, 0u}); };
    {
        f32x16 sa = MFMA(k01, BC(qa1), zero16()); sa = MFMA(kfrag2(k02, kone), BC(qa2), sa);
        f32x16 sb = MFMA(k01, BC(qb1), zero16()); sb = MFMA(kfrag2(k02, kone), BC(qb2), sb);
        float ma = sa[0], mb = sb[0];
#pragma unroll
        for (int r = 1; r < 16; ++r) { ma = __builtin_fmaxf(ma, sa[r]); mb = __builtin_fmaxf(mb, sb[r]); }
        ma = xhalf_max(ma); mb = xhalf_max(mb);
        qa2[2] = h ? 0u : (cvtpk(-ma, 0.f) & 0xffffu);
        qb2[2] = h ? 0u : (cvtpk(-mb, 0.f) & 0xffffu);
    }
    f32x16 oa = zero16(), ob = zero16();
    float ga, gb;
    asm volatile("global_load_dword %0, %2, %3 sc1\n\tglobal_load_dword %1, %2, %3 offset:128 sc1" : "=&v"(ga), "=&v"(gb) : "v"(r32 * 4), "s"(p.ws + WS_G + rb * 256) : "memory");
    STAMP(1, 2);
    {
        unsigned t_m0, t_it, t_ck, t_of;
        const u32x4 k0u = __builtin_bit_cast(u32x4, k01); const u32x2 k0a = {k0u[0], k0u[1]}, k0b = {k0u[2], k0u[3]};
        asm volatile(ATTN_LOOP_ASM
            : [oa] "+v"(oa), [ob] "+v"(ob), "+v"(ga), "+v"(gb), [m0s] "=&s"(t_m0), [it] "=&s"(t_it), [ck] "=&s"(t_ck), [of] "=&s"(t_of)
            : [qa1] "v"(qa1), [qa2] "v"(qa2), [qb1] "v"(qb1), [qb2] "v"(qb2), [k1a] "v"(lds0 + k1a), [k2a] "v"(lds0 + k2a), [va] "v"(lds0 + va),
              [kvo] "v"(kvoff), [img] "s"(kvimg), [ldst] "s"(lds0 + w * 3072), [k0a] "v"(k0a), [k0b] "v"(k0b), [k02] "v"(k02), [kone] "v"(kone)
            : ATTN_CLOBBERS, "scc", "memory");
    }
    STAMP(1, 3);
    {
        float chk = 0.f;
#pragma unroll
        for (int r = 0; r < 8; ++r) chk += oa[r] * 0.f + ob[r] * 0.f;
        if (h == 0) { chk += (oa[8] + oa[9] + oa[10] + oa[11] + ob[8] + ob[9] + ob[10] + ob[11]) * 0.f; } else { chk += (oa[8] + ob[8]) * 0.f; }
        if (h == 1 && !(oa[8] > 1e-30f && ob[8] > 1e-30f)) chk = 1.f;
        if (__builtin_expect(__any(!(chk == 0.f)), 0)) {
            __builtin_amdgcn_fence(__ATOMIC_ACQUIRE, "agent");
            qa2[2] = 0u; qb2[2] = 0u;
            oa = zero16(); ob = zero16();
            float ma = -INFINITY, mb = -INFINITY;
            for (int t = 0; t < 64; ++t) {
                const char* tb = kvimg + (size_t)t * KV_TILE_B;
                const char* kb = kvimg + (size_t)((t + 63) & 63) * KV_TILE_B;
                const bf16x8 k1 = *(const bf16x8*)(kb + k1a), k2 = kfrag2(*(const u32x2*)(kb + k2a), kone);
                const bf16x8 v0 = *(const bf16x8*)(tb + va), v1 = *(const bf16x8*)(tb + va + 672);
                f32x16 sa = MFMA(k1, BC(qa1), zero16()); sa = MFMA(k2, BC(qa2), sa);
                f32x16 sb = MFMA(k1, BC(qb1), zero16()); sb = MFMA(k2, BC(qb2), sb);
                float ta = ma, tb_ = mb;
#pragma unroll
                for (int r = 0; r < 16; ++r) { ta = __builtin_fmaxf(ta, sa[r]); tb_ = __builtin_fmaxf(tb_, sb[r]); }
                ta = xhalf_max(ta); tb_ = xhalf_max(tb_);
                const float fa = __builtin_amdgcn_exp2f(ma - ta), fb = __builtin_amdgcn_exp2f(mb - tb_);
                ma = ta; mb = tb_;
#pragma unroll
                for (int r = 0; r < 16; ++r) { oa[r] *= fa; ob[r] *= fb; sa[r] = __builtin_amdgcn_exp2f(sa[r] - ma); sb[r] = __builtin_amdgcn_exp2f(sb[r] - mb); }
                u32x4 pa0, pa1, pb0, pb1;
#pragma unroll
                for (int j = 0; j < 4; ++j) { pa0[j] = cvtpk(sa[2 * j], sa[2 * j + 1]); pa1[j] = cvtpk(sa[8 + 2 * j], sa[9 + 2 * j]); pb0[j] = cvtpk(sb[2 * j], sb[2 * j + 1]); pb1[j] = cvtpk(sb[8 + 2 * j], sb[9 + 2 * j]); }
                oa = MFMA(v0, BC(pa0), oa); oa = MFMA(v1, BC(pa1), oa);
                ob = MFMA(v0, BC(pb0), ob); ob = MFMA(v1, BC(pb1), ob);
            }
        }
    }
    const float la = __shfl(oa[8], r32 + 32), lb = __shfl(ob[8], r32 + 32);
    const float fa = ga / la, fb = gb / lb;
    const int head = 4 * c + w;
    char* oimg = p.ws + WS_O + (size_t)(rb * 2) * 40 * 1024;
#pragma unroll
    for (int qd = 0; qd < 3; ++qd) {
        const int Q = head * 5 + 2 * qd + h;
        const unsigned off = (Q >> 2) * 1024 + ((Q & 1) * 32 + r32) * 16 + ((Q >> 1) & 1) * 8;
        const u32x2 ua = {cvtpk(oa[4 * qd] * fa, oa[4 * qd + 1] * fa), cvtpk(oa[4 * qd + 2] * fa, oa[4 * qd + 3] * fa)};
        const u32x2 ub = {cvtpk(ob[4 * qd] * fb, ob[4 * qd + 1] * fb), cvtpk(ob[4 * qd + 2] * fb, ob[4 * qd + 3] * fb)};
        if (qd < 2 || h == 0) { st8_dev(oimg + off, ua); st8_dev(oimg + 40 * 1024 + off, ub); }
    }
    WAIT_BAR(0);
    if (tid == 0) flag_set(p.ws, WS_OF + (rb * 16 + c) * 8, tag);
    STAMP(1, 4);
    if (blockIdx.x == 0 && w == 0) {
        flags_wait<8, 32>(p.ws + WS_SEEN, 512, tag, lane);
        if (lane == 0) flag_set(p.ws, WS_EPOCH, tag);
    }
#undef KV_DMA
#undef Q_DMA
}

DI u64 ffn_up_wg(const Params& p, int rb2, int fs, u64 ep, char* lds) {
    const int tid = threadIdx.x, lane = tid & 63, w = __builtin_amdgcn_readfirstlane(tid >> 6);
    const int r32 = lane & 31, h = lane >> 5;
    const unsigned lds0 = (unsigned)(uintptr_t)lds;
    constexpr int XS_B = 16384, WS_B = 12288, WS_0 = 2 * XS_B;
    const int xrow = 8 * w + (lane >> 3);
    const unsigned xo = xrow * (D_ * 2) + (((lane & 7) ^ ((xrow >> 1) & 7)) << 4);
    unsigned wo_[3];
#pragma unroll
    for (int j = 0; j < 3; ++j) { const int r = ((w + 4 * j) * 64 + lane) % 384; wo_[j] = (r / 12) * (F_ * 2) + (r % 12) * 16; }
    const u16* xsrc = p.x + (size_t)rb2 * 128 * D_;
    const u16* gsrc = p.wg + fs * 96;
    const u16* usrc = p.wu + fs * 96;
    const u16* w1src = (w < 2) ? gsrc : usrc;
    const unsigned dw = lds0 + w * 1024;
#define UP_X(kb, m) glds16(xsrc + (kb) * 64 + (m) * 32 * D_, xo, dw + ((kb) & 1) * XS_B + (m) * 4096)
#define UP_W0(s) glds16(gsrc + (size_t)(s) * 32 * F_, wo_[0], dw + WS_0 + ((s) & 3) * WS_B)
#define UP_W1(s) glds16(w1src + (size_t)(s) * 32 * F_, wo_[1], dw + WS_0 + ((s) & 3) * WS_B + 4096)
#define UP_W2(s) glds16(usrc + (size_t)(s) * 32 * F_, wo_[2], dw + WS_0 + ((s) & 3) * WS_B + 8192)
#define UP_XA(kb) do { UP_X(kb, 0); UP_X(kb, 1); UP_X(kb, 2); UP_X(kb, 3); } while (0)
#define UP_WA(s) do { UP_W0(s); UP_W1(s); UP_W2(s); } while (0)
    STAMP(2, 1);
    UP_XA(0); UP_WA(0); UP_WA(1); UP_XA(1); UP_WA(2); UP_WA(3);
    const u64 tag = epoch_tag(ep);
    f32x16 acc[6];
#pragma unroll
    for (int i = 0; i < 6; ++i) acc[i] = zero16();
    const int row = 32 * w + r32;
    lds_cptr xa[4];
#pragma unroll
    for (int kk = 0; kk < 4; ++kk) xa[kk] = (lds_cptr)lds + row * 128 + (((2 * kk + h) ^ ((row >> 1) & 7)) << 4);
    const lds_cptr wa = (lds_cptr)lds + WS_0 + (8 * h + ((lane & 15) >> 2)) * 192 + ((lane >> 4) & 1) * 32 + (lane & 3) * 8;
    bf16x8 xf0, xf1, wf0[6], wf1[6];
#define UP_RX(q) ldsread16(xa[(q) & 3] + (((q) >> 2) & 1) * XS_B)
#define UP_RW(q, nt) cat8(trread(wa + (((q) >> 1) & 3) * WS_B + ((nt) / 3) * 6144 + ((q) & 1) * 3072 + ((nt) % 3) * 64), \
                          trread(wa + (((q) >> 1) & 3) * WS_B + ((nt) / 3) * 6144 + ((q) & 1) * 3072 + ((nt) % 3) * 64 + 768))
#define UP_STEP(C, N, q, D0, D1, D2, D3, D4, D5) do { xf##N = UP_RX(q); \
        acc[0] = MFMA(wf##C[0], xf##C, acc[0]); wf##N[0] = UP_RW(q, 0); D0; SBAR(); PACE(); \
        acc[1] = MFMA(wf##C[1], xf##C, acc[1]); wf##N[1] = UP_RW(q, 1); D1; SBAR(); PACE(); \
        acc[2] = MFMA(wf##C[2], xf##C, acc[2]); wf##N[2] = UP_RW(q, 2); D2; SBAR(); PACE(); \
        acc[3] = MFMA(wf##C[3], xf##C, acc[3]); wf##N[3] = UP_RW(q, 3); D3; SBAR(); PACE(); \
        acc[4] = MFMA(wf##C[4], xf##C, acc[4]); wf##N[4] = UP_RW(q, 4); D4; SBAR(); PACE(); \
        acc[5] = MFMA(wf##C[5], xf##C, acc[5]); wf##N[5] = UP_RW(q, 5); D5; SBAR(); PACE(); } while (0)
#define NOP_ do {} while (0)
    WAIT_BAR(13);
    if (tid == 0) flag_set(p.ws, WS_SEEN + blockIdx.x * 8, tag);
    xf0 = UP_RX(0);
#pragma unroll
    for (int nt = 0; nt < 6; ++nt) wf0[nt] = UP_RW(0, nt);
    for (int it = 0; it < 4; ++it) {
        const int s0 = 4 * it, kb = 2 * it;
        UP_STEP(0, 1, 1, NOP_, NOP_, NOP_, NOP_, NOP_, NOP_);
        WAIT_BAR(10);
        UP_STEP(1, 0, 2, UP_W0(s0 + 4), UP_W1(s0 + 4), UP_W2(s0 + 4), NOP_, NOP_, NOP_);
        UP_STEP(0, 1, 3, NOP_, NOP_, NOP_, NOP_, NOP_, NOP_);
        WAIT_BAR(6);
        UP_STEP(1, 0, 4, UP_X(kb + 2, 0), UP_X(kb + 2, 1), UP_X(kb + 2, 2), UP_X(kb + 2, 3), UP_W0(s0 + 5), UP_W1(s0 + 5); UP_W2(s0 + 5));
        UP_STEP(0, 1, 5, NOP_, NOP_, NOP_, NOP_, NOP_, NOP_);
        WAIT_BAR(10);
        UP_STEP(1, 0, 6, UP_W0(s0 + 6), UP_W1(s0 + 6), UP_W2(s0 + 6), NOP_, NOP_, NOP_);
        UP_STEP(0, 1, 7, NOP_, NOP_, NOP_, NOP_, NOP_, NOP_);
        WAIT_BAR(6);
        UP_STEP(1, 0, 0, UP_X(kb + 3, 0), UP_X(kb + 3, 1), UP_X(kb + 3, 2), UP_X(kb + 3, 3), UP_W0(s0 + 7), UP_W1(s0 + 7); UP_W2(s0 + 7));
    }
    UP_STEP(0, 1, 1, NOP_, NOP_, NOP_, NOP_, NOP_, NOP_);
    WAIT_BAR(0);
    UP_STEP(1, 0, 2, NOP_, NOP_, NOP_, NOP_, NOP_, NOP_);
    UP_STEP(0, 1, 3, NOP_, NOP_, NOP_, NOP_, NOP_, NOP_);
    UP_STEP(1, 0, 4, NOP_, NOP_, NOP_, NOP_, NOP_, NOP_);
    UP_STEP(0, 1, 5, NOP_, NOP_, NOP_, NOP_, NOP_, NOP_);
    UP_STEP(1, 0, 6, NOP_, NOP_, NOP_, NOP_, NOP_, NOP_);
    UP_STEP(0, 1, 7, NOP_, NOP_, NOP_, NOP_, NOP_, NOP_);
#pragma unroll
    for (int nt = 0; nt < 6; ++nt) acc[nt] = MFMA(wf1[nt], xf1, acc[nt]);
    STAMP(2, 2);
    const __amdgpu_buffer_rsrc_t rs = __builtin_amdgcn_make_buffer_rsrc(p.ws, 0, (int)WS_END, 0x00020000);
    const unsigned ho = (unsigned)WS_H + (unsigned)((rb2 * 4 + w) * 48 + fs * 3) * 2048 + lane * 16;
#pragma unroll
    for (int j = 0; j < 3; ++j) {
        float hv[16];
#pragma unroll
        for (int r = 0; r < 16; ++r) hv[r] = __builtin_fmaxf(acc[j][r], 0.f) * acc[3 + j][r];
        st16_dev(rs, ho + j * 2048, (u32x4){cvtpk(hv[0], hv[1]), cvtpk(hv[2], hv[3]), cvtpk(hv[4], hv[5]), cvtpk(hv[6], hv[7])});
        st16_dev(rs, ho + j * 2048 + 1024, (u32x4){cvtpk(hv[8], hv[9]), cvtpk(hv[10], hv[11]), cvtpk(hv[12], hv[13]), cvtpk(hv[14], hv[15])});
    }
    WAIT_BAR(0);
    if (tid == 0) flag_set(p.ws, WS_HF + (rb2 * 16 + fs) * 8, tag);
    STAMP(2, 3);
    return tag;
}

DI void ld16_dev(bf16x8& d0, bf16x8& d1, const char* sbase, unsigned voff) {
    asm volatile("global_load_dwordx4 %0, %2, %3 sc1\n\tglobal_load_dwordx4 %1, %2, %3 offset:1024 sc1" : "=&v"(d0), "=&v"(d1) : "v"(voff), "s"(sbase) : "memory");
}
DI void out_wg(const Params& p, int rb, int cn, u64 tag, char* lds) {
    const int tid = threadIdx.x, lane = tid & 63, w = __builtin_amdgcn_readfirstlane(tid >> 6);
    const int r32 = lane & 31, h = lane >> 5;
    const unsigned lds0 = (unsigned)(uintptr_t)lds + w * 20480;
    unsigned wo_[5];
#pragma unroll
    for (int j = 0; j < 5; ++j) { const int idx = j * 64 + lane; wo_[j] = (idx / 160) * 20480 + (idx % 160) * 16; }
    const char* dsrc = p.ws + WS_WT + (size_t)w * 24 * 20480 + cn * 2560;
    const char* osrc = p.ws + WS_WT + (size_t)(96 + w * 10) * 20480 + cn * 2560;
    const char* hsrc = p.ws + WS_H + ((size_t)(rb * 2) * 48 + w * 12) * 2048;
    const char* asrc = p.ws + WS_O + ((size_t)(rb * 2) * 40 + w * 10) * 1024;
    const unsigned lo16 = lane * 16;
#define W_DMA(u) do { const char* s_ = (u) < 12 ? dsrc + (size_t)(u) * 40960 : osrc + (size_t)((u) - 12) * 40960; const unsigned d_ = lds0 + ((u) & 3) * 5120; \
        glds16_dev(s_, wo_[0], d_); glds16_dev(s_, wo_[1], d_ + 1024); glds16_dev(s_, wo_[2], d_ + 2048); glds16_dev(s_, wo_[3], d_ + 3072); glds16_dev(s_, wo_[4], d_ + 4096); } while (0)
    if (w == 0) {
        flags_wait<3, 32>(p.ws + WS_WF + (blockIdx.x & 7) * 2048, 136, tag, lane);
    }
    WAIT_BAR(0);
    W_DMA(0); W_DMA(1); W_DMA(2); W_DMA(3);
    f32x16 acc[2][3];
#pragma unroll
    for (int i = 0; i < 2; ++i)
#pragma unroll
        for (int j = 0; j < 3; ++j) acc[i][j] = zero16();
    const lds_cptr wbase = (lds_cptr)lds + w * 20480 + (r32 >> 4) * 512 + h * 256 + (r32 & 15) * 16;
    const lds_cptr wbase2 = (lds_cptr)lds + w * 20480 + 2048 + h * 256 + (r32 & 15) * 16;
    bf16x8 wf[2][6];
    bf16x8 af[5][4];
#define W_RD(u, i) wf[(u) & 1][i] = ((i) % 3 == 2) ? ldsread16(wbase2 + ((u) & 3) * 5120 + ((i) / 3) * 2560) : ldsread16(wbase + ((u) & 3) * 5120 + ((i) / 3) * 2560 + ((i) % 3) * 1024)
#define H_LD(u) do { ld16_dev(af[(u) & 3][0], af[(u) & 3][1], hsrc + (u) * 2048, lo16); ld16_dev(af[(u) & 3][2], af[(u) & 3][3], hsrc + 48 * 2048 + (u) * 2048, lo16); } while (0)
#define UNIT(u, A) do { _Pragma("unroll") for (int i = 0; i < 6; ++i) { \
        acc[0][i % 3] = MFMA(wf[(u) & 1][i], A[i / 3], acc[0][i % 3]); if ((u) < 12) PACE(); acc[1][i % 3] = MFMA(wf[(u) & 1][i], A[2 + i / 3], acc[1][i % 3]); \
        if ((u) + 1 < 17) W_RD((u) + 1, i); SBAR(); if ((u) < 12) PACE(); } } while (0)
    if (w == 0) {
        flags_wait<1, 32>(p.ws + WS_HF + (rb >> 1) * 128, 16, tag, lane);
        flags_wait<1, 32>(p.ws + WS_KVF + (blockIdx.x & 15) * 1024 + (rb * 4) * 8, 4, tag, lane);
    }
    WAIT_BAR(0);
    STAMP(2, 4);
    const float g0 = 1.f - ld4_dev(p.ws + WS_G + (rb * 64 + r32) * 4), g1 = 1.f - ld4_dev(p.ws + WS_G + (rb * 64 + 32 + r32) * 4);
    asm volatile("s_waitcnt vmcnt(0)" :: "v"(g0), "v"(g1) : "memory");
    H_LD(0); H_LD(1); H_LD(2);
#pragma unroll
    for (int i = 0; i < 6; ++i) W_RD(0, i);
#pragma unroll
    for (int u = 0; u < 12; ++u) {
        if (u == 0) asm volatile("s_waitcnt vmcnt(8) lgkmcnt(0)" : "+v"(af[0][0]), "+v"(af[0][1]), "+v"(af[0][2]), "+v"(af[0][3]) :: "memory");
        else if (u == 1) asm volatile("s_waitcnt vmcnt(13) lgkmcnt(0)" : "+v"(af[1][0]), "+v"(af[1][1]), "+v"(af[1][2]), "+v"(af[1][3]) :: "memory");
        else if (u <= 9) asm volatile("s_waitcnt vmcnt(18) lgkmcnt(0)" : "+v"(af[u & 3][0]), "+v"(af[u & 3][1]), "+v"(af[u & 3][2]), "+v"(af[u & 3][3]) :: "memory");
        else if (u == 10) asm volatile("s_waitcnt vmcnt(14) lgkmcnt(0)" : "+v"(af[u & 3][0]), "+v"(af[u & 3][1]), "+v"(af[u & 3][2]), "+v"(af[u & 3][3]) :: "memory");
        else asm volatile("s_waitcnt vmcnt(10) lgkmcnt(0)" : "+v"(af[u & 3][0]), "+v"(af[u & 3][1]), "+v"(af[u & 3][2]), "+v"(af[u & 3][3]) :: "memory");
        W_DMA(u + 4);
        if (u + 3 < 12) H_LD(u + 3);
        SBAR();
        UNIT(u, af[u & 3]);
    }
    asm volatile("s_waitcnt lgkmcnt(0)" ::: "memory");
    W_DMA(16);
#pragma unroll
    for (int nt = 0; nt < 3; ++nt)
#pragma unroll
        for (int r = 0; r < 16; ++r) { acc[0][nt][r] *= g0; acc[1][nt][r] *= g1; }
    STAMP(2, 5);
    if (w == 0) flags_wait<1, 8>(p.ws + WS_OF + rb * 128, 8, tag, lane);
    WAIT_BAR(0);
    STAMP(2, 6);
#pragma unroll
    for (int v = 0; v < 5; ++v) { ld16_dev(af[v][0], af[v][1], asrc + v * 2048, lo16); ld16_dev(af[v][2], af[v][3], asrc + 40 * 1024 + v * 2048, lo16); }
    asm volatile("s_waitcnt vmcnt(16)" : "+v"(af[0][0]), "+v"(af[0][1]), "+v"(af[0][2]), "+v"(af[0][3]) :: "memory"); SBAR(); UNIT(12, af[0]);
    asm volatile("s_waitcnt vmcnt(12)" : "+v"(af[1][0]), "+v"(af[1][1]), "+v"(af[1][2]), "+v"(af[1][3]) :: "memory"); SBAR(); UNIT(13, af[1]);
    asm volatile("s_waitcnt vmcnt(8)" : "+v"(af[2][0]), "+v"(af[2][1]), "+v"(af[2][2]), "+v"(af[2][3]) :: "memory"); SBAR(); UNIT(14, af[2]);
    asm volatile("s_waitcnt vmcnt(4)" : "+v"(af[3][0]), "+v"(af[3][1]), "+v"(af[3][2]), "+v"(af[3][3]) :: "memory"); SBAR(); UNIT(15, af[3]);
    asm volatile("s_waitcnt vmcnt(0)" : "+v"(af[4][0]), "+v"(af[4][1]), "+v"(af[4][2]), "+v"(af[4][3]) :: "memory"); SBAR(); UNIT(16, af[4]);
    STAMP(2, 7);
    WAIT_BAR(0);
    LAS f32x4* slab = (LAS f32x4*)lds;
#define QUAD(q) (f32x4){acc[(q) / 10][((q) % 10) / 4][4 * (((q) % 10) % 4)], acc[(q) / 10][((q) % 10) / 4][4 * (((q) % 10) % 4) + 1], \
                        acc[(q) / 10][((q) % 10) / 4][4 * (((q) % 10) % 4) + 2], acc[(q) / 10][((q) % 10) / 4][4 * (((q) % 10) % 4) + 3]}
#pragma unroll
    for (int q = 0; q < 20; ++q) slab[(((q / 5) * 4 + w) * 5 + q % 5) * 64 + lane] = QUAD(q);
    WAIT_BAR(0);
#pragma unroll
    for (int i = 0; i < 5; ++i) {
        f32x4 s = slab[((w * 4 + 0) * 5 + i) * 64 + lane];
#pragma unroll
        for (int ww = 1; ww < 4; ++ww) s += slab[((w * 4 + ww) * 5 + i) * 64 + lane];
        const int q = w * 5 + i, rt = q / 10, n = 8 * (q % 10) + 4 * h;
        *(u32x2*)(p.out + (size_t)(rb * 64 + rt * 32 + r32) * D_ + cn * 80 + n) = (u32x2){cvtpk(s[0], s[1]), cvtpk(s[2], s[3])};
    }
    STAMP(2, 8);
}

__global__ __launch_bounds__(256, 2) void k_fused(Params p) {
    extern __shared__ __attribute__((aligned(16))) char lds[];
    const int b = blockIdx.x, j = b & 7, i = (b >> 3) & 31;
    const u64 ep = epoch_load(p.ws + WS_EPOCH);
    if (b < 256) {
        __builtin_amdgcn_s_setprio(2);
        attn_wg(p, 4 * j + (i >> 3), i & 7, ep, lds);
    } else {
        STAMP(2, 0);
        if (i < 16) { __builtin_amdgcn_s_setprio(3); kv_job(p, 8 * j + (i >> 1), i & 1, ep, lds); __builtin_amdgcn_s_setprio(1); }
        else { __builtin_amdgcn_s_setprio(1); wt_job(p, (i - 16) * 8 + j, ep, lds); if (i == 16) wt_job(p, 128 + j, ep, lds); }
        const u64 tag = ffn_up_wg(p, 8 * (j >> 2) + (i >> 2), 4 * (j & 3) + (i & 3), ep, lds);
        out_wg(p, 4 * j + (i >> 3), i & 7, tag, lds);
    }
}

extern "C" void kernel_launch(void* const* d_in, const int* in_sizes, int n_in, void* d_out, int out_size, void* d_ws, size_t ws_size,
                              hipStream_t stream) {
    Params p;
    p.x = (const u16*)d_in[0]; p.wq = (const u16*)d_in[1]; p.wk = (const u16*)d_in[2]; p.wv = (const u16*)d_in[3]; p.wo = (const u16*)d_in[4];
    p.wg = (const u16*)d_in[6]; p.wu = (const u16*)d_in[7]; p.wd = (const u16*)d_in[8]; p.gate_w = (const u16*)d_in[9];
    p.out = (u16*)d_out; p.ws = (char*)d_ws;
    (void)hipFuncSetAttribute((const void*)k_fused, hipFuncAttributeMaxDynamicSharedMemorySize, LDS_BYTES);
    k_fused<<<512, 256, LDS_BYTES, stream>>>(p);
}
```

```cpp
#include <hip/hip_runtime.h>
#include <hip/hip_bf16.h>
#include <stdint.h>

typedef __attribute__((ext_vector_type(8))) short bf16x8;
typedef __attribute__((ext_vector_type(4))) short s16x4;
typedef __attribute__((ext_vector_type(16))) float f32x16;
typedef __attribute__((ext_vector_type(4))) float f32x4;
typedef __attribute__((ext_vector_type(2))) float f32x2;
typedef __attribute__((ext_vector_type(4))) unsigned u32x4;
typedef __attribute__((ext_vector_type(2))) unsigned u32x2;
typedef __attribute__((ext_vector_type(2))) __bf16 bf16x2_t;
typedef short v4i16_t __attribute__((ext_vector_type(4)));
typedef unsigned short u16;
typedef unsigned long long u64;
#define LAS __attribute__((address_space(3)))
#define GAS __attribute__((address_space(1)))
#define CAS __attribute__((address_space(4)))
typedef LAS const char* lds_cptr;
typedef LAS char* lds_ptr;
#define DI __device__ __forceinline__

constexpr int D_ = 640, F_ = 1536;
constexpr int KV_TILE_B = 2624;
constexpr int KV_CHUNK_B = 4 * KV_TILE_B;
constexpr int KV_SLOT_B = 12288;
constexpr size_t WS_EPOCH = 0, WS_SEEN = 1024, WS_HF = 8192, WS_KVF = 16384, WS_OF = 32768;
constexpr size_t WS_KV = 64 * 1024;
constexpr size_t WS_G = 240 * 1024;
constexpr size_t WS_H = 256 * 1024;
constexpr size_t WS_O = WS_H + (size_t)64 * 48 * 2048;
constexpr size_t WS_END = WS_O + (size_t)64 * 40 * 1024;
constexpr int LDS_BYTES = 80 * 1024;
constexpr unsigned SPIN_CAP = 1u << 18;

#define MFMA(a, b, c) __builtin_amdgcn_mfma_f32_32x32x16_bf16((a), (b), (c), 0, 0, 0)
#define SBAR() __builtin_amdgcn_sched_barrier(0)
#define WAIT_VM(N) asm volatile("s_waitcnt vmcnt(" #N ")" ::: "memory")
#define WAIT_BAR(N) asm volatile("s_waitcnt vmcnt(" #N ") lgkmcnt(0)\n\ts_barrier" ::: "memory")
#define PACE() do {} while (0)
#define RLX_AGENT __ATOMIC_RELAXED, __HIP_MEMORY_SCOPE_AGENT
#define BC(x) __builtin_bit_cast(bf16x8, x)

DI unsigned cvtpk(float lo, float hi) {
    f32x2 v = {lo, hi};
    return __builtin_bit_cast(unsigned, __builtin_convertvector(v, bf16x2_t));
}
DI void glds16(const void* sbase, unsigned voff, unsigned lds_base) {
    unsigned sv;
    asm volatile("s_mov_b32 %0, m0\n\ts_mov_b32 m0, %3\n\ts_nop 0\n\tglobal_load_lds_dwordx4 %1, %2\n\ts_mov_b32 m0, %0"
                 : "=&s"(sv) : "v"(voff), "s"(sbase), "s"(lds_base) : "memory");
}
DI void glds16v(const void* ptr, unsigned lds_base) {
    unsigned sv;
    asm volatile("s_mov_b32 %0, m0\n\ts_mov_b32 m0, %2\n\ts_nop 0\n\tglobal_load_lds_dwordx4 %1, off\n\ts_mov_b32 m0, %0"
                 : "=&s"(sv) : "v"(ptr), "s"(lds_base) : "memory");
}
DI void glds16_dev(const void* sbase, unsigned voff, unsigned lds_base) {
    unsigned sv;
    asm volatile("s_mov_b32 %0, m0\n\ts_mov_b32 m0, %3\n\ts_nop 0\n\tglobal_load_lds_dwordx4 %1, %2 sc1\n\ts_mov_b32 m0, %0"
                 : "=&s"(sv) : "v"(voff), "s"(sbase), "s"(lds_base) : "memory");
}
DI s16x4 trread(lds_cptr p) { return __builtin_bit_cast(s16x4, __builtin_amdgcn_ds_read_tr16_b64_v4i16((LAS v4i16_t*)p)); }
DI bf16x8 cat8(s16x4 lo, s16x4 hi) { return __builtin_shufflevector(lo, hi, 0, 1, 2, 3, 4, 5, 6, 7); }
DI bf16x8 ldsread16(lds_cptr p) { return *(const LAS bf16x8*)p; }
DI float xhalf_max(float m) {
    auto rr = __builtin_amdgcn_permlane32_swap(__float_as_uint(m), __float_as_uint(m), false, false);
    return __builtin_fmaxf(__uint_as_float(rr[0]), __uint_as_float(rr[1]));
}
DI f32x16 zero16() { f32x16 z; for (int i = 0; i < 16; ++i) z[i] = 0.f; return z; }
DI void st16_dev(__amdgpu_buffer_rsrc_t r, unsigned off, u32x4 v) { __builtin_amdgcn_raw_buffer_store_b128(v, r, off, 0, 16); }
DI void st8_dev(void* p, u32x2 v) { __hip_atomic_store((GAS u64*)p, (u64)v[0] | ((u64)v[1] << 32), RLX_AGENT); }
DI void st4_dev(void* p, float v) { __hip_atomic_store((GAS unsigned*)p, __float_as_uint(v), RLX_AGENT); }
DI u64 ld8_dev(const void* p) { return __hip_atomic_load((const GAS u64*)p, RLX_AGENT); }
DI float ld4_dev(const void* p) { return __uint_as_float(__hip_atomic_load((const GAS unsigned*)p, RLX_AGENT)); }
DI void flag_set(char* ws, size_t off, u64 tag) { __hip_atomic_store((GAS u64*)(ws + off), tag, RLX_AGENT); }
DI u64 epoch_load(const char* ws) { u64 e; asm volatile("s_load_dwordx2 %0, %1, 0x0" : "=s"(e) : "s"(ws) : "memory"); return e; }
DI u64 epoch_tag(u64 e) { u64 r; asm volatile("s_waitcnt lgkmcnt(0)\n\ts_mov_b64 %0, %1" : "=s"(r) : "s"(e) : "memory"); return r * 6364136223846793005ull + 1442695040888963407ull; }
template <int K, int SLEEP> DI void flags_wait(const char* f, int n, u64 tag, int lane) {
    for (unsigned spin = 0; spin < SPIN_CAP; ++spin) {
        bool ok = true;
#pragma unroll
        for (int k = 0; k < K; ++k) { const int i = lane + 64 * k; const u64 v = ld8_dev(f + 8 * (i < n ? i : 0)); ok &= (v == tag); }
        if (__all(ok)) break;
        __builtin_amdgcn_s_sleep(SLEEP);
    }
}

#define STAMP(kid, i) do {} while (0)
struct Params {
    const u16* x; const u16* wq; const u16* wk; const u16* wv; const u16* wo;
    const u16* wg; const u16* wu; const u16* wd; const u16* gate_w;
    u16* out; char* ws;
};

DI void kv_job(const Params& p, int rt, int isv, u64 ep, char* lds) {
    const int tid = threadIdx.x, lane = tid & 63, w = __builtin_amdgcn_readfirstlane(tid >> 6);
    const int r32 = lane & 31, h = lane >> 5;
    const unsigned lds0 = (unsigned)(uintptr_t)lds;
    STAMP(0, 0);
    const unsigned xoff = w * 17408, woff = xoff + 10240;
    const u16* xs = p.x + (size_t)rt * 32 * D_ + w * 160;
#pragma unroll
    for (int i = 0; i < 10; ++i) {
        const int idx = i * 64 + lane, row = idx / 20, ch = (idx % 20) ^ ((row >> 2) & 3);
        glds16(xs, (unsigned)(row * (D_ * 2) + ch * 16), lds0 + xoff + i * 1024);
    }
    const u16* wsrc = (isv ? p.wv : p.wk);
#pragma unroll
    for (int i = 0; i < 6; ++i) glds16(wsrc, (unsigned)(w * 6400 + i * 1024 + lane * 16), lds0 + woff + i * 1024);
    glds16v(lane < 16 ? (const char*)wsrc + w * 6400 + 6144 + lane * 16 : (const char*)p.gate_w + w * 320 + (lane < 36 ? lane - 16 : 0) * 16, lds0 + woff + 6144);
    const u64 tag = epoch_tag(ep);
    WAIT_VM(0);
    STAMP(0, 3);
    const lds_cptr xb0 = (lds_cptr)lds + xoff + r32 * 320 + ((h ^ ((r32 >> 2) & 3)) << 4), xb1 = (lds_cptr)lds + xoff + r32 * 320 + (((2 + h) ^ ((r32 >> 2) & 3)) << 4);
    const lds_cptr wb = (lds_cptr)lds + woff + (8 * h + ((lane & 15) >> 2)) * 40 + ((lane >> 4) & 1) * 32 + (lane & 3) * 8;
    const lds_cptr gb = (lds_cptr)lds + woff + 6400 + h * 16;
    bf16x8 xf[10], wf[10], gf[10];
#pragma unroll
    for (int ks = 0; ks < 10; ++ks) { xf[ks] = ldsread16(((ks & 1) ? xb1 : xb0) + (ks >> 1) * 64); wf[ks] = cat8(trread(wb + ks * 640), trread(wb + ks * 640 + 160)); gf[ks] = ldsread16(gb + ks * 32); }
    SBAR();
    f32x16 acc = zero16();
    if (isv) {
#pragma unroll
        for (int ks = 0; ks < 10; ++ks) acc = MFMA(xf[ks], wf[ks], acc);
    } else {
#pragma unroll
        for (int ks = 0; ks < 10; ++ks) acc = MFMA((r32 == 20) ? gf[ks] : wf[ks], xf[ks], acc);
    }
    LAS f32x4* red = (LAS f32x4*)((lds_ptr)lds + 69632);
    if (w > 0) {
#pragma unroll
        for (int g = 0; g < 4; ++g) red[((w - 1) * 4 + g) * 64 + lane] = (f32x4){acc[4 * g], acc[4 * g + 1], acc[4 * g + 2], acc[4 * g + 3]};
    }
    WAIT_BAR(0);
    if (w == 0) {
#pragma unroll
        for (int ww = 0; ww < 3; ++ww)
#pragma unroll
            for (int g = 0; g < 4; ++g) { const f32x4 t = red[(ww * 4 + g) * 64 + lane]; acc[4 * g] += t[0]; acc[4 * g + 1] += t[1]; acc[4 * g + 2] += t[2]; acc[4 * g + 3] += t[3]; }
    }
    WAIT_BAR(0);
    if (w == 0) {
        const __amdgpu_buffer_rsrc_t rs = __builtin_amdgcn_make_buffer_rsrc(p.ws, 0, (int)WS_END, 0x00020000);
        const unsigned tile = (unsigned)WS_KV + (unsigned)(isv ? rt : ((rt + 63) & 63)) * KV_TILE_B;
        u32x4 lo = {cvtpk(acc[0], acc[1]), cvtpk(acc[2], acc[3]), cvtpk(acc[4], acc[5]), cvtpk(acc[6], acc[7])};
        u32x4 hi = {cvtpk(acc[8], acc[9]), cvtpk(acc[10], acc[11]), cvtpk(acc[12], acc[13]), cvtpk(acc[14], acc[15])};
        if (!isv) {
            st16_dev(rs, tile + lane * 16, lo);
            if (h == 0) st8_dev(p.ws + tile + 1024 + r32 * 8, (u32x2){hi[0], hi[1]});
            else st4_dev(p.ws + WS_G + (rt * 32 + r32) * 4, 1.f / (1.f + __expf(-acc[8])));
        } else {
            if (r32 == 20) { lo = (u32x4){0x3f803f80u, 0x3f803f80u, 0x3f803f80u, 0x3f803f80u}; hi = lo; }
            if (r32 <= 20) { st16_dev(rs, tile + 1280 + h * 336 + r32 * 16, lo); st16_dev(rs, tile + 1952 + h * 336 + r32 * 16, hi); }
        }
        STAMP(0, 6);
        WAIT_VM(0);
        STAMP(0, 7);
        if (lane < 16) flag_set(p.ws, WS_KVF + lane * 1024 + (rt * 2 + isv) * 8, tag);
    }
}

#define AE(b, i) "v_exp_f32 v[" #b "+" #i "], v[" #b "+" #i "]\n"
#define AC(pb, j, b, i) "v_cvt_pk_bf16_f32 v[" #pb "+" #j "], v[" #b "+" #i "], v[" #b "+" #i "+1]\n"
#define AG2(MA, MB, X, XI, PX, PJ) MA AE(X, XI) AE(X, XI+1) AE(X, XI+2) AE(X, XI+3) AC(PX, PJ, X, XI) AC(PX, PJ+1, X, XI+2) \
    MB AE(X, XI+4) AE(X, XI+5) AE(X, XI+6) AE(X, XI+7) AC(PX, PJ+2, X, XI+4) AC(PX, PJ+3, X, XI+6)
#define AHALF(X, PX, Y, Q1, Q2, O, PR, BUF, R1, R2, R3, R4) \
    AG2("v_mfma_f32_32x32x16_bf16 v[" #Y ":" #Y "+15], v[" #BUF ":" #BUF "+3], " Q1 ", 0\n" R1, \
        "v_mfma_f32_32x32x16_bf16 v[" #Y ":" #Y "+15], v[" #BUF "+4:" #BUF "+7], " Q2 ", v[" #Y ":" #Y "+15]\n" R2, X, 0, PX, 0) \
    AG2("v_mfma_f32_32x32x16_bf16 " O ", v[" #BUF "+8:" #BUF "+11], v[" #PR ":" #PR "+3], " O "\n" R3, \
        "v_mfma_f32_32x32x16_bf16 " O ", v[" #BUF "+12:" #BUF "+15], v[" #PR "+4:" #PR "+7], " O "\n" R4, X, 8, PX, 4)
#define ARK1(NB, sl, tl) "ds_read_b128 v[" #NB ":" #NB "+3], %[k1a] offset:" #sl "*12288+" #tl "*2624\n"
#define ARK2(NB, sl, tl) "ds_read_b64 v[" #NB "+4:" #NB "+5], %[k2a] offset:" #sl "*12288+" #tl "*2624\n"
#define ARV0(NB, sl, tl) "ds_read_b128 v[" #NB "+8:" #NB "+11], %[va] offset:" #sl "*12288+" #tl "*2624\n"
#define ARV1(NB, sl, tl) "ds_read_b128 v[" #NB "+12:" #NB "+15], %[va] offset:" #sl "*12288+" #tl "*2624+672\n"
#define AH1(PREV, R1, R2, R3, R4) AHALF(128, 160, 144, "%[qb1]", "%[qb2]", "%[ob]", 168, PREV, R1, R2, R3, R4)
#define AH2(CUR, PREV, sl, tl) AHALF(144, 168, 128, "%[qa1]", "%[qa2]", "%[oa]", 160, CUR, ARK1(PREV, sl, tl), ARK2(PREV, sl, tl), ARV0(PREV, sl, tl), ARV1(PREV, sl, tl))
#define AWL "s_waitcnt lgkmcnt(0)\n"
#define ASYNC "s_waitcnt vmcnt(6) lgkmcnt(0)\ns_barrier\n"
#define ADMA0(fs) "s_mul_i32 %[of], %[ck], 10496\n s_add_u32 %[ck], %[ck], 1\n s_and_b32 %[ck], %[ck], 15\n s_add_u32 m0, %[ldst], " #fs "*12288\n" \
                  "v_add_u32 v208, %[of], %[kvo]\n"
#define ADMA1 "global_load_lds_dwordx4 v208, %[img] sc1\n"
#define ADMA2 "global_load_lds_dwordx4 v208, %[img] offset:1024 sc1\n"
#define ADMA3 "global_load_lds_dwordx4 v208, %[img] offset:2048 sc1\n"
#define ACHUNK(sl, nsl) \
    AH1(192, "", "", "", "") AWL AH2(176, 192, sl, 2) \
    AH1(176, "", "", "", "") AWL AH2(192, 176, sl, 3) \
    AH1(192, "", "", "", "") ASYNC AH2(176, 192, nsl, 0) \
    AH1(176, ADMA0(sl), ADMA1, ADMA2, ADMA3) AWL AH2(192, 176, nsl, 1)
#define AMOV(d, s) "v_mov_b32 v" #d ", " s "\n"
#define ATTN_LOOP_ASM \
    "s_setprio 2\n s_mov_b32 %[m0s], m0\n s_mov_b32 %[it], 4\n s_mov_b32 %[ck], 4\n" \
    ARK1(176, 1, 1) ARK2(176, 1, 1) ARV0(176, 1, 1) ARV1(176, 1, 1) \
    "v_mov_b64 v[192:193], %[k0a]\n v_mov_b64 v[194:195], %[k0b]\n" \
    "v_mov_b64 v[196:197], %[k02]\n" AMOV(198, "%[kone]") AMOV(199, "0") AMOV(182, "%[kone]") AMOV(183, "0") \
    "v_mfma_f32_32x32x16_bf16 v[128:143], v[192:195], %[qa1], 0\n" \
    AMOV(200, "0") AMOV(201, "0") AMOV(202, "0") AMOV(203, "0") AMOV(204, "0") AMOV(205, "0") AMOV(206, "0") AMOV(207, "0") \
    AMOV(168, "0") AMOV(169, "0") AMOV(170, "0") AMOV(171, "0") AMOV(172, "0") AMOV(173, "0") AMOV(174, "0") AMOV(175, "0") \
    "v_mfma_f32_32x32x16_bf16 v[128:143], v[196:199], %[qa2], v[128:143]\n" \
    "s_nop 7\n s_nop 7\n" \
    "1:\n" \
    ACHUNK(1, 2) ACHUNK(2, 3) ACHUNK(3, 0) ACHUNK(0, 1) \
    "s_sub_u32 %[it], %[it], 1\n s_cmp_lg_u32 %[it], 0\n s_cbranch_scc1 1b\n" \
    "v_mfma_f32_32x32x16_bf16 %[ob], v[200:203], v[168:171], %[ob]\n" \
    "v_mfma_f32_32x32x16_bf16 %[ob], v[204:207], v[172:175], %[ob]\n" \
    "s_setprio 3\n s_waitcnt vmcnt(0) lgkmcnt(0)\n s_mov_b32 m0, %[m0s]\n s_nop 7\n s_nop 7\n"
#define ATTN_CLOBBERS "v128","v129","v130","v131","v132","v133","v134","v135","v136","v137","v138","v139","v140","v141","v142","v143", \
    "v144","v145","v146","v147","v148","v149","v150","v151","v152","v153","v154","v155","v156","v157","v158","v159", \
    "v160","v161","v162","v163","v164","v165","v166","v167","v168","v169","v170","v171","v172","v173","v174","v175", \
    "v176","v177","v178","v179","v180","v181","v182","v183","v184","v185","v186","v187","v188","v189","v190","v191", \
    "v192","v193","v194","v195","v196","v197","v198","v199","v200","v201","v202","v203","v204","v205","v206","v207","v208"

DI void attn_wg(const Params& p, int rb, int c, u64 ep, char* lds) {
    const int tid = threadIdx.x, lane = tid & 63, w = __builtin_amdgcn_readfirstlane(tid >> 6);
    const int r32 = lane & 31, h = lane >> 5;
    const unsigned lds0 = (unsigned)(uintptr_t)lds;
    const char* kvimg = p.ws + WS_KV;
    STAMP(1, 0);
    constexpr int RB = 32768;
    const unsigned kvoff = (unsigned)(w * 3072 + lane * 16);
#define KV_DMA(chunk) do { const char* s_ = kvimg + (size_t)(chunk) * KV_CHUNK_B; const unsigned d_ = lds0 + RB + (((chunk) + 1) & 3) * KV_SLOT_B + w * 3072; \
        glds16_dev(s_, kvoff, d_); glds16_dev(s_, kvoff + 1024, d_ + 1024); glds16_dev(s_, kvoff + 2048, d_ + 2048); } while (0)
    constexpr int QS_B = 18432;
    unsigned qoff[5]; unsigned qdst[5];
#pragma unroll
    for (int j = 0; j < 5; ++j) {
        int i = w + 4 * j; if (i >= 18) i -= 2;
        if (j < 2) { const int row = 8 * i + (lane >> 3), pos = lane & 7, ch = pos ^ ((row >> 1) & 7); qoff[j] = row * (D_ * 2) + ch * 16; }
        else { const int idx = (i - 8) * 64 + lane, kr = idx / 10, ch = idx % 10; qoff[j] = kr * (D_ * 2) + ch * 16; }
        qdst[j] = lds0 + i * 1024;
    }
    const u16* xsrc = p.x + (size_t)rb * 64 * D_;
    const u16* wsrc = p.wq + c * 80;
#define Q_DMA(kt) do { const unsigned d_ = ((kt) & 3) * QS_B; const u16* xs_ = xsrc + (kt) * 64; const u16* ws_ = wsrc + (size_t)(kt) * 64 * D_; \
        glds16(xs_, qoff[0], d_ + qdst[0]); glds16(xs_, qoff[1], d_ + qdst[1]); glds16(ws_, qoff[2], d_ + qdst[2]); glds16(ws_, qoff[3], d_ + qdst[3]); glds16(ws_, qoff[4], d_ + qdst[4]); } while (0)
    Q_DMA(0); Q_DMA(1); Q_DMA(2); Q_DMA(3);
    const u64 tag = epoch_tag(ep);
    f32x16 qa = zero16(), qb = zero16();
    {
        unsigned xa[4];
#pragma unroll
        for (int ks = 0; ks < 4; ++ks) xa[ks] = r32 * 128 + (((2 * ks + h) ^ ((r32 >> 1) & 7)) << 4);
        const unsigned wa = 8192 + (8 * h + ((lane & 15) >> 2)) * 160 + w * 40 + ((lane >> 4) & 1) * 32 + (lane & 3) * 8;
        bf16x8 wfA[4], x0A[4], x1A[4], wfB[4], x0B[4], x1B[4];
#define Q_RD(S, kt, ks) do { const lds_cptr sb_ = (lds_cptr)lds + ((kt) & 3) * QS_B; wf##S[ks] = cat8(trread(sb_ + wa + (ks) * 2560), trread(sb_ + wa + (ks) * 2560 + 640)); \
            x0##S[ks] = ldsread16(sb_ + xa[ks]); x1##S[ks] = ldsread16(sb_ + xa[ks] + 4096); } while (0)
#define Q_STEP(C, N, kt) do { \
            if ((kt) == 7 && w == 0) { flags_wait<2, 4>(p.ws + WS_KVF + (blockIdx.x & 15) * 1024, 128, tag, lane); STAMP(1, 5); }     \
            if ((kt) < 7) { WAIT_BAR(10); } else if ((kt) == 7) { WAIT_BAR(5); } else if ((kt) == 8) { WAIT_BAR(9); } \
            if ((kt) + 4 < 10) Q_DMA((kt) + 4); \
            if ((kt) == 7) { KV_DMA(0); KV_DMA(1); KV_DMA(2); } \
            _Pragma("unroll") for (int ks = 0; ks < 4; ++ks) { qa = MFMA(wf##C[ks], x0##C[ks], qa); qb = MFMA(wf##C[ks], x1##C[ks], qb); if ((kt) + 1 < 10) Q_RD(N, (kt) + 1, ks); SBAR(); } } while (0)
        WAIT_BAR(15);
        STAMP(3, 0);
        if (tid == 0) flag_set(p.ws, WS_SEEN + blockIdx.x * 8, tag);
#pragma unroll
        for (int ks = 0; ks < 4; ++ks) Q_RD(A, 0, ks);
#pragma unroll
        for (int kt = 0; kt < 10; kt += 2) { Q_STEP(A, B, kt); STAMP(3, 1 + kt); Q_STEP(B, A, kt + 1); }
    }
    STAMP(1, 1);
    constexpr float C2 = 0.22360679774997896f * 1.4426950408889634f;
    u32x4 qa1, qa2, qb1, qb2;
    qa1 = (u32x4){cvtpk(qa[0] * C2, qa[1] * C2), cvtpk(qa[2] * C2, qa[3] * C2), cvtpk(qa[4] * C2, qa[5] * C2), cvtpk(qa[6] * C2, qa[7] * C2)};
    qb1 = (u32x4){cvtpk(qb[0] * C2, qb[1] * C2), cvtpk(qb[2] * C2, qb[3] * C2), cvtpk(qb[4] * C2, qb[5] * C2), cvtpk(qb[6] * C2, qb[7] * C2)};
    qa2 = (u32x4){h ? 0u : cvtpk(qa[8] * C2, qa[9] * C2), h ? 0u : cvtpk(qa[10] * C2, qa[11] * C2), 0u, 0u};
    qb2 = (u32x4){h ? 0u : cvtpk(qb[8] * C2, qb[9] * C2), h ? 0u : cvtpk(qb[10] * C2, qb[11] * C2), 0u, 0u};
    WAIT_BAR(6);
    KV_DMA(3);
    const unsigned k1a = lane * 16, k2a = 1024 + r32 * 8;
    STAMP(1, 6);
    const bf16x8 k01 = ldsread16((lds_cptr)lds + RB + KV_SLOT_B + k1a);
    const u32x2 k02 = *(const LAS u32x2*)((lds_cptr)lds + RB + KV_SLOT_B + k2a);
    const unsigned va = 1280 + h * 336 + (r32 <= 20 ? r32 : (r32 < 28 ? 20 : 16)) * 16;
    const unsigned kone = h ? 0u : 0x3f80u;
    auto kfrag2 = [](u32x2 v, unsigned one) { return __builtin_bit_cast(bf16x8, (u32x4){v[0], v[1], one, 0u}); };
    {
        f32x16 sa = MFMA(k01, BC(qa1), zero16()); sa = MFMA(kfrag2(k02, kone), BC(qa2), sa);
        f32x16 sb = MFMA(k01, BC(qb1), zero16()); sb = MFMA(kfrag2(k02, kone), BC(qb2), sb);
        float ma = sa[0], mb = sb[0];
#pragma unroll
        for (int r = 1; r < 16; ++r) { ma = __builtin_fmaxf(ma, sa[r]); mb = __builtin_fmaxf(mb, sb[r]); }
        ma = xhalf_max(ma); mb = xhalf_max(mb);
        qa2[2] = h ? 0u : (cvtpk(-ma, 0.f) & 0xffffu);
        qb2[2] = h ? 0u : (cvtpk(-mb, 0.f) & 0xffffu);
    }
    f32x16 oa = zero16(), ob = zero16();
    float ga, gb;
    asm volatile("global_load_dword %0, %2, %3 sc1\n\tglobal_load_dword %1, %2, %3 offset:128 sc1" : "=&v"(ga), "=&v"(gb) : "v"(r32 * 4), "s"(p.ws + WS_G + rb * 256) : "memory");
    STAMP(1, 2);
    {
        unsigned t_m0, t_it, t_ck, t_of;
        const u32x4 k0u = __builtin_bit_cast(u32x4, k01); const u32x2 k0a = {k0u[0], k0u[1]}, k0b = {k0u[2], k0u[3]};
        asm volatile(ATTN_LOOP_ASM
            : [oa] "+v"(oa), [ob] "+v"(ob), "+v"(ga), "+v"(gb), [m0s] "=&s"(t_m0), [it] "=&s"(t_it), [ck] "=&s"(t_ck), [of] "=&s"(t_of)
            : [qa1] "v"(qa1), [qa2] "v"(qa2), [qb1] "v"(qb1), [qb2] "v"(qb2), [k1a] "v"(lds0 + RB + k1a), [k2a] "v"(lds0 + RB + k2a), [va] "v"(lds0 + RB + va),
              [kvo] "v"(kvoff), [img] "s"(kvimg), [ldst] "s"(lds0 + RB + w * 3072), [k0a] "v"(k0a), [k0b] "v"(k0b), [k02] "v"(k02), [kone] "v"(kone)
            : ATTN_CLOBBERS, "scc", "memory");
    }
    STAMP(1, 3);
    {
        float chk = 0.f;
#pragma unroll
        for (int r = 0; r < 8; ++r) chk += oa[r] * 0.f + ob[r] * 0.f;
        if (h == 0) { chk += (oa[8] + oa[9] + oa[10] + oa[11] + ob[8] + ob[9] + ob[10] + ob[11]) * 0.f; } else { chk += (oa[8] + ob[8]) * 0.f; }
        if (h == 1 && !(oa[8] > 1e-30f && ob[8] > 1e-30f)) chk = 1.f;
        if (__builtin_expect(__any(!(chk == 0.f)), 0)) {
            __builtin_amdgcn_fence(__ATOMIC_ACQUIRE, "agent");
            qa2[2] = 0u; qb2[2] = 0u;
            oa = zero16(); ob = zero16();
            float ma = -INFINITY, mb = -INFINITY;
            for (int t = 0; t < 64; ++t) {
                const char* tb = kvimg + (size_t)t * KV_TILE_B;
                const char* kb = kvimg + (size_t)((t + 63) & 63) * KV_TILE_B;
                const bf16x8 k1 = *(const bf16x8*)(kb + k1a), k2 = kfrag2(*(const u32x2*)(kb + k2a), kone);
                const bf16x8 v0 = *(const bf16x8*)(tb + va), v1 = *(const bf16x8*)(tb + va + 672);
                f32x16 sa = MFMA(k1, BC(qa1), zero16()); sa = MFMA(k2, BC(qa2), sa);
                f32x16 sb = MFMA(k1, BC(qb1), zero16()); sb = MFMA(k2, BC(qb2), sb);
                float ta = ma, tb_ = mb;
#pragma unroll
                for (int r = 0; r < 16; ++r) { ta = __builtin_fmaxf(ta, sa[r]); tb_ = __builtin_fmaxf(tb_, sb[r]); }
                ta = xhalf_max(ta); tb_ = xhalf_max(tb_);
                const float fa = __builtin_amdgcn_exp2f(ma - ta), fb = __builtin_amdgcn_exp2f(mb - tb_);
                ma = ta; mb = tb_;
#pragma unroll
                for (int r = 0; r < 16; ++r) { oa[r] *= fa; ob[r] *= fb; sa[r] = __builtin_amdgcn_exp2f(sa[r] - ma); sb[r] = __builtin_amdgcn_exp2f(sb[r] - mb); }
                u32x4 pa0, pa1, pb0, pb1;
#pragma unroll
                for (int j = 0; j < 4; ++j) { pa0[j] = cvtpk(sa[2 * j], sa[2 * j + 1]); pa1[j] = cvtpk(sa[8 + 2 * j], sa[9 + 2 * j]); pb0[j] = cvtpk(sb[2 * j], sb[2 * j + 1]); pb1[j] = cvtpk(sb[8 + 2 * j], sb[9 + 2 * j]); }
                oa = MFMA(v0, BC(pa0), oa); oa = MFMA(v1, BC(pa1), oa);
                ob = MFMA(v0, BC(pb0), ob); ob = MFMA(v1, BC(pb1), ob);
            }
        }
    }
    const float la = __shfl(oa[8], r32 + 32), lb = __shfl(ob[8], r32 + 32);
    const float fa = ga * __builtin_amdgcn_rcpf(la), fb = gb * __builtin_amdgcn_rcpf(lb);
    const int head = 4 * c + w;
    const __amdgpu_buffer_rsrc_t rs = __builtin_amdgcn_make_buffer_rsrc(p.ws, 0, (int)WS_END, 0x00020000);
    const unsigned ob0 = (unsigned)WS_O + (unsigned)(rb * 80 + (head >> 3) * 10) * 1024;
    st16_dev(rs, ob0 + (head & 7) * 1024 + lane * 16, (u32x4){cvtpk(oa[0] * fa, oa[1] * fa), cvtpk(oa[2] * fa, oa[3] * fa), cvtpk(oa[4] * fa, oa[5] * fa), cvtpk(oa[6] * fa, oa[7] * fa)});
    st16_dev(rs, ob0 + 40960 + (head & 7) * 1024 + lane * 16, (u32x4){cvtpk(ob[0] * fb, ob[1] * fb), cvtpk(ob[2] * fb, ob[3] * fb), cvtpk(ob[4] * fb, ob[5] * fb), cvtpk(ob[6] * fb, ob[7] * fb)});
    {
        const u32x2 ua = {cvtpk(oa[8] * fa, oa[9] * fa), cvtpk(oa[10] * fa, oa[11] * fa)}, ub = {cvtpk(ob[8] * fb, ob[9] * fb), cvtpk(ob[10] * fb, ob[11] * fb)};
        const unsigned s0 = (unsigned)__shfl((int)ub[0], r32), s1 = (unsigned)__shfl((int)ub[1], r32);
        const u32x2 uv = {h ? s0 : ua[0], h ? s1 : ua[1]};
        st8_dev(p.ws + ob0 + h * 40960 + (8 + ((head >> 2) & 1)) * 1024 + ((head & 1) * 32 + r32) * 16 + ((head >> 1) & 1) * 8, uv);
    }
    WAIT_VM(0);
    if (lane == 0) flag_set(p.ws, WS_OF + (rb * 4 + (head >> 3)) * 128 + (head & 7) * 8, tag);
    STAMP(1, 4);
    if (blockIdx.x == 0 && w == 0) {
        flags_wait<8, 32>(p.ws + WS_SEEN, 512, tag, lane);
        if (lane == 0) flag_set(p.ws, WS_EPOCH, tag);
    }
#undef KV_DMA
#undef Q_DMA
}

DI u64 ffn_up_wg(const Params& p, int rb2, int fs, u64 ep, char* lds) {
    const int tid = threadIdx.x, lane = tid & 63, w = __builtin_amdgcn_readfirstlane(tid >> 6);
    const int r32 = lane & 31, h = lane >> 5;
    const unsigned lds0 = (unsigned)(uintptr_t)lds;
    constexpr int XS_B = 16384, WS_B = 12288, WS_0 = 2 * XS_B;
    const int xrow = 8 * w + (lane >> 3);
    const unsigned xo = xrow * (D_ * 2) + (((lane & 7) ^ ((xrow >> 1) & 7)) << 4);
    unsigned wo_[3];
#pragma unroll
    for (int j = 0; j < 3; ++j) { const int idx = (w + 4 * j) * 64 + lane, r = idx % 384; wo_[j] = (r / 12) * (F_ * 2) + ((r % 12) ^ (idx >= 384 ? 2 : 0)) * 16; }
    const u16* xsrc = p.x + (size_t)rb2 * 128 * D_;
    const u16* gsrc = p.wg + fs * 96;
    const u16* usrc = p.wu + fs * 96;
    const u16* w1src = (w < 2) ? gsrc : usrc;
    const unsigned dw = lds0 + w * 1024;
#define UP_X(kb, m) glds16(xsrc + (kb) * 64 + (m) * 32 * D_, xo, dw + ((kb) & 1) * XS_B + (m) * 4096)
#define UP_W0(s) glds16(gsrc + (size_t)(s) * 32 * F_, wo_[0], dw + WS_0 + ((s) & 3) * WS_B)
#define UP_W1(s) glds16(w1src + (size_t)(s) * 32 * F_, wo_[1], dw + WS_0 + ((s) & 3) * WS_B + 4096)
#define UP_W2(s) glds16(usrc + (size_t)(s) * 32 * F_, wo_[2], dw + WS_0 + ((s) & 3) * WS_B + 8192)
#define UP_XA(kb) do { UP_X(kb, 0); UP_X(kb, 1); UP_X(kb, 2); UP_X(kb, 3); } while (0)
#define UP_WA(s) do { UP_W0(s); UP_W1(s); UP_W2(s); } while (0)
#define X_UPPRIO 1
    __builtin_amdgcn_s_setprio(X_UPPRIO);
    STAMP(2, 1);
    UP_XA(0); UP_WA(0); UP_WA(1); UP_XA(1); UP_WA(2); UP_WA(3);
    const u64 tag = epoch_tag(ep);
    f32x16 acc[6];
#pragma unroll
    for (int i = 0; i < 6; ++i) acc[i] = zero16();
    const int wr = w >> 1, wc = w & 1, row = 64 * wr + r32, blk = (lane >> 4) & 1;
    lds_cptr xa[4], wa[3];
#pragma unroll
    for (int kk = 0; kk < 4; ++kk) xa[kk] = (lds_cptr)lds + row * 128 + (((2 * kk + h) ^ ((row >> 1) & 7)) << 4);
#pragma unroll
    for (int i = 0; i < 3; ++i) wa[i] = (lds_cptr)lds + WS_0 + blk * 6144 + (8 * h + ((lane & 15) >> 2)) * 192 + ((3 * wc + i) ^ blk) * 32 + (lane & 3) * 8;
    bf16x8 xf0[2], xf1[2], wf0[3], wf1[3];
#define UP_RX(q, rt) ldsread16(xa[(q) & 3] + (((q) >> 2) & 1) * XS_B + (rt) * 4096)
#define UP_RW(q, i) cat8(trread(wa[i] + (((q) >> 1) & 3) * WS_B + ((q) & 1) * 3072), trread(wa[i] + (((q) >> 1) & 3) * WS_B + ((q) & 1) * 3072 + 768))
#define UP_STEP(C, N, q, D0, D1, D2, D3, D4, D5) do { \
        acc[0] = MFMA(wf##C[0], xf##C[0], acc[0]); xf##N[0] = UP_RX(q, 0); D0; SBAR(); PACE(); \
        acc[3] = MFMA(wf##C[0], xf##C[1], acc[3]); wf##N[0] = UP_RW(q, 0); D1; SBAR(); PACE(); \
        acc[1] = MFMA(wf##C[1], xf##C[0], acc[1]); xf##N[1] = UP_RX(q, 1); D2; SBAR(); PACE(); \
        acc[4] = MFMA(wf##C[1], xf##C[1], acc[4]); wf##N[1] = UP_RW(q, 1); D3; SBAR(); PACE(); \
        acc[2] = MFMA(wf##C[2], xf##C[0], acc[2]); wf##N[2] = UP_RW(q, 2); D4; SBAR(); PACE(); \
        acc[5] = MFMA(wf##C[2], xf##C[1], acc[5]); D5; SBAR(); PACE(); } while (0)
#define NOP_ do {} while (0)
    WAIT_BAR(13);
    STAMP(2, 9);
    if (tid == 0) flag_set(p.ws, WS_SEEN + blockIdx.x * 8, tag);
    xf0[0] = UP_RX(0, 0); xf0[1] = UP_RX(0, 1);
#pragma unroll
    for (int i = 0; i < 3; ++i) wf0[i] = UP_RW(0, i);
    for (int it = 0; it < 4; ++it) {
        const int s0 = 4 * it, kb = 2 * it;
        UP_STEP(0, 1, 1, NOP_, NOP_, NOP_, NOP_, NOP_, NOP_);
        WAIT_BAR(10);
        UP_STEP(1, 0, 2, UP_W0(s0 + 4), UP_W1(s0 + 4), UP_W2(s0 + 4), NOP_, NOP_, NOP_);
        UP_STEP(0, 1, 3, NOP_, NOP_, NOP_, NOP_, NOP_, NOP_);
        WAIT_BAR(6);
        UP_STEP(1, 0, 4, UP_X(kb + 2, 0), UP_X(kb + 2, 1), UP_X(kb + 2, 2), UP_X(kb + 2, 3), UP_W0(s0 + 5), UP_W1(s0 + 5); UP_W2(s0 + 5));
        UP_STEP(0, 1, 5, NOP_, NOP_, NOP_, NOP_, NOP_, NOP_);
        WAIT_BAR(10);
        UP_STEP(1, 0, 6, UP_W0(s0 + 6), UP_W1(s0 + 6), UP_W2(s0 + 6), NOP_, NOP_, NOP_);
        UP_STEP(0, 1, 7, NOP_, NOP_, NOP_, NOP_, NOP_, NOP_);
        WAIT_BAR(6);
        UP_STEP(1, 0, 0, UP_X(kb + 3, 0), UP_X(kb + 3, 1), UP_X(kb + 3, 2), UP_X(kb + 3, 3), UP_W0(s0 + 7), UP_W1(s0 + 7); UP_W2(s0 + 7));
        STAMP(2, 10 + it);
    }
    UP_STEP(0, 1, 1, NOP_, NOP_, NOP_, NOP_, NOP_, NOP_);
    WAIT_BAR(0);
    UP_STEP(1, 0, 2, NOP_, NOP_, NOP_, NOP_, NOP_, NOP_);
    UP_STEP(0, 1, 3, NOP_, NOP_, NOP_, NOP_, NOP_, NOP_);
    UP_STEP(1, 0, 4, NOP_, NOP_, NOP_, NOP_, NOP_, NOP_);
    UP_STEP(0, 1, 5, NOP_, NOP_, NOP_, NOP_, NOP_, NOP_);
    UP_STEP(1, 0, 6, NOP_, NOP_, NOP_, NOP_, NOP_, NOP_);
    UP_STEP(0, 1, 7, NOP_, NOP_, NOP_, NOP_, NOP_, NOP_);
#pragma unroll
    for (int i = 0; i < 6; ++i) acc[i] = MFMA(wf1[i % 3], xf1[i / 3], acc[i]);
    __builtin_amdgcn_s_setprio(3);
    STAMP(2, 2);
    const __amdgpu_buffer_rsrc_t rs = __builtin_amdgcn_make_buffer_rsrc(p.ws, 0, (int)WS_END, 0x00020000);
    const unsigned ho = (unsigned)WS_H + (unsigned)((rb2 * 4 + 2 * wr) * 96 + fs * 6 + 3 * wc) * 1024 + lane * 16;
#pragma unroll
    for (int j = 0; j < 6; ++j) {
        float hv[8];
#pragma unroll
        for (int r = 0; r < 8; ++r) hv[r] = __builtin_fmaxf(acc[j][r], 0.f) * acc[j][8 + r];
        st16_dev(rs, ho + (j / 3) * 96 * 1024 + (j % 3) * 1024, (u32x4){cvtpk(hv[0], hv[1]), cvtpk(hv[2], hv[3]), cvtpk(hv[4], hv[5]), cvtpk(hv[6], hv[7])});
    }
    WAIT_BAR(0);
    if (tid == 0) flag_set(p.ws, WS_HF + (rb2 * 4 + (fs >> 2)) * 128 + (fs & 3) * 8, tag);
    STAMP(2, 3);
    return tag;
}

DI void ld16_dev(bf16x8& d0, bf16x8& d1, const char* sbase, unsigned voff) {
    asm volatile("global_load_dwordx4 %0, %2, %3 sc1\n\tglobal_load_dwordx4 %1, %2, %3 offset:1024 sc1" : "=&v"(d0), "=&v"(d1) : "v"(voff), "s"(sbase) : "memory");
}
DI void out_wg(const Params& p, int rb, int cn, u64 tag, char* lds) {
    const int tid = threadIdx.x, lane = tid & 63, w = __builtin_amdgcn_readfirstlane(tid >> 6);
    const int r32 = lane & 31, h = lane >> 5;
    const unsigned lds0 = (unsigned)(uintptr_t)lds + w * 20480;
    unsigned wo_[5], wm_[5], wl_[5];
#pragma unroll
    for (int j = 0; j < 5; ++j) {
        const int idx = j * 64 + lane, r = idx / 10, ch = (idx % 10) * 16;
        wo_[j] = r * (D_ * 2) + ch; wm_[j] = ((r >> 4) * 20 + (r & 15)) * (D_ * 2) + ch; wl_[j] = ((r >> 2) * 20 + 16 + (r & 3)) * (D_ * 2) + ch;
    }
    const u16* dsrc = p.wd + (size_t)w * 384 * D_ + cn * 80;
    const u16* osrc = p.wo + (size_t)w * 160 * D_ + cn * 80;
    const char* hsrc = p.ws + WS_H + ((size_t)(rb * 2) * 48 + w * 12) * 2048;
    const char* asrc = p.ws + WS_O + ((size_t)(rb * 2) * 40 + w * 10) * 1024;
    const unsigned lo16 = lane * 16;
#define W_PIECES(s_, o_, d_) do { glds16(s_, o_[0], d_); glds16(s_, o_[1], d_ + 1024); glds16(s_, o_[2], d_ + 2048); glds16(s_, o_[3], d_ + 3072); glds16(s_, o_[4], d_ + 4096); } while (0)
#define W_DMA(u) do { const unsigned d_ = lds0 + ((u) & 3) * 5120; \
        if ((u) < 12) W_PIECES(dsrc + (size_t)(u) * 32 * D_, wo_, d_); else if ((u) < 16) W_PIECES(osrc + (size_t)((u) - 12) * 40 * D_, wm_, d_); else W_PIECES(osrc, wl_, d_); } while (0)
    W_DMA(0); W_DMA(1); W_DMA(2); W_DMA(3);
    f32x16 acc[2][3];
#pragma unroll
    for (int i = 0; i < 2; ++i)
#pragma unroll
        for (int j = 0; j < 3; ++j) acc[i][j] = zero16();
    const lds_cptr wbase = (lds_cptr)lds + w * 20480 + (4 * h + ((lane & 15) >> 2)) * 160 + ((lane >> 4) & 1) * 32 + (lane & 3) * 8;
    bf16x8 wf[2][6];
    bf16x8 af[5][4];
#define W_RD(u, i) wf[(u) & 1][i] = cat8(trread(wbase + ((u) & 3) * 5120 + ((i) / 3) * 2560 + ((i) % 3) * 64), trread(wbase + ((u) & 3) * 5120 + ((i) / 3) * 2560 + ((i) % 3) * 64 + 1280))
#define H_LD(u) do { ld16_dev(af[(u) & 3][0], af[(u) & 3][1], hsrc + (u) * 2048, lo16); ld16_dev(af[(u) & 3][2], af[(u) & 3][3], hsrc + 48 * 2048 + (u) * 2048, lo16); } while (0)
#define UNIT(u, A) do { _Pragma("unroll") for (int i = 0; i < 6; ++i) { \
        acc[0][i % 3] = MFMA(wf[(u) & 1][i], A[i / 3], acc[0][i % 3]); if ((u) < 12) PACE(); acc[1][i % 3] = MFMA(wf[(u) & 1][i], A[2 + i / 3], acc[1][i % 3]); \
        if ((u) + 1 < 17) W_RD((u) + 1, i); SBAR(); if ((u) < 12) PACE(); } } while (0)
    flags_wait<1, 32>(p.ws + WS_KVF + (blockIdx.x & 15) * 1024 + (rb * 4) * 8, 4, tag, lane);
    flags_wait<1, 16>(p.ws + WS_HF + ((rb >> 1) * 4 + w) * 128, 4, tag, lane);
    STAMP(2, 4);
    const float g0 = 1.f - ld4_dev(p.ws + WS_G + (rb * 64 + r32) * 4), g1 = 1.f - ld4_dev(p.ws + WS_G + (rb * 64 + 32 + r32) * 4);
    asm volatile("s_waitcnt vmcnt(0)" :: "v"(g0), "v"(g1) : "memory");
    H_LD(0); H_LD(1); H_LD(2);
#pragma unroll
    for (int i = 0; i < 6; ++i) W_RD(0, i);
    __builtin_amdgcn_s_setprio(1);
#pragma unroll
    for (int u = 0; u < 12; ++u) {
        if (u == 0) asm volatile("s_waitcnt vmcnt(8) lgkmcnt(0)" : "+v"(af[0][0]), "+v"(af[0][1]), "+v"(af[0][2]), "+v"(af[0][3]) :: "memory");
        else if (u == 1) asm volatile("s_waitcnt vmcnt(13) lgkmcnt(0)" : "+v"(af[1][0]), "+v"(af[1][1]), "+v"(af[1][2]), "+v"(af[1][3]) :: "memory");
        else if (u <= 9) asm volatile("s_waitcnt vmcnt(18) lgkmcnt(0)" : "+v"(af[u & 3][0]), "+v"(af[u & 3][1]), "+v"(af[u & 3][2]), "+v"(af[u & 3][3]) :: "memory");
        else if (u == 10) asm volatile("s_waitcnt vmcnt(14) lgkmcnt(0)" : "+v"(af[u & 3][0]), "+v"(af[u & 3][1]), "+v"(af[u & 3][2]), "+v"(af[u & 3][3]) :: "memory");
        else asm volatile("s_waitcnt vmcnt(10) lgkmcnt(0)" : "+v"(af[u & 3][0]), "+v"(af[u & 3][1]), "+v"(af[u & 3][2]), "+v"(af[u & 3][3]) :: "memory");
        W_DMA(u + 4);
        if (u + 3 < 12) H_LD(u + 3);
        SBAR();
        UNIT(u, af[u & 3]);
    }
    __builtin_amdgcn_s_setprio(3);
    asm volatile("s_waitcnt lgkmcnt(0)" ::: "memory");
    W_DMA(16);
#pragma unroll
    for (int nt = 0; nt < 3; ++nt)
#pragma unroll
        for (int r = 0; r < 16; ++r) { acc[0][nt][r] *= g0; acc[1][nt][r] *= g1; }
    STAMP(2, 5);
    flags_wait<1, 6>(p.ws + WS_OF + (rb * 4 + w) * 128, 8, tag, lane);
    STAMP(2, 6);
#pragma unroll
    for (int v = 0; v < 5; ++v) { ld16_dev(af[v][0], af[v][1], asrc + v * 2048, lo16); ld16_dev(af[v][2], af[v][3], asrc + 40 * 1024 + v * 2048, lo16); }
    asm volatile("s_waitcnt vmcnt(16)" : "+v"(af[0][0]), "+v"(af[0][1]), "+v"(af[0][2]), "+v"(af[0][3]) :: "memory"); SBAR(); UNIT(12, af[0]);
    asm volatile("s_waitcnt vmcnt(12)" : "+v"(af[1][0]), "+v"(af[1][1]), "+v"(af[1][2]), "+v"(af[1][3]) :: "memory"); SBAR(); UNIT(13, af[1]);
    asm volatile("s_waitcnt vmcnt(8)" : "+v"(af[2][0]), "+v"(af[2][1]), "+v"(af[2][2]), "+v"(af[2][3]) :: "memory"); SBAR(); UNIT(14, af[2]);
    asm volatile("s_waitcnt vmcnt(4)" : "+v"(af[3][0]), "+v"(af[3][1]), "+v"(af[3][2]), "+v"(af[3][3]) :: "memory"); SBAR(); UNIT(15, af[3]);
    asm volatile("s_waitcnt vmcnt(0)" : "+v"(af[4][0]), "+v"(af[4][1]), "+v"(af[4][2]), "+v"(af[4][3]) :: "memory"); SBAR(); UNIT(16, af[4]);
    STAMP(2, 7);
    WAIT_BAR(0);
    LAS f32x4* slab = (LAS f32x4*)lds;
#define QUAD(q) (f32x4){acc[(q) / 10][((q) % 10) / 4][4 * (((q) % 10) % 4)], acc[(q) / 10][((q) % 10) / 4][4 * (((q) % 10) % 4) + 1], \
                        acc[(q) / 10][((q) % 10) / 4][4 * (((q) % 10) % 4) + 2], acc[(q) / 10][((q) % 10) / 4][4 * (((q) % 10) % 4) + 3]}
#pragma unroll
    for (int q = 0; q < 20; ++q) slab[(((q / 5) * 4 + w) * 5 + q % 5) * 64 + lane] = QUAD(q);
    WAIT_BAR(0);
#pragma unroll
    for (int i = 0; i < 5; ++i) {
        f32x4 s = slab[((w * 4 + 0) * 5 + i) * 64 + lane];
#pragma unroll
        for (int ww = 1; ww < 4; ++ww) s += slab[((w * 4 + ww) * 5 + i) * 64 + lane];
        const int q = w * 5 + i, rt = q / 10, n = 8 * (q % 10) + 4 * h;
        *(u32x2*)(p.out + (size_t)(rb * 64 + rt * 32 + r32) * D_ + cn * 80 + n) = (u32x2){cvtpk(s[0], s[1]), cvtpk(s[2], s[3])};
    }
    STAMP(2, 8);
}

__global__ __launch_bounds__(256, 2) void k_fused(Params p) {
    extern __shared__ __attribute__((aligned(16))) char lds[];
    const int b = blockIdx.x, j = b & 7, i = (b >> 3) & 31;
    const u64 ep = epoch_load(p.ws + WS_EPOCH);
    if (b < 256) {
#define X_APRIO 2
        __builtin_amdgcn_s_setprio(X_APRIO);
        attn_wg(p, 16 * (j >> 2) + (i >> 1), 2 * (j & 3) + (i & 1), ep, lds);
    } else {
        STAMP(2, 0);
        if (i < 16) { __builtin_amdgcn_s_setprio(3); kv_job(p, 8 * j + (i >> 1), i & 1, ep, lds); __builtin_amdgcn_s_setprio(1); }
        const u64 tag = ffn_up_wg(p, 8 * (j >> 2) + (i >> 2), 4 * (j & 3) + (i & 3), ep, lds);
        out_wg(p, 4 * j + (i >> 3), i & 7, tag, lds);
    }
}

extern "C" void kernel_launch(void* const* d_in, const int* in_sizes, int n_in, void* d_out, int out_size, void* d_ws, size_t ws_size,
                              hipStream_t stream) {
    Params p;
    p.x = (const u16*)d_in[0]; p.wq = (const u16*)d_in[1]; p.wk = (const u16*)d_in[2]; p.wv = (const u16*)d_in[3]; p.wo = (const u16*)d_in[4];
    p.wg = (const u16*)d_in[6]; p.wu = (const u16*)d_in[7]; p.wd = (const u16*)d_in[8]; p.gate_w = (const u16*)d_in[9];
    p.out = (u16*)d_out; p.ws = (char*)d_ws;
    (void)hipFuncSetAttribute((const void*)k_fused, hipFuncAttributeMaxDynamicSharedMemorySize, LDS_BYTES);
    k_fused<<<512, 256, LDS_BYTES, stream>>>(p);
}
```

```cpp
#include <hip/hip_runtime.h>
#include <hip/hip_bf16.h>
#include <stdint.h>

typedef __attribute__((ext_vector_type(8))) short bf16x8;
typedef __attribute__((ext_vector_type(4))) short s16x4;
typedef __attribute__((ext_vector_type(16))) float f32x16;
typedef __attribute__((ext_vector_type(4))) float f32x4;
typedef __attribute__((ext_vector_type(2))) float f32x2;
typedef __attribute__((ext_vector_type(4))) unsigned u32x4;
typedef __attribute__((ext_vector_type(2))) unsigned u32x2;
typedef __attribute__((ext_vector_type(2))) __bf16 bf16x2_t;
typedef short v4i16_t __attribute__((ext_vector_type(4)));
typedef unsigned short u16;
typedef unsigned long long u64;
#define LAS __attribute__((address_space(3)))
#define GAS __attribute__((address_space(1)))
#define CAS __attribute__((address_space(4)))
typedef LAS const char* lds_cptr;
typedef LAS char* lds_ptr;
#define DI __device__ __forceinline__

constexpr int D_ = 640, F_ = 1536;
constexpr int KV_TILE_B = 2624;
constexpr int KV_CHUNK_B = 4 * KV_TILE_B;
constexpr int KV_SLOT_B = 12288;
constexpr size_t WS_EPOCH = 0, WS_SEEN = 1024, WS_HF = 8192, WS_KVF = 16384, WS_OF = 32768;
constexpr size_t WS_KV = 64 * 1024;
constexpr size_t WS_G = 240 * 1024;
constexpr size_t WS_H = 256 * 1024;
constexpr size_t WS_O = WS_H + (size_t)64 * 48 * 2048;
constexpr size_t WS_END = WS_O + (size_t)64 * 40 * 1024;
constexpr int LDS_BYTES = 80 * 1024;
constexpr unsigned SPIN_CAP = 1u << 18;

#define MFMA(a, b, c) __builtin_amdgcn_mfma_f32_32x32x16_bf16((a), (b), (c), 0, 0, 0)
#define SBAR() __builtin_amdgcn_sched_barrier(0)
#define WAIT_VM(N) asm volatile("s_waitcnt vmcnt(" #N ")" ::: "memory")
#define WAIT_BAR(N) asm volatile("s_waitcnt vmcnt(" #N ") lgkmcnt(0)\n\ts_barrier" ::: "memory")
#define PACE() do {} while (0)
#define RLX_AGENT __ATOMIC_RELAXED, __HIP_MEMORY_SCOPE_AGENT
#define BC(x) __builtin_bit_cast(bf16x8, x)

DI unsigned cvtpk(float lo, float hi) {
    f32x2 v = {lo, hi};
    return __builtin_bit_cast(unsigned, __builtin_convertvector(v, bf16x2_t));
}
DI void glds16(const void* sbase, unsigned voff, unsigned lds_base) {
    unsigned sv;
    asm volatile("s_mov_b32 %0, m0\n\ts_mov_b32 m0, %3\n\ts_nop 0\n\tglobal_load_lds_dwordx4 %1, %2\n\ts_mov_b32 m0, %0"
                 : "=&s"(sv) : "v"(voff), "s"(sbase), "s"(lds_base) : "memory");
}
DI void glds16v(const void* ptr, unsigned lds_base) {
    unsigned sv;
    asm volatile("s_mov_b32 %0, m0\n\ts_mov_b32 m0, %2\n\ts_nop 0\n\tglobal_load_lds_dwordx4 %1, off\n\ts_mov_b32 m0, %0"
                 : "=&s"(sv) : "v"(ptr), "s"(lds_base) : "memory");
}
DI void glds16_dev(const void* sbase, unsigned voff, unsigned lds_base) {
    unsigned sv;
    asm volatile("s_mov_b32 %0, m0\n\ts_mov_b32 m0, %3\n\ts_nop 0\n\tglobal_load_lds_dwordx4 %1, %2 sc1\n\ts_mov_b32 m0, %0"
                 : "=&s"(sv) : "v"(voff), "s"(sbase), "s"(lds_base) : "memory");
}
DI s16x4 trread(lds_cptr p) { return __builtin_bit_cast(s16x4, __builtin_amdgcn_ds_read_tr16_b64_v4i16((LAS v4i16_t*)p)); }
DI bf16x8 cat8(s16x4 lo, s16x4 hi) { return __builtin_shufflevector(lo, hi, 0, 1, 2, 3, 4, 5, 6, 7); }
DI bf16x8 ldsread16(lds_cptr p) { return *(const LAS bf16x8*)p; }
DI float xhalf_max(float m) {
    auto rr = __builtin_amdgcn_permlane32_swap(__float_as_uint(m), __float_as_uint(m), false, false);
    return __builtin_fmaxf(__uint_as_float(rr[0]), __uint_as_float(rr[1]));
}
DI f32x16 zero16() { f32x16 z; for (int i = 0; i < 16; ++i) z[i] = 0.f; return z; }
DI void st16_dev(__amdgpu_buffer_rsrc_t r, unsigned off, u32x4 v) { __builtin_amdgcn_raw_buffer_store_b128(v, r, off, 0, 16); }
DI void st8_dev(void* p, u32x2 v) { __hip_atomic_store((GAS u64*)p, (u64)v[0] | ((u64)v[1] << 32), RLX_AGENT); }
DI void st4_dev(void* p, float v) { __hip_atomic_store((GAS unsigned*)p, __float_as_uint(v), RLX_AGENT); }
DI u64 ld8_dev(const void* p) { return __hip_atomic_load((const GAS u64*)p, RLX_AGENT); }
DI float ld4_dev(const void* p) { return __uint_as_float(__hip_atomic_load((const GAS unsigned*)p, RLX_AGENT)); }
DI void flag_set(char* ws, size_t off, u64 tag) { __hip_atomic_store((GAS u64*)(ws + off), tag, RLX_AGENT); }
DI u64 epoch_load(const char* ws) { u64 e; asm volatile("s_load_dwordx2 %0, %1, 0x0" : "=s"(e) : "s"(ws) : "memory"); return e; }
DI u64 epoch_tag(u64 e) { u64 r; asm volatile("s_waitcnt lgkmcnt(0)\n\ts_mov_b64 %0, %1" : "=s"(r) : "s"(e) : "memory"); return r * 6364136223846793005ull + 1442695040888963407ull; }
template <int K, int SLEEP> DI void flags_wait(const char* f, int n, u64 tag, int lane) {
    for (unsigned spin = 0; spin < SPIN_CAP; ++spin) {
        bool ok = true;
#pragma unroll
        for (int k = 0; k < K; ++k) { const int i = lane + 64 * k; const u64 v = ld8_dev(f + 8 * (i < n ? i : 0)); ok &= (v == tag); }
        if (__all(ok)) break;
        __builtin_amdgcn_s_sleep(SLEEP);
    }
}

#define STAMP(kid, i) do {} while (0)
struct Params {
    const u16* x; const u16* wq; const u16* wk; const u16* wv; const u16* wo;
    const u16* wg; const u16* wu; const u16* wd; const u16* gate_w;
    u16* out; char* ws;
};

DI void kv_job(const Params& p, int rt, int isv, u64 ep, char* lds) {
    const int tid = threadIdx.x, lane = tid & 63, w = __builtin_amdgcn_readfirstlane(tid >> 6);
    const int r32 = lane & 31, h = lane >> 5;
    const unsigned lds0 = (unsigned)(uintptr_t)lds;
    STAMP(0, 0);
    const unsigned xoff = w * 17408, woff = xoff + 10240;
    const u16* xs = p.x + (size_t)rt * 32 * D_ + w * 160;
#pragma unroll
    for (int i = 0; i < 10; ++i) {
        const int idx = i * 64 + lane, row = idx / 20, ch = (idx % 20) ^ ((row >> 2) & 3);
        glds16(xs, (unsigned)(row * (D_ * 2) + ch * 16), lds0 + xoff + i * 1024);
    }
    const u16* wsrc = (isv ? p.wv : p.wk);
#pragma unroll
    for (int i = 0; i < 6; ++i) glds16(wsrc, (unsigned)(w * 6400 + i * 1024 + lane * 16), lds0 + woff + i * 1024);
    glds16v(lane < 16 ? (const char*)wsrc + w * 6400 + 6144 + lane * 16 : (const char*)p.gate_w + w * 320 + (lane < 36 ? lane - 16 : 0) * 16, lds0 + woff + 6144);
    const u64 tag = epoch_tag(ep);
    WAIT_VM(0);
    STAMP(0, 3);
    const lds_cptr xb0 = (lds_cptr)lds + xoff + r32 * 320 + ((h ^ ((r32 >> 2) & 3)) << 4), xb1 = (lds_cptr)lds + xoff + r32 * 320 + (((2 + h) ^ ((r32 >> 2) & 3)) << 4);
    const lds_cptr wb = (lds_cptr)lds + woff + (8 * h + ((lane & 15) >> 2)) * 40 + ((lane >> 4) & 1) * 32 + (lane & 3) * 8;
    const lds_cptr gb = (lds_cptr)lds + woff + 6400 + h * 16;
    bf16x8 xf[10], wf[10], gf[10];
#pragma unroll
    for (int ks = 0; ks < 10; ++ks) { xf[ks] = ldsread16(((ks & 1) ? xb1 : xb0) + (ks >> 1) * 64); wf[ks] = cat8(trread(wb + ks * 640), trread(wb + ks * 640 + 160)); gf[ks] = ldsread16(gb + ks * 32); }
    SBAR();
    f32x16 acc = zero16();
    if (isv) {
#pragma unroll
        for (int ks = 0; ks < 10; ++ks) acc = MFMA(xf[ks], wf[ks], acc);
    } else {
#pragma unroll
        for (int ks = 0; ks < 10; ++ks) acc = MFMA((r32 == 20) ? gf[ks] : wf[ks], xf[ks], acc);
    }
    LAS f32x4* red = (LAS f32x4*)((lds_ptr)lds + 69632);
    if (w > 0) {
#pragma unroll
        for (int g = 0; g < 4; ++g) red[((w - 1) * 4 + g) * 64 + lane] = (f32x4){acc[4 * g], acc[4 * g + 1], acc[4 * g + 2], acc[4 * g + 3]};
    }
    WAIT_BAR(0);
    if (w == 0) {
#pragma unroll
        for (int ww = 0; ww < 3; ++ww)
#pragma unroll
            for (int g = 0; g < 4; ++g) { const f32x4 t = red[(ww * 4 + g) * 64 + lane]; acc[4 * g] += t[0]; acc[4 * g + 1] += t[1]; acc[4 * g + 2] += t[2]; acc[4 * g + 3] += t[3]; }
    }
    WAIT_BAR(0);
    if (w == 0) {
        const __amdgpu_buffer_rsrc_t rs = __builtin_amdgcn_make_buffer_rsrc(p.ws, 0, (int)WS_END, 0x00020000);
        const unsigned tile = (unsigned)WS_KV + (unsigned)(isv ? rt : ((rt + 63) & 63)) * KV_TILE_B;
        u32x4 lo = {cvtpk(acc[0], acc[1]), cvtpk(acc[2], acc[3]), cvtpk(acc[4], acc[5]), cvtpk(acc[6], acc[7])};
        u32x4 hi = {cvtpk(acc[8], acc[9]), cvtpk(acc[10], acc[11]), cvtpk(acc[12], acc[13]), cvtpk(acc[14], acc[15])};
        if (!isv) {
            st16_dev(rs, tile + lane * 16, lo);
            if (h == 0) st8_dev(p.ws + tile + 1024 + r32 * 8, (u32x2){hi[0], hi[1]});
            else { const float lg = __uint_as_float(cvtpk(acc[8], 0.f) << 16);
                   st4_dev(p.ws + WS_G + (rt * 32 + r32) * 4, __uint_as_float(cvtpk(1.f / (1.f + __expf(-lg)), 0.f) << 16)); }
        } else {
            if (r32 == 20) { lo = (u32x4){0x3f803f80u, 0x3f803f80u, 0x3f803f80u, 0x3f803f80u}; hi = lo; }
            if (r32 <= 20) { st16_dev(rs, tile + 1280 + h * 336 + r32 * 16, lo); st16_dev(rs, tile + 1952 + h * 336 + r32 * 16, hi); }
        }
        STAMP(0, 6);
        WAIT_VM(0);
        STAMP(0, 7);
        if (lane < 16) flag_set(p.ws, WS_KVF + lane * 1024 + (rt * 2 + isv) * 8, tag);
    }
}

#define AE(b, i) "v_exp_f32 v[" #b "+" #i "], v[" #b "+" #i "]\n"
#define AC(pb, j, b, i) "v_cvt_pk_bf16_f32 v[" #pb "+" #j "], v[" #b "+" #i "], v[" #b "+" #i "+1]\n"
#define AG2(MA, MB, X, XI, PX, PJ) MA AE(X, XI) AE(X, XI+1) AE(X, XI+2) AE(X, XI+3) AC(PX, PJ, X, XI) AC(PX, PJ+1, X, XI+2) \
    MB AE(X, XI+4) AE(X, XI+5) AE(X, XI+6) AE(X, XI+7) AC(PX, PJ+2, X, XI+4) AC(PX, PJ+3, X, XI+6)
#define AHALF(X, PX, Y, Q1, Q2, O, PR, BUF, R1, R2, R3, R4) \
    AG2("v_mfma_f32_32x32x16_bf16 v[" #Y ":" #Y "+15], v[" #BUF ":" #BUF "+3], " Q1 ", 0\n" R1, \
        "v_mfma_f32_32x32x16_bf16 v[" #Y ":" #Y "+15], v[" #BUF "+4:" #BUF "+7], " Q2 ", v[" #Y ":" #Y "+15]\n" R2, X, 0, PX, 0) \
    AG2("v_mfma_f32_32x32x16_bf16 " O ", v[" #BUF "+8:" #BUF "+11], v[" #PR ":" #PR "+3], " O "\n" R3, \
        "v_mfma_f32_32x32x16_bf16 " O ", v[" #BUF "+12:" #BUF "+15], v[" #PR "+4:" #PR "+7], " O "\n" R4, X, 8, PX, 4)
#define ARK1(NB, sl, tl) "ds_read_b128 v[" #NB ":" #NB "+3], %[k1a] offset:" #sl "*12288+" #tl "*2624\n"
#define ARK2(NB, sl, tl) "ds_read_b64 v[" #NB "+4:" #NB "+5], %[k2a] offset:" #sl "*12288+" #tl "*2624\n"
#define ARV0(NB, sl, tl) "ds_read_b128 v[" #NB "+8:" #NB "+11], %[va] offset:" #sl "*12288+" #tl "*2624\n"
#define ARV1(NB, sl, tl) "ds_read_b128 v[" #NB "+12:" #NB "+15], %[va] offset:" #sl "*12288+" #tl "*2624+672\n"
#define AH1(PREV, R1, R2, R3, R4) AHALF(128, 160, 144, "%[qb1]", "%[qb2]", "%[ob]", 168, PREV, R1, R2, R3, R4)
#define AH2(CUR, PREV, sl, tl) AHALF(144, 168, 128, "%[qa1]", "%[qa2]", "%[oa]", 160, CUR, ARK1(PREV, sl, tl), ARK2(PREV, sl, tl), ARV0(PREV, sl, tl), ARV1(PREV, sl, tl))
#define AWL "s_waitcnt lgkmcnt(0)\n"
#define ASYNC "s_waitcnt vmcnt(6) lgkmcnt(0)\ns_barrier\n"
#define ADMA0(fs) "s_mul_i32 %[of], %[ck], 10496\n s_add_u32 %[ck], %[ck], 1\n s_and_b32 %[ck], %[ck], 15\n s_add_u32 m0, %[ldst], " #fs "*12288\n" \
                  "v_add_u32 v208, %[of], %[kvo]\n"
#define ADMA1 "global_load_lds_dwordx4 v208, %[img] sc1\n"
#define ADMA2 "global_load_lds_dwordx4 v208, %[img] offset:1024 sc1\n"
#define ADMA3 "global_load_lds_dwordx4 v208, %[img] offset:2048 sc1\n"
#define ACHUNK(sl, nsl) \
    AH1(192, "", "", "", "") AWL AH2(176, 192, sl, 2) \
    AH1(176, "", "", "", "") AWL AH2(192, 176, sl, 3) \
    AH1(192, "", "", "", "") ASYNC AH2(176, 192, nsl, 0) \
    AH1(176, ADMA0(sl), ADMA1, ADMA2, ADMA3) AWL AH2(192, 176, nsl, 1)
#define AMOV(d, s) "v_mov_b32 v" #d ", " s "\n"
#define ATTN_LOOP_ASM \
    "s_setprio 2\n s_mov_b32 %[m0s], m0\n s_mov_b32 %[it], 4\n s_mov_b32 %[ck], 4\n" \
    ARK1(176, 1, 1) ARK2(176, 1, 1) ARV0(176, 1, 1) ARV1(176, 1, 1) \
    "v_mov_b64 v[192:193], %[k0a]\n v_mov_b64 v[194:195], %[k0b]\n" \
    "v_mov_b64 v[196:197], %[k02]\n" AMOV(198, "%[kone]") AMOV(199, "0") AMOV(182, "%[kone]") AMOV(183, "0") \
    "v_mfma_f32_32x32x16_bf16 v[128:143], v[192:195], %[qa1], 0\n" \
    AMOV(200, "0") AMOV(201, "0") AMOV(202, "0") AMOV(203, "0") AMOV(204, "0") AMOV(205, "0") AMOV(206, "0") AMOV(207, "0") \
    AMOV(168, "0") AMOV(169, "0") AMOV(170, "0") AMOV(171, "0") AMOV(172, "0") AMOV(173, "0") AMOV(174, "0") AMOV(175, "0") \
    "v_mfma_f32_32x32x16_bf16 v[128:143], v[196:199], %[qa2], v[128:143]\n" \
    "s_nop 7\n s_nop 7\n" \
    "1:\n" \
    ACHUNK(1, 2) ACHUNK(2, 3) ACHUNK(3, 0) ACHUNK(0, 1) \
    "s_sub_u32 %[it], %[it], 1\n s_cmp_lg_u32 %[it], 0\n s_cbranch_scc1 1b\n" \
    "v_mfma_f32_32x32x16_bf16 %[ob], v[200:203], v[168:171], %[ob]\n" \
    "v_mfma_f32_32x32x16_bf16 %[ob], v[204:207], v[172:175], %[ob]\n" \
    "s_setprio 3\n s_waitcnt vmcnt(0) lgkmcnt(0)\n s_mov_b32 m0, %[m0s]\n s_nop 7\n s_nop 7\n"
#define ATTN_CLOBBERS "v128","v129","v130","v131","v132","v133","v134","v135","v136","v137","v138","v139","v140","v141","v142","v143", \
    "v144","v145","v146","v147","v148","v149","v150","v151","v152","v153","v154","v155","v156","v157","v158","v159", \
    "v160","v161","v162","v163","v164","v165","v166","v167","v168","v169","v170","v171","v172","v173","v174","v175", \
    "v176","v177","v178","v179","v180","v181","v182","v183","v184","v185","v186","v187","v188","v189","v190","v191", \
    "v192","v193","v194","v195","v196","v197","v198","v199","v200","v201","v202","v203","v204","v205","v206","v207","v208"

DI void attn_wg(const Params& p, int rb, int c, u64 ep, char* lds) {
    const int tid = threadIdx.x, lane = tid & 63, w = __builtin_amdgcn_readfirstlane(tid >> 6);
    const int r32 = lane & 31, h = lane >> 5;
    const unsigned lds0 = (unsigned)(uintptr_t)lds;
    const char* kvimg = p.ws + WS_KV;
    STAMP(1, 0);
    constexpr int RB = 32768;
    const unsigned kvoff = (unsigned)(w * 3072 + lane * 16);
#define KV_DMA(chunk) do { const char* s_ = kvimg + (size_t)(chunk) * KV_CHUNK_B; const unsigned d_ = lds0 + RB + (((chunk) + 1) & 3) * KV_SLOT_B + w * 3072; \
        glds16_dev(s_, kvoff, d_); glds16_dev(s_, kvoff + 1024, d_ + 1024); glds16_dev(s_, kvoff + 2048, d_ + 2048); } while (0)
    constexpr int QS_B = 18432;
    unsigned qoff[5]; unsigned qdst[5];
#pragma unroll
    for (int j = 0; j < 5; ++j) {
        int i = w + 4 * j; if (i >= 18) i -= 2;
        if (j < 2) { const int row = 8 * i + (lane >> 3), pos = lane & 7, ch = pos ^ ((row >> 1) & 7); qoff[j] = row * (D_ * 2) + ch * 16; }
        else { const int idx = (i - 8) * 64 + lane, kr = idx / 10, ch = idx % 10; qoff[j] = kr * (D_ * 2) + ch * 16; }
        qdst[j] = lds0 + i * 1024;
    }
    const u16* xsrc = p.x + (size_t)rb * 64 * D_;
    const u16* wsrc = p.wq + c * 80;
#define Q_DMA(kt) do { const unsigned d_ = ((kt) & 3) * QS_B; const u16* xs_ = xsrc + (kt) * 64; const u16* ws_ = wsrc + (size_t)(kt) * 64 * D_; \
        glds16(xs_, qoff[0], d_ + qdst[0]); glds16(xs_, qoff[1], d_ + qdst[1]); glds16(ws_, qoff[2], d_ + qdst[2]); glds16(ws_, qoff[3], d_ + qdst[3]); glds16(ws_, qoff[4], d_ + qdst[4]); } while (0)
    Q_DMA(0); Q_DMA(1); Q_DMA(2); Q_DMA(3);
    const u64 tag = epoch_tag(ep);
    f32x16 qa = zero16(), qb = zero16();
    {
        unsigned xa[4];
#pragma unroll
        for (int ks = 0; ks < 4; ++ks) xa[ks] = r32 * 128 + (((2 * ks + h) ^ ((r32 >> 1) & 7)) << 4);
        const unsigned wa = 8192 + (8 * h + ((lane & 15) >> 2)) * 160 + w * 40 + ((lane >> 4) & 1) * 32 + (lane & 3) * 8;
        bf16x8 wfA[4], x0A[4], x1A[4], wfB[4], x0B[4], x1B[4];
#define Q_RD(S, kt, ks) do { const lds_cptr sb_ = (lds_cptr)lds + ((kt) & 3) * QS_B; wf##S[ks] = cat8(trread(sb_ + wa + (ks) * 2560), trread(sb_ + wa + (ks) * 2560 + 640)); \
            x0##S[ks] = ldsread16(sb_ + xa[ks]); x1##S[ks] = ldsread16(sb_ + xa[ks] + 4096); } while (0)
#define Q_STEP(C, N, kt) do { \
            if ((kt) == 7 && w == 0) { flags_wait<2, 4>(p.ws + WS_KVF + (blockIdx.x & 15) * 1024, 128, tag, lane); STAMP(1, 5); }     \
            if ((kt) < 7) { WAIT_BAR(10); } else if ((kt) == 7) { WAIT_BAR(5); } else if ((kt) == 8) { WAIT_BAR(9); } \
            if ((kt) + 4 < 10) Q_DMA((kt) + 4); \
            if ((kt) == 7) { KV_DMA(0); KV_DMA(1); KV_DMA(2); } \
            _Pragma("unroll") for (int ks = 0; ks < 4; ++ks) { qa = MFMA(wf##C[ks], x0##C[ks], qa); qb = MFMA(wf##C[ks], x1##C[ks], qb); if ((kt) + 1 < 10) Q_RD(N, (kt) + 1, ks); SBAR(); } } while (0)
        WAIT_BAR(15);
        STAMP(3, 0);
        if (tid == 0) flag_set(p.ws, WS_SEEN + blockIdx.x * 8, tag);
#pragma unroll
        for (int ks = 0; ks < 4; ++ks) Q_RD(A, 0, ks);
#pragma unroll
        for (int kt = 0; kt < 10; kt += 2) { Q_STEP(A, B, kt); STAMP(3, 1 + kt); Q_STEP(B, A, kt + 1); }
    }
    STAMP(1, 1);
    constexpr float C2 = 0.22360679774997896f * 1.4426950408889634f;
    u32x4 qa1, qa2, qb1, qb2;
    qa1 = (u32x4){cvtpk(qa[0] * C2, qa[1] * C2), cvtpk(qa[2] * C2, qa[3] * C2), cvtpk(qa[4] * C2, qa[5] * C2), cvtpk(qa[6] * C2, qa[7] * C2)};
    qb1 = (u32x4){cvtpk(qb[0] * C2, qb[1] * C2), cvtpk(qb[2] * C2, qb[3] * C2), cvtpk(qb[4] * C2, qb[5] * C2), cvtpk(qb[6] * C2, qb[7] * C2)};
    qa2 = (u32x4){h ? 0u : cvtpk(qa[8] * C2, qa[9] * C2), h ? 0u : cvtpk(qa[10] * C2, qa[11] * C2), 0u, 0u};
    qb2 = (u32x4){h ? 0u : cvtpk(qb[8] * C2, qb[9] * C2), h ? 0u : cvtpk(qb[10] * C2, qb[11] * C2), 0u, 0u};
    WAIT_BAR(6);
    KV_DMA(3);
    const unsigned k1a = lane * 16, k2a = 1024 + r32 * 8;
    STAMP(1, 6);
    const bf16x8 k01 = ldsread16((lds_cptr)lds + RB + KV_SLOT_B + k1a);
    const u32x2 k02 = *(const LAS u32x2*)((lds_cptr)lds + RB + KV_SLOT_B + k2a);
    const unsigned va = 1280 + h * 336 + (r32 <= 20 ? r32 : (r32 < 28 ? 20 : 16)) * 16;
    const unsigned kone = h ? 0u : 0x3f80u;
    auto kfrag2 = [](u32x2 v, unsigned one) { return __builtin_bit_cast(bf16x8, (u32x4){v[0], v[1], one, 0u}); };
    {
        f32x16 sa = MFMA(k01, BC(qa1), zero16()); sa = MFMA(kfrag2(k02, kone), BC(qa2), sa);
        f32x16 sb = MFMA(k01, BC(qb1), zero16()); sb = MFMA(kfrag2(k02, kone), BC(qb2), sb);
        float ma = sa[0], mb = sb[0];
#pragma unroll
        for (int r = 1; r < 16; ++r) { ma = __builtin_fmaxf(ma, sa[r]); mb = __builtin_fmaxf(mb, sb[r]); }
        ma = xhalf_max(ma); mb = xhalf_max(mb);
        qa2[2] = h ? 0u : (cvtpk(-ma, 0.f) & 0xffffu);
        qb2[2] = h ? 0u : (cvtpk(-mb, 0.f) & 0xffffu);
    }
    f32x16 oa = zero16(), ob = zero16();
    float ga, gb;
    asm volatile("global_load_dword %0, %2, %3 sc1\n\tglobal_load_dword %1, %2, %3 offset:128 sc1" : "=&v"(ga), "=&v"(gb) : "v"(r32 * 4), "s"(p.ws + WS_G + rb * 256) : "memory");
    STAMP(1, 2);
    {
        unsigned t_m0, t_it, t_ck, t_of;
        const u32x4 k0u = __builtin_bit_cast(u32x4, k01); const u32x2 k0a = {k0u[0], k0u[1]}, k0b = {k0u[2], k0u[3]};
        asm volatile(ATTN_LOOP_ASM
            : [oa] "+v"(oa), [ob] "+v"(ob), "+v"(ga), "+v"(gb), [m0s] "=&s"(t_m0), [it] "=&s"(t_it), [ck] "=&s"(t_ck), [of] "=&s"(t_of)
            : [qa1] "v"(qa1), [qa2] "v"(qa2), [qb1] "v"(qb1), [qb2] "v"(qb2), [k1a] "v"(lds0 + RB + k1a), [k2a] "v"(lds0 + RB + k2a), [va] "v"(lds0 + RB + va),
              [kvo] "v"(kvoff), [img] "s"(kvimg), [ldst] "s"(lds0 + RB + w * 3072), [k0a] "v"(k0a), [k0b] "v"(k0b), [k02] "v"(k02), [kone] "v"(kone)
            : ATTN_CLOBBERS, "scc", "memory");
    }
    STAMP(1, 3);
    {
        float chk = 0.f;
#pragma unroll
        for (int r = 0; r < 8; ++r) chk += oa[r] * 0.f + ob[r] * 0.f;
        if (h == 0) { chk += (oa[8] + oa[9] + oa[10] + oa[11] + ob[8] + ob[9] + ob[10] + ob[11]) * 0.f; } else { chk += (oa[8] + ob[8]) * 0.f; }
        if (h == 1 && !(oa[8] > 1e-30f && ob[8] > 1e-30f)) chk = 1.f;
        if (__builtin_expect(__any(!(chk == 0.f)), 0)) {
            __builtin_amdgcn_fence(__ATOMIC_ACQUIRE, "agent");
            qa2[2] = 0u; qb2[2] = 0u;
            oa = zero16(); ob = zero16();
            float ma = -INFINITY, mb = -INFINITY;
            for (int t = 0; t < 64; ++t) {
                const char* tb = kvimg + (size_t)t * KV_TILE_B;
                const char* kb = kvimg + (size_t)((t + 63) & 63) * KV_TILE_B;
                const bf16x8 k1 = *(const bf16x8*)(kb + k1a), k2 = kfrag2(*(const u32x2*)(kb + k2a), kone);
                const bf16x8 v0 = *(const bf16x8*)(tb + va), v1 = *(const bf16x8*)(tb + va + 672);
                f32x16 sa = MFMA(k1, BC(qa1), zero16()); sa = MFMA(k2, BC(qa2), sa);
                f32x16 sb = MFMA(k1, BC(qb1), zero16()); sb = MFMA(k2, BC(qb2), sb);
                float ta = ma, tb_ = mb;
#pragma unroll
                for (int r = 0; r < 16; ++r) { ta = __builtin_fmaxf(ta, sa[r]); tb_ = __builtin_fmaxf(tb_, sb[r]); }
                ta = xhalf_max(ta); tb_ = xhalf_max(tb_);
                const float fa = __builtin_amdgcn_exp2f(ma - ta), fb = __builtin_amdgcn_exp2f(mb - tb_);
                ma = ta; mb = tb_;
#pragma unroll
                for (int r = 0; r < 16; ++r) { oa[r] *= fa; ob[r] *= fb; sa[r] = __builtin_amdgcn_exp2f(sa[r] - ma); sb[r] = __builtin_amdgcn_exp2f(sb[r] - mb); }
                u32x4 pa0, pa1, pb0, pb1;
#pragma unroll
                for (int j = 0; j < 4; ++j) { pa0[j] = cvtpk(sa[2 * j], sa[2 * j + 1]); pa1[j] = cvtpk(sa[8 + 2 * j], sa[9 + 2 * j]); pb0[j] = cvtpk(sb[2 * j], sb[2 * j + 1]); pb1[j] = cvtpk(sb[8 + 2 * j], sb[9 + 2 * j]); }
                oa = MFMA(v0, BC(pa0), oa); oa = MFMA(v1, BC(pa1), oa);
                ob = MFMA(v0, BC(pb0), ob); ob = MFMA(v1, BC(pb1), ob);
            }
        }
    }
    const float la = __shfl(oa[8], r32 + 32), lb = __shfl(ob[8], r32 + 32);
    const float fa = ga * __builtin_amdgcn_rcpf(la), fb = gb * __builtin_amdgcn_rcpf(lb);
    const int head = 4 * c + w;
    const __amdgpu_buffer_rsrc_t rs = __builtin_amdgcn_make_buffer_rsrc(p.ws, 0, (int)WS_END, 0x00020000);
    const unsigned ob0 = (unsigned)WS_O + (unsigned)(rb * 80 + (head >> 3) * 10) * 1024;
    st16_dev(rs, ob0 + (head & 7) * 1024 + lane * 16, (u32x4){cvtpk(oa[0] * fa, oa[1] * fa), cvtpk(oa[2] * fa, oa[3] * fa), cvtpk(oa[4] * fa, oa[5] * fa), cvtpk(oa[6] * fa, oa[7] * fa)});
    st16_dev(rs, ob0 + 40960 + (head & 7) * 1024 + lane * 16, (u32x4){cvtpk(ob[0] * fb, ob[1] * fb), cvtpk(ob[2] * fb, ob[3] * fb), cvtpk(ob[4] * fb, ob[5] * fb), cvtpk(ob[6] * fb, ob[7] * fb)});
    {
        const u32x2 ua = {cvtpk(oa[8] * fa, oa[9] * fa), cvtpk(oa[10] * fa, oa[11] * fa)}, ub = {cvtpk(ob[8] * fb, ob[9] * fb), cvtpk(ob[10] * fb, ob[11] * fb)};
        const unsigned s0 = (unsigned)__shfl((int)ub[0], r32), s1 = (unsigned)__shfl((int)ub[1], r32);
        const u32x2 uv = {h ? s0 : ua[0], h ? s1 : ua[1]};
        st8_dev(p.ws + ob0 + h * 40960 + (8 + ((head >> 2) & 1)) * 1024 + ((head & 1) * 32 + r32) * 16 + ((head >> 1) & 1) * 8, uv);
    }
    WAIT_VM(0);
    if (lane == 0) flag_set(p.ws, WS_OF + (rb * 4 + (head >> 3)) * 128 + (head & 7) * 8, tag);
    STAMP(1, 4);
    if (blockIdx.x == 0 && w == 0) {
        flags_wait<8, 32>(p.ws + WS_SEEN, 512, tag, lane);
        if (lane == 0) flag_set(p.ws, WS_EPOCH, tag);
    }
#undef KV_DMA
#undef Q_DMA
}

DI u64 ffn_up_wg(const Params& p, int rb2, int fs, u64 ep, char* lds) {
    const int tid = threadIdx.x, lane = tid & 63, w = __builtin_amdgcn_readfirstlane(tid >> 6);
    const int r32 = lane & 31, h = lane >> 5;
    const unsigned lds0 = (unsigned)(uintptr_t)lds;
    constexpr int XS_B = 16384, WS_B = 12288, WS_0 = 2 * XS_B;
    const int xrow = 8 * w + (lane >> 3);
    const unsigned xo = xrow * (D_ * 2) + (((lane & 7) ^ ((xrow >> 1) & 7)) << 4);
    unsigned wo_[3];
#pragma unroll
    for (int j = 0; j < 3; ++j) { const int idx = (w + 4 * j) * 64 + lane, r = idx % 384; wo_[j] = (r / 12) * (F_ * 2) + ((r % 12) ^ (idx >= 384 ? 2 : 0)) * 16; }
    const u16* xsrc = p.x + (size_t)rb2 * 128 * D_;
    const u16* gsrc = p.wg + fs * 96;
    const u16* usrc = p.wu + fs * 96;
    const u16* w1src = (w < 2) ? gsrc : usrc;
    const unsigned dw = lds0 + w * 1024;
#define UP_X(kb, m) glds16(xsrc + (kb) * 64 + (m) * 32 * D_, xo, dw + ((kb) & 1) * XS_B + (m) * 4096)
#define UP_W0(s) glds16(gsrc + (size_t)(s) * 32 * F_, wo_[0], dw + WS_0 + ((s) & 3) * WS_B)
#define UP_W1(s) glds16(w1src + (size_t)(s) * 32 * F_, wo_[1], dw + WS_0 + ((s) & 3) * WS_B + 4096)
#define UP_W2(s) glds16(usrc + (size_t)(s) * 32 * F_, wo_[2], dw + WS_0 + ((s) & 3) * WS_B + 8192)
#define UP_XA(kb) do { UP_X(kb, 0); UP_X(kb, 1); UP_X(kb, 2); UP_X(kb, 3); } while (0)
#define UP_WA(s) do { UP_W0(s); UP_W1(s); UP_W2(s); } while (0)
#define X_UPPRIO 1
#define X_UPPRIO0 1
    __builtin_amdgcn_s_setprio(X_UPPRIO0);
    STAMP(2, 1);
    UP_XA(0); UP_WA(0); UP_WA(1); UP_XA(1); UP_WA(2); UP_WA(3);
    __builtin_amdgcn_s_setprio(X_UPPRIO);
    const u64 tag = epoch_tag(ep);
    f32x16 acc[6];
#pragma unroll
    for (int i = 0; i < 6; ++i) acc[i] = zero16();
    const int wr = w >> 1, wc = w & 1, row = 64 * wr + r32, blk = (lane >> 4) & 1;
    lds_cptr xa[4], wa[3];
#pragma unroll
    for (int kk = 0; kk < 4; ++kk) xa[kk] = (lds_cptr)lds + row * 128 + (((2 * kk + h) ^ ((row >> 1) & 7)) << 4);
#pragma unroll
    for (int i = 0; i < 3; ++i) wa[i] = (lds_cptr)lds + WS_0 + blk * 6144 + (8 * h + ((lane & 15) >> 2)) * 192 + ((3 * wc + i) ^ blk) * 32 + (lane & 3) * 8;
    bf16x8 xf0[2], xf1[2], wf0[3], wf1[3];
#define UP_RX(q, rt) ldsread16(xa[(q) & 3] + (((q) >> 2) & 1) * XS_B + (rt) * 4096)
#define UP_RW(q, i) cat8(trread(wa[i] + (((q) >> 1) & 3) * WS_B + ((q) & 1) * 3072), trread(wa[i] + (((q) >> 1) & 3) * WS_B + ((q) & 1) * 3072 + 768))
#define UP_STEP(C, N, q, D0, D1, D2, D3, D4, D5) do { \
        acc[0] = MFMA(wf##C[0], xf##C[0], acc[0]); xf##N[0] = UP_RX(q, 0); D0; SBAR(); PACE(); \
        acc[3] = MFMA(wf##C[0], xf##C[1], acc[3]); wf##N[0] = UP_RW(q, 0); D1; SBAR(); PACE(); \
        acc[1] = MFMA(wf##C[1], xf##C[0], acc[1]); xf##N[1] = UP_RX(q, 1); D2; SBAR(); PACE(); \
        acc[4] = MFMA(wf##C[1], xf##C[1], acc[4]); wf##N[1] = UP_RW(q, 1); D3; SBAR(); PACE(); \
        acc[2] = MFMA(wf##C[2], xf##C[0], acc[2]); wf##N[2] = UP_RW(q, 2); D4; SBAR(); PACE(); \
        acc[5] = MFMA(wf##C[2], xf##C[1], acc[5]); D5; SBAR(); PACE(); } while (0)
#define NOP_ do {} while (0)
    WAIT_BAR(13);
    STAMP(2, 9);
    if (tid == 0) flag_set(p.ws, WS_SEEN + blockIdx.x * 8, tag);
    xf0[0] = UP_RX(0, 0); xf0[1] = UP_RX(0, 1);
#pragma unroll
    for (int i = 0; i < 3; ++i) wf0[i] = UP_RW(0, i);
    for (int it = 0; it < 4; ++it) {
        const int s0 = 4 * it, kb = 2 * it;
        UP_STEP(0, 1, 1, NOP_, NOP_, NOP_, NOP_, NOP_, NOP_);
        WAIT_BAR(10);
        UP_STEP(1, 0, 2, UP_W0(s0 + 4), UP_W1(s0 + 4), UP_W2(s0 + 4), NOP_, NOP_, NOP_);
        UP_STEP(0, 1, 3, NOP_, NOP_, NOP_, NOP_, NOP_, NOP_);
        WAIT_BAR(6);
        UP_STEP(1, 0, 4, UP_X(kb + 2, 0), UP_X(kb + 2, 1), UP_X(kb + 2, 2), UP_X(kb + 2, 3), UP_W0(s0 + 5), UP_W1(s0 + 5); UP_W2(s0 + 5));
        UP_STEP(0, 1, 5, NOP_, NOP_, NOP_, NOP_, NOP_, NOP_);
        WAIT_BAR(10);
        UP_STEP(1, 0, 6, UP_W0(s0 + 6), UP_W1(s0 + 6), UP_W2(s0 + 6), NOP_, NOP_, NOP_);
        UP_STEP(0, 1, 7, NOP_, NOP_, NOP_, NOP_, NOP_, NOP_);
        WAIT_BAR(6);
        UP_STEP(1, 0, 0, UP_X(kb + 3, 0), UP_X(kb + 3, 1), UP_X(kb + 3, 2), UP_X(kb + 3, 3), UP_W0(s0 + 7), UP_W1(s0 + 7); UP_W2(s0 + 7));
        STAMP(2, 10 + it);
    }
    UP_STEP(0, 1, 1, NOP_, NOP_, NOP_, NOP_, NOP_, NOP_);
    WAIT_BAR(0);
    UP_STEP(1, 0, 2, NOP_, NOP_, NOP_, NOP_, NOP_, NOP_);
    UP_STEP(0, 1, 3, NOP_, NOP_, NOP_, NOP_, NOP_, NOP_);
    UP_STEP(1, 0, 4, NOP_, NOP_, NOP_, NOP_, NOP_, NOP_);
    UP_STEP(0, 1, 5, NOP_, NOP_, NOP_, NOP_, NOP_, NOP_);
    UP_STEP(1, 0, 6, NOP_, NOP_, NOP_, NOP_, NOP_, NOP_);
    UP_STEP(0, 1, 7, NOP_, NOP_, NOP_, NOP_, NOP_, NOP_);
#pragma unroll
    for (int i = 0; i < 6; ++i) acc[i] = MFMA(wf1[i % 3], xf1[i / 3], acc[i]);
    __builtin_amdgcn_s_setprio(3);
    STAMP(2, 2);
    const __amdgpu_buffer_rsrc_t rs = __builtin_amdgcn_make_buffer_rsrc(p.ws, 0, (int)WS_END, 0x00020000);
    const unsigned ho = (unsigned)WS_H + (unsigned)((rb2 * 4 + 2 * wr) * 96 + fs * 6 + 3 * wc) * 1024 + lane * 16;
#pragma unroll
    for (int j = 0; j < 6; ++j) {
        float hv[8];
#pragma unroll
        for (int r = 0; r < 8; ++r) hv[r] = __builtin_fmaxf(acc[j][r], 0.f) * acc[j][8 + r];
        st16_dev(rs, ho + (j / 3) * 96 * 1024 + (j % 3) * 1024, (u32x4){cvtpk(hv[0], hv[1]), cvtpk(hv[2], hv[3]), cvtpk(hv[4], hv[5]), cvtpk(hv[6], hv[7])});
    }
    WAIT_BAR(0);
    if (tid == 0) flag_set(p.ws, WS_HF + (rb2 * 4 + (fs >> 2)) * 128 + (fs & 3) * 8, tag);
    STAMP(2, 3);
    return tag;
}

#define MFMA16(a, b, c) __builtin_amdgcn_mfma_f32_16x16x32_bf16((a), (b), (c), 0, 0, 0)
DI void ld16_dev(bf16x8& d0, bf16x8& d1, const char* sbase, unsigned voff) {
    asm volatile("global_load_dwordx4 %0, %2, %3 sc1\n\tglobal_load_dwordx4 %1, %2, %3 offset:256 sc1" : "=&v"(d0), "=&v"(d1) : "v"(voff), "s"(sbase) : "memory");
}
DI void out_wg(const Params& p, int rb, int cn, u64 tag, char* lds) {
    const int tid = threadIdx.x, lane = tid & 63, w = __builtin_amdgcn_readfirstlane(tid >> 6);
    const int r16 = lane & 15, g = lane >> 4;
    const unsigned lds0 = (unsigned)(uintptr_t)lds + w * 20480;
    unsigned wo_[5], wm_[5], wl_[5];
#pragma unroll
    for (int j = 0; j < 5; ++j) {
        const int idx = j * 64 + lane, r = idx / 10, ch = (idx % 10) * 16;
        wo_[j] = r * (D_ * 2) + ch; wm_[j] = ((r >> 4) * 20 + (r & 15)) * (D_ * 2) + ch; wl_[j] = ((r >> 2) * 20 + 16 + (r & 3)) * (D_ * 2) + ch;
    }
    const u16* dsrc = p.wd + (size_t)w * 384 * D_ + cn * 80;
    const u16* osrc = p.wo + (size_t)w * 160 * D_ + cn * 80;
    const char* hsrc = p.ws + WS_H + ((size_t)(rb * 2) * 48 + w * 12) * 2048;
    const char* asrc = p.ws + WS_O + ((size_t)(rb * 2) * 40 + w * 10) * 1024;
    const unsigned lo16 = (g >> 1) * 1024 + (g & 1) * 512 + r16 * 16;
#define W_PIECES(s_, o_, d_) do { glds16(s_, o_[0], d_); glds16(s_, o_[1], d_ + 1024); glds16(s_, o_[2], d_ + 2048); glds16(s_, o_[3], d_ + 3072); glds16(s_, o_[4], d_ + 4096); } while (0)
#define W_DMA(u) do { const unsigned d_ = lds0 + ((u) & 3) * 5120; \
        if ((u) < 12) W_PIECES(dsrc + (size_t)(u) * 32 * D_, wo_, d_); else if ((u) < 16) W_PIECES(osrc + (size_t)((u) - 12) * 40 * D_, wm_, d_); else W_PIECES(osrc, wl_, d_); } while (0)
    W_DMA(0); W_DMA(1); W_DMA(2); W_DMA(3);
    f32x4 acc[4][5];
#pragma unroll
    for (int i = 0; i < 4; ++i)
#pragma unroll
        for (int j = 0; j < 5; ++j) acc[i][j] = (f32x4){0.f, 0.f, 0.f, 0.f};
    const lds_cptr wbase = (lds_cptr)lds + w * 20480 + (16 * (g >> 1) + 4 * (g & 1) + (r16 >> 2)) * 160 + (lane & 3) * 8;
    bf16x8 wf[2][5];
    bf16x8 af[5][4];
#define W_RD(u, i) wf[(u) & 1][i] = cat8(trread(wbase + ((u) & 3) * 5120 + (i) * 32), trread(wbase + ((u) & 3) * 5120 + (i) * 32 + 1280))
#define H_LD(u) do { ld16_dev(af[(u) & 3][0], af[(u) & 3][1], hsrc + (u) * 2048, lo16); ld16_dev(af[(u) & 3][2], af[(u) & 3][3], hsrc + 48 * 2048 + (u) * 2048, lo16); } while (0)
#define UNIT(u, A) do { _Pragma("unroll") for (int i = 0; i < 5; ++i) { \
        _Pragma("unroll") for (int rg = 0; rg < 4; ++rg) acc[rg][i] = MFMA16(wf[(u) & 1][i], A[rg], acc[rg][i]); \
        if ((u) + 1 < 17) W_RD((u) + 1, i); SBAR(); } } while (0)
#define PINA(A) "+v"(A[0]), "+v"(A[1]), "+v"(A[2]), "+v"(A[3])
    flags_wait<1, 32>(p.ws + WS_KVF + (blockIdx.x & 15) * 1024 + (rb * 4) * 8, 4, tag, lane);
    flags_wait<1, 16>(p.ws + WS_HF + ((rb >> 1) * 4 + w) * 128, 4, tag, lane);
    STAMP(2, 4);
    float gm[4];
#pragma unroll
    for (int rg = 0; rg < 4; ++rg) gm[rg] = __uint_as_float(cvtpk(1.f - ld4_dev(p.ws + WS_G + (rb * 64 + rg * 16 + r16) * 4), 0.f) << 16);
    asm volatile("s_waitcnt vmcnt(0)" :: "v"(gm[0]), "v"(gm[1]), "v"(gm[2]), "v"(gm[3]) : "memory");
    H_LD(0); H_LD(1); H_LD(2);
#pragma unroll
    for (int i = 0; i < 5; ++i) W_RD(0, i);
    __builtin_amdgcn_s_setprio(1);
#pragma unroll
    for (int u = 0; u < 12; ++u) {
        if (u == 0) asm volatile("s_waitcnt vmcnt(8) lgkmcnt(0)" : PINA(af[0]) :: "memory");
        else if (u == 1) asm volatile("s_waitcnt vmcnt(13) lgkmcnt(0)" : PINA(af[1]) :: "memory");
        else if (u <= 9) asm volatile("s_waitcnt vmcnt(18) lgkmcnt(0)" : PINA(af[u & 3]) :: "memory");
        else if (u == 10) asm volatile("s_waitcnt vmcnt(14) lgkmcnt(0)" : PINA(af[u & 3]) :: "memory");
        else asm volatile("s_waitcnt vmcnt(10) lgkmcnt(0)" : PINA(af[u & 3]) :: "memory");
        W_DMA(u + 4);
        if (u + 3 < 12) H_LD(u + 3);
        SBAR();
        UNIT(u, af[u & 3]);
    }
    __builtin_amdgcn_s_setprio(3);
    asm volatile("s_waitcnt lgkmcnt(0)" ::: "memory");
    W_DMA(16);
#pragma unroll
    for (int rg = 0; rg < 4; ++rg)
#pragma unroll
        for (int i = 0; i < 5; ++i) acc[rg][i] *= gm[rg];
    STAMP(2, 5);
    flags_wait<1, 6>(p.ws + WS_OF + (rb * 4 + w) * 128, 8, tag, lane);
    STAMP(2, 6);
#pragma unroll
    for (int v = 0; v < 5; ++v) { ld16_dev(af[v][0], af[v][1], asrc + v * 2048, lo16); ld16_dev(af[v][2], af[v][3], asrc + 40 * 1024 + v * 2048, lo16); }
    asm volatile("s_waitcnt vmcnt(16)" : PINA(af[0]) :: "memory"); SBAR(); UNIT(12, af[0]);
    asm volatile("s_waitcnt vmcnt(12)" : PINA(af[1]) :: "memory"); SBAR(); UNIT(13, af[1]);
    asm volatile("s_waitcnt vmcnt(8)" : PINA(af[2]) :: "memory"); SBAR(); UNIT(14, af[2]);
    asm volatile("s_waitcnt vmcnt(4)" : PINA(af[3]) :: "memory"); SBAR(); UNIT(15, af[3]);
    asm volatile("s_waitcnt vmcnt(0)" : PINA(af[4]) :: "memory"); SBAR(); UNIT(16, af[4]);
    STAMP(2, 7);
    LAS f32x4* mine = (LAS f32x4*)((lds_ptr)lds + w * 20480);
#pragma unroll
    for (int rg = 0; rg < 4; ++rg)
#pragma unroll
        for (int i = 0; i < 5; ++i) mine[(rg * 5 + i) * 64 + lane] = acc[rg][i];
    WAIT_BAR(0);
    const LAS f32x4* all = (const LAS f32x4*)lds + w * 5 * 64 + lane;
    u16* orow = p.out + (size_t)(rb * 64 + w * 16 + r16) * D_ + cn * 80 + 4 * g;
#pragma unroll
    for (int i = 0; i < 5; ++i) {
        const f32x4 s = (all[i * 64] + all[1280 + i * 64]) + (all[2560 + i * 64] + all[3840 + i * 64]);
        *(u32x2*)(orow + 16 * i) = (u32x2){cvtpk(s[0], s[1]), cvtpk(s[2], s[3])};
    }
    STAMP(2, 8);
}

__global__ __launch_bounds__(256, 2) void k_fused(Params p) {
    extern __shared__ __attribute__((aligned(16))) char lds[];
    const int b = blockIdx.x, j = b & 7, i = (b >> 3) & 31;
    const u64 ep = epoch_load(p.ws + WS_EPOCH);
    if (b < 256) {
#define X_APRIO 2
        __builtin_amdgcn_s_setprio(X_APRIO);
        attn_wg(p, 16 * (j >> 2) + (i >> 1), 2 * (j & 3) + (i & 1), ep, lds);
    } else {
        STAMP(2, 0);
        if (i < 16) { __builtin_amdgcn_s_setprio(3); kv_job(p, 8 * j + (i >> 1), i & 1, ep, lds); __builtin_amdgcn_s_setprio(1); }
        const u64 tag = ffn_up_wg(p, 8 * (j >> 2) + (i >> 2), 4 * (j & 3) + (i & 3), ep, lds);
        out_wg(p, 4 * j + (i >> 3), i & 7, tag, lds);
    }
}

extern "C" void kernel_launch(void* const* d_in, const int* in_sizes, int n_in, void* d_out, int out_size, void* d_ws, size_t ws_size,
                              hipStream_t stream) {
    Params p;
    p.x = (const u16*)d_in[0]; p.wq = (const u16*)d_in[1]; p.wk = (const u16*)d_in[2]; p.wv = (const u16*)d_in[3]; p.wo = (const u16*)d_in[4];
    p.wg = (const u16*)d_in[6]; p.wu = (const u16*)d_in[7]; p.wd = (const u16*)d_in[8]; p.gate_w = (const u16*)d_in[9];
    p.out = (u16*)d_out; p.ws = (char*)d_ws;
    (void)hipFuncSetAttribute((const void*)k_fused, hipFuncAttributeMaxDynamicSharedMemorySize, LDS_BYTES);
    k_fused<<<512, 256, LDS_BYTES, stream>>>(p);
}
```

```cpp
#include <hip/hip_runtime.h>
#include <hip/hip_bf16.h>
#include <stdint.h>

typedef __attribute__((ext_vector_type(8))) short bf16x8;
typedef __attribute__((ext_vector_type(4))) short s16x4;
typedef __attribute__((ext_vector_type(16))) float f32x16;
typedef __attribute__((ext_vector_type(4))) float f32x4;
typedef __attribute__((ext_vector_type(2))) float f32x2;
typedef __attribute__((ext_vector_type(4))) unsigned u32x4;
typedef __attribute__((ext_vector_type(2))) unsigned u32x2;
typedef __attribute__((ext_vector_type(2))) __bf16 bf16x2_t;
typedef short v4i16_t __attribute__((ext_vector_type(4)));
typedef unsigned short u16;
typedef unsigned long long u64;
#define LAS __attribute__((address_space(3)))
#define GAS __attribute__((address_space(1)))
#define CAS __attribute__((address_space(4)))
typedef LAS const char* lds_cptr;
typedef LAS char* lds_ptr;
#define DI __device__ __forceinline__

constexpr int D_ = 640, F_ = 1536;
constexpr int KV_TILE_B = 2624;
constexpr int KV_CHUNK_B = 4 * KV_TILE_B;
constexpr int KV_SLOT_B = 12288;
constexpr size_t WS_EPOCH = 0, WS_SEEN = 1024, WS_HF = 8192, WS_KVF = 16384, WS_OF = 32768;
constexpr size_t WS_KV = 64 * 1024;
constexpr size_t WS_G = 240 * 1024;
constexpr size_t WS_H = 256 * 1024;
constexpr size_t WS_O = WS_H + (size_t)64 * 48 * 2048;
constexpr size_t WS_END = WS_O + (size_t)64 * 40 * 1024;
constexpr int LDS_BYTES = 80 * 1024;
constexpr unsigned SPIN_CAP = 1u << 18;

#define MFMA(a, b, c) __builtin_amdgcn_mfma_f32_32x32x16_bf16((a), (b), (c), 0, 0, 0)
#define SBAR() __builtin_amdgcn_sched_barrier(0)
#define WAIT_VM(N) asm volatile("s_waitcnt vmcnt(" #N ")" ::: "memory")
#define WAIT_BAR(N) asm volatile("s_waitcnt vmcnt(" #N ") lgkmcnt(0)\n\ts_barrier" ::: "memory")
#define PACE() do {} while (0)
#define RLX_AGENT __ATOMIC_RELAXED, __HIP_MEMORY_SCOPE_AGENT
#define BC(x) __builtin_bit_cast(bf16x8, x)

DI unsigned cvtpk(float lo, float hi) {
    f32x2 v = {lo, hi};
    return __builtin_bit_cast(unsigned, __builtin_convertvector(v, bf16x2_t));
}
DI void glds16(const void* sbase, unsigned voff, unsigned lds_base) {
    unsigned sv;
    asm volatile("s_mov_b32 %0, m0\n\ts_mov_b32 m0, %3\n\ts_nop 0\n\tglobal_load_lds_dwordx4 %1, %2\n\ts_mov_b32 m0, %0"
                 : "=&s"(sv) : "v"(voff), "s"(sbase), "s"(lds_base) : "memory");
}
DI void glds16v(const void* ptr, unsigned lds_base) {
    unsigned sv;
    asm volatile("s_mov_b32 %0, m0\n\ts_mov_b32 m0, %2\n\ts_nop 0\n\tglobal_load_lds_dwordx4 %1, off\n\ts_mov_b32 m0, %0"
                 : "=&s"(sv) : "v"(ptr), "s"(lds_base) : "memory");
}
DI void glds16_dev(const void* sbase, unsigned voff, unsigned lds_base) {
    unsigned sv;
    asm volatile("s_mov_b32 %0, m0\n\ts_mov_b32 m0, %3\n\ts_nop 0\n\tglobal_load_lds_dwordx4 %1, %2 sc1\n\ts_mov_b32 m0, %0"
                 : "=&s"(sv) : "v"(voff), "s"(sbase), "s"(lds_base) : "memory");
}
DI s16x4 trread(lds_cptr p) { return __builtin_bit_cast(s16x4, __builtin_amdgcn_ds_read_tr16_b64_v4i16((LAS v4i16_t*)p)); }
DI bf16x8 cat8(s16x4 lo, s16x4 hi) { return __builtin_shufflevector(lo, hi, 0, 1, 2, 3, 4, 5, 6, 7); }
DI bf16x8 ldsread16(lds_cptr p) { return *(const LAS bf16x8*)p; }
DI float xhalf_max(float m) {
    auto rr = __builtin_amdgcn_permlane32_swap(__float_as_uint(m), __float_as_uint(m), false, false);
    return __builtin_fmaxf(__uint_as_float(rr[0]), __uint_as_float(rr[1]));
}
DI f32x16 zero16() { f32x16 z; for (int i = 0; i < 16; ++i) z[i] = 0.f; return z; }
DI void st16_dev(__amdgpu_buffer_rsrc_t r, unsigned off, u32x4 v) { __builtin_amdgcn_raw_buffer_store_b128(v, r, off, 0, 16); }
DI void st8_dev(void* p, u32x2 v) { __hip_atomic_store((GAS u64*)p, (u64)v[0] | ((u64)v[1] << 32), RLX_AGENT); }
DI void st4_dev(void* p, float v) { __hip_atomic_store((GAS unsigned*)p, __float_as_uint(v), RLX_AGENT); }
DI u64 ld8_dev(const void* p) { return __hip_atomic_load((const GAS u64*)p, RLX_AGENT); }
DI float ld4_dev(const void* p) { return __uint_as_float(__hip_atomic_load((const GAS unsigned*)p, RLX_AGENT)); }
DI void flag_set(char* ws, size_t off, u64 tag) { __hip_atomic_store((GAS u64*)(ws + off), tag, RLX_AGENT); }
DI u64 epoch_load(const char* ws) { u64 e; asm volatile("s_load_dwordx2 %0, %1, 0x0" : "=s"(e) : "s"(ws) : "memory"); return e; }
DI u64 epoch_tag(u64 e) { u64 r; asm volatile("s_waitcnt lgkmcnt(0)\n\ts_mov_b64 %0, %1" : "=s"(r) : "s"(e) : "memory"); return r * 6364136223846793005ull + 1442695040888963407ull; }
template <int K, int SLEEP> DI void flags_wait(const char* f, int n, u64 tag, int lane) {
    for (unsigned spin = 0; spin < SPIN_CAP; ++spin) {
        bool ok = true;
#pragma unroll
        for (int k = 0; k < K; ++k) { const int i = lane + 64 * k; const u64 v = ld8_dev(f + 8 * (i < n ? i : 0)); ok &= (v == tag); }
        if (__all(ok)) break;
        __builtin_amdgcn_s_sleep(SLEEP);
    }
}

#define STAMP(kid, i) do {} while (0)
struct Params {
    const u16* x; const u16* wq; const u16* wk; const u16* wv; const u16* wo;
    const u16* wg; const u16* wu; const u16* wd; const u16* gate_w;
    u16* out; char* ws;
};

DI void kv_job(const Params& p, int rt, int isv, u64 ep, char* lds) {
    const int tid = threadIdx.x, lane = tid & 63, w = __builtin_amdgcn_readfirstlane(tid >> 6);
    const int r32 = lane & 31, h = lane >> 5;
    const unsigned lds0 = (unsigned)(uintptr_t)lds;
    STAMP(0, 0);
    const unsigned xoff = w * 17408, woff = xoff + 10240;
    const u16* xs = p.x + (size_t)rt * 32 * D_ + w * 160;
#pragma unroll
    for (int i = 0; i < 10; ++i) {
        const int idx = i * 64 + lane, row = idx / 20, ch = (idx % 20) ^ ((row >> 2) & 3);
        glds16(xs, (unsigned)(row * (D_ * 2) + ch * 16), lds0 + xoff + i * 1024);
    }
    const u16* wsrc = (isv ? p.wv : p.wk);
#pragma unroll
    for (int i = 0; i < 6; ++i) glds16(wsrc, (unsigned)(w * 6400 + i * 1024 + lane * 16), lds0 + woff + i * 1024);
    glds16v(lane < 16 ? (const char*)wsrc + w * 6400 + 6144 + lane * 16 : (const char*)p.gate_w + w * 320 + (lane < 36 ? lane - 16 : 0) * 16, lds0 + woff + 6144);
    const u64 tag = epoch_tag(ep);
    WAIT_VM(0);
    STAMP(0, 3);
    const lds_cptr xb0 = (lds_cptr)lds + xoff + r32 * 320 + ((h ^ ((r32 >> 2) & 3)) << 4), xb1 = (lds_cptr)lds + xoff + r32 * 320 + (((2 + h) ^ ((r32 >> 2) & 3)) << 4);
    const lds_cptr wb = (lds_cptr)lds + woff + (8 * h + ((lane & 15) >> 2)) * 40 + ((lane >> 4) & 1) * 32 + (lane & 3) * 8;
    const lds_cptr gb = (lds_cptr)lds + woff + 6400 + h * 16;
    bf16x8 xf[10], wf[10], gf[10];
#pragma unroll
    for (int ks = 0; ks < 10; ++ks) { xf[ks] = ldsread16(((ks & 1) ? xb1 : xb0) + (ks >> 1) * 64); wf[ks] = cat8(trread(wb + ks * 640), trread(wb + ks * 640 + 160)); gf[ks] = ldsread16(gb + ks * 32); }
    SBAR();
    f32x16 acc = zero16();
    if (isv) {
#pragma unroll
        for (int ks = 0; ks < 10; ++ks) acc = MFMA(xf[ks], wf[ks], acc);
    } else {
#pragma unroll
        for (int ks = 0; ks < 10; ++ks) acc = MFMA((r32 == 20) ? gf[ks] : wf[ks], xf[ks], acc);
    }
    LAS f32x4* red = (LAS f32x4*)((lds_ptr)lds + 69632);
    if (w > 0) {
#pragma unroll
        for (int g = 0; g < 4; ++g) red[((w - 1) * 4 + g) * 64 + lane] = (f32x4){acc[4 * g], acc[4 * g + 1], acc[4 * g + 2], acc[4 * g + 3]};
    }
    WAIT_BAR(0);
    if (w == 0) {
#pragma unroll
        for (int ww = 0; ww < 3; ++ww)
#pragma unroll
            for (int g = 0; g < 4; ++g) { const f32x4 t = red[(ww * 4 + g) * 64 + lane]; acc[4 * g] += t[0]; acc[4 * g + 1] += t[1]; acc[4 * g + 2] += t[2]; acc[4 * g + 3] += t[3]; }
    }
    WAIT_BAR(0);
    if (w == 0) {
        const __amdgpu_buffer_rsrc_t rs = __builtin_amdgcn_make_buffer_rsrc(p.ws, 0, (int)WS_END, 0x00020000);
        const unsigned tile = (unsigned)WS_KV + (unsigned)(isv ? rt : ((rt + 63) & 63)) * KV_TILE_B;
        u32x4 lo = {cvtpk(acc[0], acc[1]), cvtpk(acc[2], acc[3]), cvtpk(acc[4], acc[5]), cvtpk(acc[6], acc[7])};
        u32x4 hi = {cvtpk(acc[8], acc[9]), cvtpk(acc[10], acc[11]), cvtpk(acc[12], acc[13]), cvtpk(acc[14], acc[15])};
        if (!isv) {
            st16_dev(rs, tile + lane * 16, lo);
            if (h == 0) st8_dev(p.ws + tile + 1024 + r32 * 8, (u32x2){hi[0], hi[1]});
            else { const float lg = __uint_as_float(cvtpk(acc[8], 0.f) << 16);
                   st4_dev(p.ws + WS_G + (rt * 32 + r32) * 4, __uint_as_float(cvtpk(1.f / (1.f + __expf(-lg)), 0.f) << 16)); }
        } else {
            if (r32 == 20) { lo = (u32x4){0x3f803f80u, 0x3f803f80u, 0x3f803f80u, 0x3f803f80u}; hi = lo; }
            if (r32 <= 20) { st16_dev(rs, tile + 1280 + h * 336 + r32 * 16, lo); st16_dev(rs, tile + 1952 + h * 336 + r32 * 16, hi); }
        }
        STAMP(0, 6);
        WAIT_VM(0);
        STAMP(0, 7);
        if (lane < 16) flag_set(p.ws, WS_KVF + lane * 1024 + (rt * 2 + isv) * 8, tag);
    }
}

#define AE(b, i) "v_exp_f32 v[" #b "+" #i "], v[" #b "+" #i "]\n"
#define AC(pb, j, b, i) "v_cvt_pk_bf16_f32 v[" #pb "+" #j "], v[" #b "+" #i "], v[" #b "+" #i "+1]\n"
#define AG2(MA, MB, X, XI, PX, PJ) MA AE(X, XI) AE(X, XI+1) AE(X, XI+2) AE(X, XI+3) AC(PX, PJ, X, XI) AC(PX, PJ+1, X, XI+2) \
    MB AE(X, XI+4) AE(X, XI+5) AE(X, XI+6) AE(X, XI+7) AC(PX, PJ+2, X, XI+4) AC(PX, PJ+3, X, XI+6)
#define AHALF(X, PX, Y, Q1, Q2, O, PR, BUF, R1, R2, R3, R4) \
    AG2("v_mfma_f32_32x32x16_bf16 v[" #Y ":" #Y "+15], v[" #BUF ":" #BUF "+3], " Q1 ", 0\n" R1, \
        "v_mfma_f32_32x32x16_bf16 v[" #Y ":" #Y "+15], v[" #BUF "+4:" #BUF "+7], " Q2 ", v[" #Y ":" #Y "+15]\n" R2, X, 0, PX, 0) \
    AG2("v_mfma_f32_32x32x16_bf16 " O ", v[" #BUF "+8:" #BUF "+11], v[" #PR ":" #PR "+3], " O "\n" R3, \
        "v_mfma_f32_32x32x16_bf16 " O ", v[" #BUF "+12:" #BUF "+15], v[" #PR "+4:" #PR "+7], " O "\n" R4, X, 8, PX, 4)
#define ARK1(NB, sl, tl) "ds_read_b128 v[" #NB ":" #NB "+3], %[k1a] offset:" #sl "*12288+" #tl "*2624\n"
#define ARK2(NB, sl, tl) "ds_read_b64 v[" #NB "+4:" #NB "+5], %[k2a] offset:" #sl "*12288+" #tl "*2624\n"
#define ARV0(NB, sl, tl) "ds_read_b128 v[" #NB "+8:" #NB "+11], %[va] offset:" #sl "*12288+" #tl "*2624\n"
#define ARV1(NB, sl, tl) "ds_read_b128 v[" #NB "+12:" #NB "+15], %[va] offset:" #sl "*12288+" #tl "*2624+672\n"
#define AH1(PREV, R1, R2, R3, R4) AHALF(128, 160, 144, "%[qb1]", "%[qb2]", "%[ob]", 168, PREV, R1, R2, R3, R4)
#define AH2(CUR, PREV, sl, tl) AHALF(144, 168, 128, "%[qa1]", "%[qa2]", "%[oa]", 160, CUR, ARK1(PREV, sl, tl), ARK2(PREV, sl, tl), ARV0(PREV, sl, tl), ARV1(PREV, sl, tl))
#define AWL "s_waitcnt lgkmcnt(0)\n"
#define ASYNC "s_waitcnt vmcnt(6) lgkmcnt(0)\ns_barrier\n"
#define ADMA0(fs) "s_mul_i32 %[of], %[ck], 10496\n s_add_u32 %[ck], %[ck], 1\n s_and_b32 %[ck], %[ck], 15\n s_add_u32 m0, %[ldst], " #fs "*12288\n" \
                  "v_add_u32 v208, %[of], %[kvo]\n"
#define ADMA1 "global_load_lds_dwordx4 v208, %[img] sc1\n"
#define ADMA2 "global_load_lds_dwordx4 v208, %[img] offset:1024 sc1\n"
#define ADMA3 "global_load_lds_dwordx4 v208, %[img] offset:2048 sc1\n"
#define ACHUNK(sl, nsl) \
    AH1(192, "", "", "", "") AWL AH2(176, 192, sl, 2) \
    AH1(176, "", "", "", "") AWL AH2(192, 176, sl, 3) \
    AH1(192, "", "", "", "") ASYNC AH2(176, 192, nsl, 0) \
    AH1(176, ADMA0(sl), ADMA1, ADMA2, ADMA3) AWL AH2(192, 176, nsl, 1)
#define AMOV(d, s) "v_mov_b32 v" #d ", " s "\n"
#define ATTN_LOOP_ASM \
    "s_setprio 2\n s_mov_b32 %[m0s], m0\n s_mov_b32 %[it], 4\n s_mov_b32 %[ck], 4\n" \
    ARK1(176, 1, 1) ARK2(176, 1, 1) ARV0(176, 1, 1) ARV1(176, 1, 1) \
    "v_mov_b64 v[192:193], %[k0a]\n v_mov_b64 v[194:195], %[k0b]\n" \
    "v_mov_b64 v[196:197], %[k02]\n" AMOV(198, "%[kone]") AMOV(199, "0") AMOV(182, "%[kone]") AMOV(183, "0") \
    "v_mfma_f32_32x32x16_bf16 v[128:143], v[192:195], %[qa1], 0\n" \
    AMOV(200, "0") AMOV(201, "0") AMOV(202, "0") AMOV(203, "0") AMOV(204, "0") AMOV(205, "0") AMOV(206, "0") AMOV(207, "0") \
    AMOV(168, "0") AMOV(169, "0") AMOV(170, "0") AMOV(171, "0") AMOV(172, "0") AMOV(173, "0") AMOV(174, "0") AMOV(175, "0") \
    "v_mfma_f32_32x32x16_bf16 v[128:143], v[196:199], %[qa2], v[128:143]\n" \
    "s_nop 7\n s_nop 7\n" \
    "1:\n" \
    ACHUNK(1, 2) ACHUNK(2, 3) ACHUNK(3, 0) ACHUNK(0, 1) \
    "s_sub_u32 %[it], %[it], 1\n s_cmp_lg_u32 %[it], 0\n s_cbranch_scc1 1b\n" \
    "v_mfma_f32_32x32x16_bf16 %[ob], v[200:203], v[168:171], %[ob]\n" \
    "v_mfma_f32_32x32x16_bf16 %[ob], v[204:207], v[172:175], %[ob]\n" \
    "s_setprio 3\n s_waitcnt vmcnt(9) lgkmcnt(0)\n s_mov_b32 m0, %[m0s]\n s_nop 7\n s_nop 7\n"
#define ATTN_CLOBBERS "v128","v129","v130","v131","v132","v133","v134","v135","v136","v137","v138","v139","v140","v141","v142","v143", \
    "v144","v145","v146","v147","v148","v149","v150","v151","v152","v153","v154","v155","v156","v157","v158","v159", \
    "v160","v161","v162","v163","v164","v165","v166","v167","v168","v169","v170","v171","v172","v173","v174","v175", \
    "v176","v177","v178","v179","v180","v181","v182","v183","v184","v185","v186","v187","v188","v189","v190","v191", \
    "v192","v193","v194","v195","v196","v197","v198","v199","v200","v201","v202","v203","v204","v205","v206","v207","v208"

DI void attn_wg(const Params& p, int rb, int c, u64 ep, char* lds) {
    const int tid = threadIdx.x, lane = tid & 63, w = __builtin_amdgcn_readfirstlane(tid >> 6);
    const int r32 = lane & 31, h = lane >> 5;
    const unsigned lds0 = (unsigned)(uintptr_t)lds;
    const char* kvimg = p.ws + WS_KV;
    STAMP(1, 0);
    constexpr int RB = 32768;
    const unsigned kvoff = (unsigned)(w * 3072 + lane * 16);
#define KV_DMA(chunk) do { const char* s_ = kvimg + (size_t)(chunk) * KV_CHUNK_B; const unsigned d_ = lds0 + RB + (((chunk) + 1) & 3) * KV_SLOT_B + w * 3072; \
        glds16_dev(s_, kvoff, d_); glds16_dev(s_, kvoff + 1024, d_ + 1024); glds16_dev(s_, kvoff + 2048, d_ + 2048); } while (0)
    constexpr int QS_B = 18432;
    unsigned qoff[5]; unsigned qdst[5];
#pragma unroll
    for (int j = 0; j < 5; ++j) {
        int i = w + 4 * j; if (i >= 18) i -= 2;
        if (j < 2) { const int row = 8 * i + (lane >> 3), pos = lane & 7, ch = pos ^ ((row >> 1) & 7); qoff[j] = row * (D_ * 2) + ch * 16; }
        else { const int idx = (i - 8) * 64 + lane, kr = idx / 10, ch = idx % 10; qoff[j] = kr * (D_ * 2) + ch * 16; }
        qdst[j] = lds0 + i * 1024;
    }
    const u16* xsrc = p.x + (size_t)rb * 64 * D_;
    const u16* wsrc = p.wq + c * 80;
#define Q_DMA(kt) do { const unsigned d_ = ((kt) & 3) * QS_B; const u16* xs_ = xsrc + (kt) * 64; const u16* ws_ = wsrc + (size_t)(kt) * 64 * D_; \
        glds16(xs_, qoff[0], d_ + qdst[0]); glds16(xs_, qoff[1], d_ + qdst[1]); glds16(ws_, qoff[2], d_ + qdst[2]); glds16(ws_, qoff[3], d_ + qdst[3]); glds16(ws_, qoff[4], d_ + qdst[4]); } while (0)
#define Q_PIECE(kt, j) glds16((j) < 2 ? xsrc + (kt) * 64 : wsrc + (size_t)(kt) * 64 * D_, qoff[j], ((kt) & 3) * QS_B + qdst[j])
    Q_DMA(0); Q_DMA(1); Q_DMA(2); Q_DMA(3);
    const u64 tag = epoch_tag(ep);
    f32x16 qa = zero16(), qb = zero16();
    {
        unsigned xa[4];
#pragma unroll
        for (int ks = 0; ks < 4; ++ks) xa[ks] = r32 * 128 + (((2 * ks + h) ^ ((r32 >> 1) & 7)) << 4);
        const unsigned wa = 8192 + (8 * h + ((lane & 15) >> 2)) * 160 + w * 40 + ((lane >> 4) & 1) * 32 + (lane & 3) * 8;
        bf16x8 wfA[4], x0A[4], x1A[4], wfB[4], x0B[4], x1B[4];
#define Q_RD(S, kt, ks) do { const lds_cptr sb_ = (lds_cptr)lds + ((kt) & 3) * QS_B; wf##S[ks] = cat8(trread(sb_ + wa + (ks) * 2560), trread(sb_ + wa + (ks) * 2560 + 640)); \
            x0##S[ks] = ldsread16(sb_ + xa[ks]); x1##S[ks] = ldsread16(sb_ + xa[ks] + 4096); } while (0)
#define Q_STEP(C, N, kt) do { \
            if ((kt) == 7 && w == 0) { flags_wait<2, 4>(p.ws + WS_KVF + (blockIdx.x & 15) * 1024, 128, tag, lane); STAMP(1, 5); }     \
            if ((kt) < 7) { WAIT_BAR(10); } else if ((kt) == 7) { WAIT_BAR(5); } else if ((kt) == 8) { WAIT_BAR(9); } \
            if ((kt) == 7) { KV_DMA(0); KV_DMA(1); KV_DMA(2); } \
            _Pragma("unroll") for (int ks = 0; ks < 4; ++ks) { qa = MFMA(wf##C[ks], x0##C[ks], qa); if ((kt) + 1 < 10) Q_RD(N, (kt) + 1, ks); SBAR(); qb = MFMA(wf##C[ks], x1##C[ks], qb); \
                if ((kt) + 4 < 10) { Q_PIECE((kt) + 4, ks); if (ks == 3) Q_PIECE((kt) + 4, 4); } SBAR(); } } while (0)
        WAIT_BAR(15);
        STAMP(3, 0);
        if (tid == 0) flag_set(p.ws, WS_SEEN + blockIdx.x * 8, tag);
#pragma unroll
        for (int ks = 0; ks < 4; ++ks) Q_RD(A, 0, ks);
#pragma unroll
        for (int kt = 0; kt < 10; kt += 2) { Q_STEP(A, B, kt); STAMP(3, 1 + kt); Q_STEP(B, A, kt + 1); }
    }
    STAMP(1, 1);
    constexpr float C2 = 0.22360679774997896f * 1.4426950408889634f;
    u32x4 qa1, qa2, qb1, qb2;
    qa1 = (u32x4){cvtpk(qa[0] * C2, qa[1] * C2), cvtpk(qa[2] * C2, qa[3] * C2), cvtpk(qa[4] * C2, qa[5] * C2), cvtpk(qa[6] * C2, qa[7] * C2)};
    qb1 = (u32x4){cvtpk(qb[0] * C2, qb[1] * C2), cvtpk(qb[2] * C2, qb[3] * C2), cvtpk(qb[4] * C2, qb[5] * C2), cvtpk(qb[6] * C2, qb[7] * C2)};
    qa2 = (u32x4){h ? 0u : cvtpk(qa[8] * C2, qa[9] * C2), h ? 0u : cvtpk(qa[10] * C2, qa[11] * C2), 0u, 0u};
    qb2 = (u32x4){h ? 0u : cvtpk(qb[8] * C2, qb[9] * C2), h ? 0u : cvtpk(qb[10] * C2, qb[11] * C2), 0u, 0u};
    WAIT_BAR(6);
    KV_DMA(3);
    const unsigned k1a = lane * 16, k2a = 1024 + r32 * 8;
    STAMP(1, 6);
    const bf16x8 k01 = ldsread16((lds_cptr)lds + RB + KV_SLOT_B + k1a);
    const u32x2 k02 = *(const LAS u32x2*)((lds_cptr)lds + RB + KV_SLOT_B + k2a);
    const unsigned va = 1280 + h * 336 + (r32 <= 20 ? r32 : (r32 < 28 ? 20 : 16)) * 16;
    const unsigned kone = h ? 0u : 0x3f80u;
    auto kfrag2 = [](u32x2 v, unsigned one) { return __builtin_bit_cast(bf16x8, (u32x4){v[0], v[1], one, 0u}); };
    {
        f32x16 sa = MFMA(k01, BC(qa1), zero16()); sa = MFMA(kfrag2(k02, kone), BC(qa2), sa);
        f32x16 sb = MFMA(k01, BC(qb1), zero16()); sb = MFMA(kfrag2(k02, kone), BC(qb2), sb);
        float ma = sa[0], mb = sb[0];
#pragma unroll
        for (int r = 1; r < 16; ++r) { ma = __builtin_fmaxf(ma, sa[r]); mb = __builtin_fmaxf(mb, sb[r]); }
        ma = xhalf_max(ma); mb = xhalf_max(mb);
        qa2[2] = h ? 0u : (cvtpk(-ma, 0.f) & 0xffffu);
        qb2[2] = h ? 0u : (cvtpk(-mb, 0.f) & 0xffffu);
    }
    f32x16 oa = zero16(), ob = zero16();
    float ga, gb;
    asm volatile("global_load_dword %0, %2, %3 sc1\n\tglobal_load_dword %1, %2, %3 offset:128 sc1" : "=&v"(ga), "=&v"(gb) : "v"(r32 * 4), "s"(p.ws + WS_G + rb * 256) : "memory");
    STAMP(1, 2);
    {
        unsigned t_m0, t_it, t_ck, t_of;
        const u32x4 k0u = __builtin_bit_cast(u32x4, k01); const u32x2 k0a = {k0u[0], k0u[1]}, k0b = {k0u[2], k0u[3]};
        asm volatile(ATTN_LOOP_ASM
            : [oa] "+v"(oa), [ob] "+v"(ob), "+v"(ga), "+v"(gb), [m0s] "=&s"(t_m0), [it] "=&s"(t_it), [ck] "=&s"(t_ck), [of] "=&s"(t_of)
            : [qa1] "v"(qa1), [qa2] "v"(qa2), [qb1] "v"(qb1), [qb2] "v"(qb2), [k1a] "v"(lds0 + RB + k1a), [k2a] "v"(lds0 + RB + k2a), [va] "v"(lds0 + RB + va),
              [kvo] "v"(kvoff), [img] "s"(kvimg), [ldst] "s"(lds0 + RB + w * 3072), [k0a] "v"(k0a), [k0b] "v"(k0b), [k02] "v"(k02), [kone] "v"(kone)
            : ATTN_CLOBBERS, "scc", "memory");
    }
    STAMP(1, 3);
    {
        float chk = 0.f;
#pragma unroll
        for (int r = 0; r < 8; ++r) chk += oa[r] * 0.f + ob[r] * 0.f;
        if (h == 0) { chk += (oa[8] + oa[9] + oa[10] + oa[11] + ob[8] + ob[9] + ob[10] + ob[11]) * 0.f; } else { chk += (oa[8] + ob[8]) * 0.f; }
        if (h == 1 && !(oa[8] > 1e-30f && ob[8] > 1e-30f)) chk = 1.f;
        if (__builtin_expect(__any(!(chk == 0.f)), 0)) {
            __builtin_amdgcn_fence(__ATOMIC_ACQUIRE, "agent");
            qa2[2] = 0u; qb2[2] = 0u;
            oa = zero16(); ob = zero16();
            float ma = -INFINITY, mb = -INFINITY;
            for (int t = 0; t < 64; ++t) {
                const char* tb = kvimg + (size_t)t * KV_TILE_B;
                const char* kb = kvimg + (size_t)((t + 63) & 63) * KV_TILE_B;
                const bf16x8 k1 = *(const bf16x8*)(kb + k1a), k2 = kfrag2(*(const u32x2*)(kb + k2a), kone);
                const bf16x8 v0 = *(const bf16x8*)(tb + va), v1 = *(const bf16x8*)(tb + va + 672);
                f32x16 sa = MFMA(k1, BC(qa1), zero16()); sa = MFMA(k2, BC(qa2), sa);
                f32x16 sb = MFMA(k1, BC(qb1), zero16()); sb = MFMA(k2, BC(qb2), sb);
                float ta = ma, tb_ = mb;
#pragma unroll
                for (int r = 0; r < 16; ++r) { ta = __builtin_fmaxf(ta, sa[r]); tb_ = __builtin_fmaxf(tb_, sb[r]); }
                ta = xhalf_max(ta); tb_ = xhalf_max(tb_);
                const float fa = __builtin_amdgcn_exp2f(ma - ta), fb = __builtin_amdgcn_exp2f(mb - tb_);
                ma = ta; mb = tb_;
#pragma unroll
                for (int r = 0; r < 16; ++r) { oa[r] *= fa; ob[r] *= fb; sa[r] = __builtin_amdgcn_exp2f(sa[r] - ma); sb[r] = __builtin_amdgcn_exp2f(sb[r] - mb); }
                u32x4 pa0, pa1, pb0, pb1;
#pragma unroll
                for (int j = 0; j < 4; ++j) { pa0[j] = cvtpk(sa[2 * j], sa[2 * j + 1]); pa1[j] = cvtpk(sa[8 + 2 * j], sa[9 + 2 * j]); pb0[j] = cvtpk(sb[2 * j], sb[2 * j + 1]); pb1[j] = cvtpk(sb[8 + 2 * j], sb[9 + 2 * j]); }
                oa = MFMA(v0, BC(pa0), oa); oa = MFMA(v1, BC(pa1), oa);
                ob = MFMA(v0, BC(pb0), ob); ob = MFMA(v1, BC(pb1), ob);
            }
        }
    }
    const float la = __shfl(oa[8], r32 + 32), lb = __shfl(ob[8], r32 + 32);
    const float fa = ga * __builtin_amdgcn_rcpf(la), fb = gb * __builtin_amdgcn_rcpf(lb);
    const int head = 4 * c + w;
    const __amdgpu_buffer_rsrc_t rs = __builtin_amdgcn_make_buffer_rsrc(p.ws, 0, (int)WS_END, 0x00020000);
    const unsigned ob0 = (unsigned)WS_O + (unsigned)(rb * 80 + (head >> 3) * 10) * 1024;
    st16_dev(rs, ob0 + (head & 7) * 1024 + lane * 16, (u32x4){cvtpk(oa[0] * fa, oa[1] * fa), cvtpk(oa[2] * fa, oa[3] * fa), cvtpk(oa[4] * fa, oa[5] * fa), cvtpk(oa[6] * fa, oa[7] * fa)});
    st16_dev(rs, ob0 + 40960 + (head & 7) * 1024 + lane * 16, (u32x4){cvtpk(ob[0] * fb, ob[1] * fb), cvtpk(ob[2] * fb, ob[3] * fb), cvtpk(ob[4] * fb, ob[5] * fb), cvtpk(ob[6] * fb, ob[7] * fb)});
    {
        const u32x2 ua = {cvtpk(oa[8] * fa, oa[9] * fa), cvtpk(oa[10] * fa, oa[11] * fa)}, ub = {cvtpk(ob[8] * fb, ob[9] * fb), cvtpk(ob[10] * fb, ob[11] * fb)};
        const unsigned s0 = (unsigned)__shfl((int)ub[0], r32), s1 = (unsigned)__shfl((int)ub[1], r32);
        const u32x2 uv = {h ? s0 : ua[0], h ? s1 : ua[1]};
        st8_dev(p.ws + ob0 + h * 40960 + (8 + ((head >> 2) & 1)) * 1024 + ((head & 1) * 32 + r32) * 16 + ((head >> 1) & 1) * 8, uv);
    }
    WAIT_VM(0);
    if (lane == 0) flag_set(p.ws, WS_OF + (rb * 4 + (head >> 3)) * 128 + (head & 7) * 8, tag);
    STAMP(1, 4);
    if (blockIdx.x == 0 && w == 0) {
        flags_wait<8, 32>(p.ws + WS_SEEN, 512, tag, lane);
        if (lane == 0) flag_set(p.ws, WS_EPOCH, tag);
    }
#undef KV_DMA
#undef Q_DMA
}

DI u64 ffn_up_wg(const Params& p, int rb2, int fs, u64 ep, char* lds) {
    const int tid = threadIdx.x, lane = tid & 63, w = __builtin_amdgcn_readfirstlane(tid >> 6);
    const int r32 = lane & 31, h = lane >> 5;
    const unsigned lds0 = (unsigned)(uintptr_t)lds;
    constexpr int XS_B = 16384, WS_B = 12288, WS_0 = 2 * XS_B;
    const int xrow = 8 * w + (lane >> 3);
    const unsigned xo = xrow * (D_ * 2) + (((lane & 7) ^ ((xrow >> 1) & 7)) << 4);
    unsigned wo_[3];
#pragma unroll
    for (int j = 0; j < 3; ++j) { const int idx = (w + 4 * j) * 64 + lane, r = idx % 384; wo_[j] = (r / 12) * (F_ * 2) + ((r % 12) ^ (idx >= 384 ? 2 : 0)) * 16; }
    const u16* xsrc = p.x + (size_t)rb2 * 128 * D_;
    const u16* gsrc = p.wg + fs * 96;
    const u16* usrc = p.wu + fs * 96;
    const u16* w1src = (w < 2) ? gsrc : usrc;
    const unsigned dw = lds0 + w * 1024;
#define UP_X(kb, m) glds16(xsrc + (kb) * 64 + (m) * 32 * D_, xo, dw + ((kb) & 1) * XS_B + (m) * 4096)
#define UP_W0(s) glds16(gsrc + (size_t)(s) * 32 * F_, wo_[0], dw + WS_0 + ((s) & 3) * WS_B)
#define UP_W1(s) glds16(w1src + (size_t)(s) * 32 * F_, wo_[1], dw + WS_0 + ((s) & 3) * WS_B + 4096)
#define UP_W2(s) glds16(usrc + (size_t)(s) * 32 * F_, wo_[2], dw + WS_0 + ((s) & 3) * WS_B + 8192)
#define UP_XA(kb) do { UP_X(kb, 0); UP_X(kb, 1); UP_X(kb, 2); UP_X(kb, 3); } while (0)
#define UP_WA(s) do { UP_W0(s); UP_W1(s); UP_W2(s); } while (0)
#define X_UPPRIO 1
#define X_UPPRIO0 3
    __builtin_amdgcn_s_setprio(X_UPPRIO0);
    STAMP(2, 1);
    UP_XA(0); UP_WA(0); UP_WA(1); UP_XA(1); UP_WA(2);
    __builtin_amdgcn_s_setprio(X_UPPRIO);
    const u64 tag = epoch_tag(ep);
    f32x16 acc[6];
#pragma unroll
    for (int i = 0; i < 6; ++i) acc[i] = zero16();
    const int wr = w >> 1, wc = w & 1, row = 64 * wr + r32, blk = (lane >> 4) & 1;
    lds_cptr xa[4], wa[3];
#pragma unroll
    for (int kk = 0; kk < 4; ++kk) xa[kk] = (lds_cptr)lds + row * 128 + (((2 * kk + h) ^ ((row >> 1) & 7)) << 4);
#pragma unroll
    for (int i = 0; i < 3; ++i) wa[i] = (lds_cptr)lds + WS_0 + blk * 6144 + (8 * h + ((lane & 15) >> 2)) * 192 + ((3 * wc + i) ^ blk) * 32 + (lane & 3) * 8;
    bf16x8 xf0[2], xf1[2], wf0[3], wf1[3];
#define UP_RX(q, rt) ldsread16(xa[(q) & 3] + (((q) >> 2) & 1) * XS_B + (rt) * 4096)
#define UP_RW(q, i) cat8(trread(wa[i] + (((q) >> 1) & 3) * WS_B + ((q) & 1) * 3072), trread(wa[i] + (((q) >> 1) & 3) * WS_B + ((q) & 1) * 3072 + 768))
#define UP_STEP(C, N, q, D0, D1, D2, D3, D4, D5) do { \
        acc[0] = MFMA(wf##C[0], xf##C[0], acc[0]); xf##N[0] = UP_RX(q, 0); D0; SBAR(); PACE(); \
        acc[3] = MFMA(wf##C[0], xf##C[1], acc[3]); wf##N[0] = UP_RW(q, 0); D1; SBAR(); PACE(); \
        acc[1] = MFMA(wf##C[1], xf##C[0], acc[1]); xf##N[1] = UP_RX(q, 1); D2; SBAR(); PACE(); \
        acc[4] = MFMA(wf##C[1], xf##C[1], acc[4]); wf##N[1] = UP_RW(q, 1); D3; SBAR(); PACE(); \
        acc[2] = MFMA(wf##C[2], xf##C[0], acc[2]); wf##N[2] = UP_RW(q, 2); D4; SBAR(); PACE(); \
        acc[5] = MFMA(wf##C[2], xf##C[1], acc[5]); D5; SBAR(); PACE(); } while (0)
#define NOP_ do {} while (0)
    WAIT_BAR(10);
    STAMP(2, 9);
    if (tid == 0) flag_set(p.ws, WS_SEEN + blockIdx.x * 8, tag);
    xf0[0] = UP_RX(0, 0); xf0[1] = UP_RX(0, 1);
#pragma unroll
    for (int i = 0; i < 3; ++i) wf0[i] = UP_RW(0, i);
    for (int it = 0; it < 4; ++it) {
        const int s0 = 4 * it, kb = 2 * it;
        UP_STEP(0, 1, 1, NOP_, UP_W0(s0 + 3), NOP_, UP_W1(s0 + 3), NOP_, UP_W2(s0 + 3));
        WAIT_BAR(10);
        UP_STEP(1, 0, 2, UP_W0(s0 + 4), NOP_, NOP_, UP_W1(s0 + 4), NOP_, NOP_);
        UP_STEP(0, 1, 3, UP_W2(s0 + 4), NOP_, NOP_, NOP_, NOP_, NOP_);
        WAIT_BAR(6);
        UP_STEP(1, 0, 4, UP_X(kb + 2, 0), UP_X(kb + 2, 1), NOP_, UP_X(kb + 2, 2), NOP_, UP_X(kb + 2, 3));
        UP_STEP(0, 1, 5, NOP_, UP_W0(s0 + 5), NOP_, UP_W1(s0 + 5), NOP_, UP_W2(s0 + 5));
        WAIT_BAR(10);
        UP_STEP(1, 0, 6, UP_W0(s0 + 6), NOP_, NOP_, UP_W1(s0 + 6), NOP_, NOP_);
        UP_STEP(0, 1, 7, UP_W2(s0 + 6), NOP_, NOP_, NOP_, NOP_, NOP_);
        WAIT_BAR(6);
        UP_STEP(1, 0, 0, UP_X(kb + 3, 0), UP_X(kb + 3, 1), NOP_, UP_X(kb + 3, 2), NOP_, UP_X(kb + 3, 3));
        STAMP(2, 10 + it);
    }
    UP_STEP(0, 1, 1, UP_W0(19), UP_W1(19), UP_W2(19), NOP_, NOP_, NOP_);
    WAIT_BAR(3);
    UP_STEP(1, 0, 2, NOP_, NOP_, NOP_, NOP_, NOP_, NOP_);
    UP_STEP(0, 1, 3, NOP_, NOP_, NOP_, NOP_, NOP_, NOP_);
    UP_STEP(1, 0, 4, NOP_, NOP_, NOP_, NOP_, NOP_, NOP_);
    UP_STEP(0, 1, 5, NOP_, NOP_, NOP_, NOP_, NOP_, NOP_);
    WAIT_BAR(0);
    UP_STEP(1, 0, 6, NOP_, NOP_, NOP_, NOP_, NOP_, NOP_);
    UP_STEP(0, 1, 7, NOP_, NOP_, NOP_, NOP_, NOP_, NOP_);
#pragma unroll
    for (int i = 0; i < 6; ++i) acc[i] = MFMA(wf1[i % 3], xf1[i / 3], acc[i]);
    __builtin_amdgcn_s_setprio(3);
    STAMP(2, 2);
    const __amdgpu_buffer_rsrc_t rs = __builtin_amdgcn_make_buffer_rsrc(p.ws, 0, (int)WS_END, 0x00020000);
    const unsigned ho = (unsigned)WS_H + (unsigned)((rb2 * 4 + 2 * wr) * 96 + fs * 6 + 3 * wc) * 1024 + lane * 16;
#pragma unroll
    for (int j = 0; j < 6; ++j) {
        float hv[8];
#pragma unroll
        for (int r = 0; r < 8; ++r) hv[r] = __builtin_fmaxf(acc[j][r], 0.f) * acc[j][8 + r];
        st16_dev(rs, ho + (j / 3) * 96 * 1024 + (j % 3) * 1024, (u32x4){cvtpk(hv[0], hv[1]), cvtpk(hv[2], hv[3]), cvtpk(hv[4], hv[5]), cvtpk(hv[6], hv[7])});
    }
    WAIT_BAR(0);
    if (tid == 0) flag_set(p.ws, WS_HF + (rb2 * 4 + (fs >> 2)) * 128 + (fs & 3) * 8, tag);
    STAMP(2, 3);
    return tag;
}

#define MFMA16(a, b, c) __builtin_amdgcn_mfma_f32_16x16x32_bf16((a), (b), (c), 0, 0, 0)
DI void ld16_dev(bf16x8& d0, bf16x8& d1, const char* sbase, unsigned voff) {
    asm volatile("global_load_dwordx4 %0, %2, %3 sc1\n\tglobal_load_dwordx4 %1, %2, %3 offset:256 sc1" : "=&v"(d0), "=&v"(d1) : "v"(voff), "s"(sbase) : "memory");
}
DI void out_wg(const Params& p, int rb, int cn, u64 tag, char* lds) {
    const int tid = threadIdx.x, lane = tid & 63, w = __builtin_amdgcn_readfirstlane(tid >> 6);
    const int r16 = lane & 15, g = lane >> 4;
    const unsigned lds0 = (unsigned)(uintptr_t)lds + w * 20480;
    unsigned wo_[5], wm_[5], wl_[5];
#pragma unroll
    for (int j = 0; j < 5; ++j) {
        const int idx = j * 64 + lane, r = idx / 10, ch = (idx % 10) * 16;
        wo_[j] = r * (D_ * 2) + ch; wm_[j] = ((r >> 4) * 20 + (r & 15)) * (D_ * 2) + ch; wl_[j] = ((r >> 2) * 20 + 16 + (r & 3)) * (D_ * 2) + ch;
    }
    const u16* dsrc = p.wd + (size_t)w * 384 * D_ + cn * 80;
    const u16* osrc = p.wo + (size_t)w * 160 * D_ + cn * 80;
    const char* hsrc = p.ws + WS_H + ((size_t)(rb * 2) * 48 + w * 12) * 2048;
    const char* asrc = p.ws + WS_O + ((size_t)(rb * 2) * 40 + w * 10) * 1024;
    const unsigned lo16 = (g >> 1) * 1024 + (g & 1) * 512 + r16 * 16;
#define W_PIECES(s_, o_, d_) do { glds16(s_, o_[0], d_); glds16(s_, o_[1], d_ + 1024); glds16(s_, o_[2], d_ + 2048); glds16(s_, o_[3], d_ + 3072); glds16(s_, o_[4], d_ + 4096); } while (0)
#define W_DMA(u) do { const unsigned d_ = lds0 + ((u) & 3) * 5120; \
        if ((u) < 12) W_PIECES(dsrc + (size_t)(u) * 32 * D_, wo_, d_); else if ((u) < 16) W_PIECES(osrc + (size_t)((u) - 12) * 40 * D_, wm_, d_); else W_PIECES(osrc, wl_, d_); } while (0)
    asm volatile("" :: "v"(wo_[0]), "v"(wo_[1]), "v"(wo_[2]), "v"(wo_[3]), "v"(wo_[4]), "v"(wm_[0]), "v"(wm_[1]), "v"(wm_[2]), "v"(wm_[3]), "v"(wm_[4]), "v"(wl_[0]), "v"(wl_[1]), "v"(wl_[2]), "v"(wl_[3]), "v"(wl_[4]));
    __builtin_amdgcn_s_setprio(1);
    W_DMA(0); W_DMA(1); W_DMA(2); W_DMA(3);
    f32x4 acc[4][5];
#pragma unroll
    for (int i = 0; i < 4; ++i)
#pragma unroll
        for (int j = 0; j < 5; ++j) acc[i][j] = (f32x4){0.f, 0.f, 0.f, 0.f};
    const lds_cptr wbase = (lds_cptr)lds + w * 20480 + (16 * (g >> 1) + 4 * (g & 1) + (r16 >> 2)) * 160 + (lane & 3) * 8;
    bf16x8 wf[2][5];
    bf16x8 af[5][4];
#define W_RD(u, i) wf[(u) & 1][i] = cat8(trread(wbase + ((u) & 3) * 5120 + (i) * 32), trread(wbase + ((u) & 3) * 5120 + (i) * 32 + 1280))
#define H_LD(u) do { ld16_dev(af[(u) & 3][0], af[(u) & 3][1], hsrc + (u) * 2048, lo16); ld16_dev(af[(u) & 3][2], af[(u) & 3][3], hsrc + 48 * 2048 + (u) * 2048, lo16); } while (0)
#define W_PIECE(u, i) glds16((u) < 12 ? (const void*)(dsrc + (size_t)(u) * 32 * D_) : (const void*)(osrc + (size_t)((u) - 12) * 40 * D_), (u) < 12 ? wo_[i] : wm_[i], lds0 + ((u) & 3) * 5120 + (i) * 1024)
#define UNIT_D(u, A) do { _Pragma("unroll") for (int i = 0; i < 5; ++i) { \
        acc[0][i] = MFMA16(wf[(u) & 1][i], A[0], acc[0][i]); acc[1][i] = MFMA16(wf[(u) & 1][i], A[1], acc[1][i]); W_PIECE((u) + 4, i); SBAR(); \
        acc[2][i] = MFMA16(wf[(u) & 1][i], A[2], acc[2][i]); acc[3][i] = MFMA16(wf[(u) & 1][i], A[3], acc[3][i]); W_RD((u) + 1, i); \
        if ((u) + 3 < 12 && i == 1) ld16_dev(af[((u) + 3) & 3][0], af[((u) + 3) & 3][1], hsrc + ((u) + 3) * 2048, lo16); \
        if ((u) + 3 < 12 && i == 3) ld16_dev(af[((u) + 3) & 3][2], af[((u) + 3) & 3][3], hsrc + 48 * 2048 + ((u) + 3) * 2048, lo16); \
        SBAR(); } } while (0)
#define UNIT(u, A) do { _Pragma("unroll") for (int i = 0; i < 5; ++i) { \
        _Pragma("unroll") for (int rg = 0; rg < 4; ++rg) acc[rg][i] = MFMA16(wf[(u) & 1][i], A[rg], acc[rg][i]); \
        if ((u) + 1 < 17) W_RD((u) + 1, i); SBAR(); } } while (0)
#define PINA(A) "+v"(A[0]), "+v"(A[1]), "+v"(A[2]), "+v"(A[3])
    flags_wait<1, 32>(p.ws + WS_KVF + (blockIdx.x & 15) * 1024 + (rb * 4) * 8, 4, tag, lane);
    flags_wait<1, 16>(p.ws + WS_HF + ((rb >> 1) * 4 + w) * 128, 4, tag, lane);
    STAMP(2, 4);
    float gm[4];
#pragma unroll
    for (int rg = 0; rg < 4; ++rg) gm[rg] = __uint_as_float(cvtpk(1.f - ld4_dev(p.ws + WS_G + (rb * 64 + rg * 16 + r16) * 4), 0.f) << 16);
    asm volatile("s_waitcnt vmcnt(0)" :: "v"(gm[0]), "v"(gm[1]), "v"(gm[2]), "v"(gm[3]) : "memory");
    H_LD(0); H_LD(1); H_LD(2);
#pragma unroll
    for (int i = 0; i < 5; ++i) W_RD(0, i);
    __builtin_amdgcn_s_setprio(1);
#pragma unroll
    for (int u = 0; u < 12; ++u) {
        if (u == 0) asm volatile("s_waitcnt vmcnt(8) lgkmcnt(0)" : PINA(af[0]) :: "memory");
        else if (u == 1) asm volatile("s_waitcnt vmcnt(13) lgkmcnt(0)" : PINA(af[1]) :: "memory");
        else if (u <= 9) asm volatile("s_waitcnt vmcnt(18) lgkmcnt(0)" : PINA(af[u & 3]) :: "memory");
        else if (u == 10) asm volatile("s_waitcnt vmcnt(14) lgkmcnt(0)" : PINA(af[u & 3]) :: "memory");
        else asm volatile("s_waitcnt vmcnt(10) lgkmcnt(0)" : PINA(af[u & 3]) :: "memory");
        SBAR();
        UNIT_D(u, af[u & 3]);
    }
    __builtin_amdgcn_s_setprio(3);
    asm volatile("s_waitcnt lgkmcnt(0)" ::: "memory");
    W_DMA(16);
#pragma unroll
    for (int rg = 0; rg < 4; ++rg)
#pragma unroll
        for (int i = 0; i < 5; ++i) acc[rg][i] *= gm[rg];
    STAMP(2, 5);
    flags_wait<1, 6>(p.ws + WS_OF + (rb * 4 + w) * 128, 8, tag, lane);
    STAMP(2, 6);
#pragma unroll
    for (int v = 0; v < 5; ++v) { ld16_dev(af[v][0], af[v][1], asrc + v * 2048, lo16); ld16_dev(af[v][2], af[v][3], asrc + 40 * 1024 + v * 2048, lo16); }
    asm volatile("s_waitcnt vmcnt(16)" : PINA(af[0]) :: "memory"); SBAR(); UNIT(12, af[0]);
    asm volatile("s_waitcnt vmcnt(12)" : PINA(af[1]) :: "memory"); SBAR(); UNIT(13, af[1]);
    asm volatile("s_waitcnt vmcnt(8)" : PINA(af[2]) :: "memory"); SBAR(); UNIT(14, af[2]);
    asm volatile("s_waitcnt vmcnt(4)" : PINA(af[3]) :: "memory"); SBAR(); UNIT(15, af[3]);
    asm volatile("s_waitcnt vmcnt(0)" : PINA(af[4]) :: "memory"); SBAR(); UNIT(16, af[4]);
    STAMP(2, 7);
    LAS f32x4* mine = (LAS f32x4*)((lds_ptr)lds + w * 20480);
#pragma unroll
    for (int rg = 0; rg < 4; ++rg)
#pragma unroll
        for (int i = 0; i < 5; ++i) mine[(rg * 5 + i) * 64 + lane] = acc[rg][i];
    WAIT_BAR(0);
    const LAS f32x4* all = (const LAS f32x4*)lds + w * 5 * 64 + lane;
    u16* orow = p.out + (size_t)(rb * 64 + w * 16 + r16) * D_ + cn * 80 + 4 * g;
#pragma unroll
    for (int i = 0; i < 5; ++i) {
        const f32x4 s = (all[i * 64] + all[1280 + i * 64]) + (all[2560 + i * 64] + all[3840 + i * 64]);
        st8_dev(orow + 16 * i, (u32x2){cvtpk(s[0], s[1]), cvtpk(s[2], s[3])});
    }
    STAMP(2, 8);
}

__global__ __launch_bounds__(256, 2) void k_fused(Params p) {
    extern __shared__ __attribute__((aligned(16))) char lds[];
    const int b = blockIdx.x, j = b & 7, i = (b >> 3) & 31;
    const u64 ep = epoch_load(p.ws + WS_EPOCH);
    if (b < 256) {
#define X_APRIO 2
        __builtin_amdgcn_s_setprio(X_APRIO);
        attn_wg(p, 16 * (j >> 2) + (i >> 1), 2 * (j & 3) + (i & 1), ep, lds);
    } else {
        STAMP(2, 0);
        if (i < 16) { __builtin_amdgcn_s_setprio(3); kv_job(p, 8 * j + (i >> 1), i & 1, ep, lds); __builtin_amdgcn_s_setprio(1); }
        const u64 tag = ffn_up_wg(p, 8 * (j >> 2) + (i >> 2), 4 * (j & 3) + (i & 3), ep, lds);
        out_wg(p, 4 * j + (i >> 3), i & 7, tag, lds);
    }
}

extern "C" void kernel_launch(void* const* d_in, const int* in_sizes, int n_in, void* d_out, int out_size, void* d_ws, size_t ws_size,
                              hipStream_t stream) {
    Params p;
    p.x = (const u16*)d_in[0]; p.wq = (const u16*)d_in[1]; p.wk = (const u16*)d_in[2]; p.wv = (const u16*)d_in[3]; p.wo = (const u16*)d_in[4];
    p.wg = (const u16*)d_in[6]; p.wu = (const u16*)d_in[7]; p.wd = (const u16*)d_in[8]; p.gate_w = (const u16*)d_in[9];
    p.out = (u16*)d_out; p.ws = (char*)d_ws;
    (void)hipFuncSetAttribute((const void*)k_fused, hipFuncAttributeMaxDynamicSharedMemorySize, LDS_BYTES);
    k_fused<<<512, 256, LDS_BYTES, stream>>>(p);
}
```

```cpp
#include <hip/hip_runtime.h>
#include <hip/hip_bf16.h>
#include <stdint.h>

typedef __attribute__((ext_vector_type(8))) short bf16x8;
typedef __attribute__((ext_vector_type(4))) short s16x4;
typedef __attribute__((ext_vector_type(16))) float f32x16;
typedef __attribute__((ext_vector_type(4))) float f32x4;
typedef __attribute__((ext_vector_type(2))) float f32x2;
typedef __attribute__((ext_vector_type(4))) unsigned u32x4;
typedef __attribute__((ext_vector_type(2))) unsigned u32x2;
typedef __attribute__((ext_vector_type(2))) __bf16 bf16x2_t;
typedef short v4i16_t __attribute__((ext_vector_type(4)));
typedef unsigned short u16;
typedef unsigned long long u64;
#define LAS __attribute__((address_space(3)))
#define GAS __attribute__((address_space(1)))
#define CAS __attribute__((address_space(4)))
typedef LAS const char* lds_cptr;
typedef LAS char* lds_ptr;
#define DI __device__ __forceinline__

constexpr int D_ = 640, F_ = 1536;
constexpr int KV_TILE_B = 2624;
constexpr int KV_CHUNK_B = 4 * KV_TILE_B;
constexpr int KV_SLOT_B = 12288;
constexpr size_t WS_EPOCH = 0, WS_SEEN = 1024, WS_HF = 8192, WS_KVF = 16384, WS_OF = 32768;
constexpr size_t WS_KV = 64 * 1024;
constexpr size_t WS_G = 240 * 1024;
constexpr size_t WS_H = 256 * 1024;
constexpr size_t WS_O = WS_H + (size_t)64 * 48 * 2048;
constexpr size_t WS_END = WS_O + (size_t)64 * 40 * 1024;
constexpr int LDS_BYTES = 80 * 1024;
constexpr unsigned SPIN_CAP = 1u << 18;

#define MFMA(a, b, c) __builtin_amdgcn_mfma_f32_32x32x16_bf16((a), (b), (c), 0, 0, 0)
#define SBAR() __builtin_amdgcn_sched_barrier(0)
#define WAIT_VM(N) asm volatile("s_waitcnt vmcnt(" #N ")" ::: "memory")
#define WAIT_BAR(N) asm volatile("s_waitcnt vmcnt(" #N ") lgkmcnt(0)\n\ts_barrier" ::: "memory")
#define PACE() do {} while (0)
#define RLX_AGENT __ATOMIC_RELAXED, __HIP_MEMORY_SCOPE_AGENT
#define BC(x) __builtin_bit_cast(bf16x8, x)

DI unsigned cvtpk(float lo, float hi) {
    f32x2 v = {lo, hi};
    return __builtin_bit_cast(unsigned, __builtin_convertvector(v, bf16x2_t));
}
DI void glds16(const void* sbase, unsigned voff, unsigned lds_base) {
    unsigned sv;
    asm volatile("s_mov_b32 %0, m0\n\ts_mov_b32 m0, %3\n\ts_nop 0\n\tglobal_load_lds_dwordx4 %1, %2\n\ts_mov_b32 m0, %0"
                 : "=&s"(sv) : "v"(voff), "s"(sbase), "s"(lds_base) : "memory");
}
DI void glds16v(const void* ptr, unsigned lds_base) {
    unsigned sv;
    asm volatile("s_mov_b32 %0, m0\n\ts_mov_b32 m0, %2\n\ts_nop 0\n\tglobal_load_lds_dwordx4 %1, off\n\ts_mov_b32 m0, %0"
                 : "=&s"(sv) : "v"(ptr), "s"(lds_base) : "memory");
}
DI void glds16_dev(const void* sbase, unsigned voff, unsigned lds_base) {
    unsigned sv;
    asm volatile("s_mov_b32 %0, m0\n\ts_mov_b32 m0, %3\n\ts_nop 0\n\tglobal_load_lds_dwordx4 %1, %2 sc1\n\ts_mov_b32 m0, %0"
                 : "=&s"(sv) : "v"(voff), "s"(sbase), "s"(lds_base) : "memory");
}
DI s16x4 trread(lds_cptr p) { return __builtin_bit_cast(s16x4, __builtin_amdgcn_ds_read_tr16_b64_v4i16((LAS v4i16_t*)p)); }
DI bf16x8 cat8(s16x4 lo, s16x4 hi) { return __builtin_shufflevector(lo, hi, 0, 1, 2, 3, 4, 5, 6, 7); }
DI bf16x8 ldsread16(lds_cptr p) { return *(const LAS bf16x8*)p; }
DI float xhalf_max(float m) {
    auto rr = __builtin_amdgcn_permlane32_swap(__float_as_uint(m), __float_as_uint(m), false, false);
    return __builtin_fmaxf(__uint_as_float(rr[0]), __uint_as_float(rr[1]));
}
DI f32x16 zero16() { f32x16 z; for (int i = 0; i < 16; ++i) z[i] = 0.f; return z; }
DI void st16_dev(__amdgpu_buffer_rsrc_t r, unsigned off, u32x4 v) { __builtin_amdgcn_raw_buffer_store_b128(v, r, off, 0, 16); }
DI void st8_dev(void* p, u32x2 v) { __hip_atomic_store((GAS u64*)p, (u64)v[0] | ((u64)v[1] << 32), RLX_AGENT); }
DI void st4_dev(void* p, float v) { __hip_atomic_store((GAS unsigned*)p, __float_as_uint(v), RLX_AGENT); }
DI u64 ld8_dev(const void* p) { return __hip_atomic_load((const GAS u64*)p, RLX_AGENT); }
DI float ld4_dev(const void* p) { return __uint_as_float(__hip_atomic_load((const GAS unsigned*)p, RLX_AGENT)); }
DI void flag_set(char* ws, size_t off, u64 tag) { __hip_atomic_store((GAS u64*)(ws + off), tag, RLX_AGENT); }
DI u64 epoch_load(const char* ws) { u64 e; asm volatile("s_load_dwordx2 %0, %1, 0x0" : "=s"(e) : "s"(ws) : "memory"); return e; }
DI u64 epoch_tag(u64 e) { u64 r; asm volatile("s_waitcnt lgkmcnt(0)\n\ts_mov_b64 %0, %1" : "=s"(r) : "s"(e) : "memory"); return r * 6364136223846793005ull + 1442695040888963407ull; }
template <int K, int SLEEP> DI void flags_wait(const char* f, int n, u64 tag, int lane) {
    for (unsigned spin = 0; spin < SPIN_CAP; ++spin) {
        bool ok = true;
#pragma unroll
        for (int k = 0; k < K; ++k) { const int i = lane + 64 * k; const u64 v = ld8_dev(f + 8 * (i < n ? i : 0)); ok &= (v == tag); }
        if (__all(ok)) break;
        __builtin_amdgcn_s_sleep(SLEEP);
    }
}

#define STAMP(kid, i) do {} while (0)
struct Params {
    const u16* x; const u16* wq; const u16* wk; const u16* wv; const u16* wo;
    const u16* wg; const u16* wu; const u16* wd; const u16* gate_w;
    u16* out; char* ws;
};

DI void kv_job(const Params& p, int rt, int isv, u64 ep, char* lds) {
    const int tid = threadIdx.x, lane = tid & 63, w = __builtin_amdgcn_readfirstlane(tid >> 6);
    const int r32 = lane & 31, h = lane >> 5;
    const unsigned lds0 = (unsigned)(uintptr_t)lds;
    STAMP(0, 0);
    const unsigned xoff = w * 17408, woff = xoff + 10240;
    const u16* xs = p.x + (size_t)rt * 32 * D_ + w * 160;
#pragma unroll
    for (int i = 0; i < 10; ++i) {
        const int idx = i * 64 + lane, row = idx / 20, ch = (idx % 20) ^ ((row >> 2) & 3);
        glds16(xs, (unsigned)(row * (D_ * 2) + ch * 16), lds0 + xoff + i * 1024);
    }
    const u16* wsrc = (isv ? p.wv : p.wk);
#pragma unroll
    for (int i = 0; i < 6; ++i) glds16(wsrc, (unsigned)(w * 6400 + i * 1024 + lane * 16), lds0 + woff + i * 1024);
    glds16v(lane < 16 ? (const char*)wsrc + w * 6400 + 6144 + lane * 16 : (const char*)p.gate_w + w * 320 + (lane < 36 ? lane - 16 : 0) * 16, lds0 + woff + 6144);
    const u64 tag = epoch_tag(ep);
    WAIT_VM(0);
    STAMP(0, 3);
    const lds_cptr xb0 = (lds_cptr)lds + xoff + r32 * 320 + ((h ^ ((r32 >> 2) & 3)) << 4), xb1 = (lds_cptr)lds + xoff + r32 * 320 + (((2 + h) ^ ((r32 >> 2) & 3)) << 4);
    const lds_cptr wb = (lds_cptr)lds + woff + (8 * h + ((lane & 15) >> 2)) * 40 + ((lane >> 4) & 1) * 32 + (lane & 3) * 8;
    const lds_cptr gb = (lds_cptr)lds + woff + 6400 + h * 16;
    bf16x8 xf[10], wf[10], gf[10];
#pragma unroll
    for (int ks = 0; ks < 10; ++ks) { xf[ks] = ldsread16(((ks & 1) ? xb1 : xb0) + (ks >> 1) * 64); wf[ks] = cat8(trread(wb + ks * 640), trread(wb + ks * 640 + 160)); gf[ks] = ldsread16(gb + ks * 32); }
    SBAR();
    f32x16 acc = zero16();
    if (isv) {
#pragma unroll
        for (int ks = 0; ks < 10; ++ks) acc = MFMA(xf[ks], wf[ks], acc);
    } else {
#pragma unroll
        for (int ks = 0; ks < 10; ++ks) acc = MFMA((r32 == 20) ? gf[ks] : wf[ks], xf[ks], acc);
    }
    LAS f32x4* red = (LAS f32x4*)((lds_ptr)lds + 69632);
    if (w > 0) {
#pragma unroll
        for (int g = 0; g < 4; ++g) red[((w - 1) * 4 + g) * 64 + lane] = (f32x4){acc[4 * g], acc[4 * g + 1], acc[4 * g + 2], acc[4 * g + 3]};
    }
    WAIT_BAR(0);
    if (w == 0) {
#pragma unroll
        for (int ww = 0; ww < 3; ++ww)
#pragma unroll
            for (int g = 0; g < 4; ++g) { const f32x4 t = red[(ww * 4 + g) * 64 + lane]; acc[4 * g] += t[0]; acc[4 * g + 1] += t[1]; acc[4 * g + 2] += t[2]; acc[4 * g + 3] += t[3]; }
    }
    WAIT_BAR(0);
    if (w == 0) {
        const __amdgpu_buffer_rsrc_t rs = __builtin_amdgcn_make_buffer_rsrc(p.ws, 0, (int)WS_END, 0x00020000);
        const unsigned tile = (unsigned)WS_KV + (unsigned)(isv ? rt : ((rt + 63) & 63)) * KV_TILE_B;
        u32x4 lo = {cvtpk(acc[0], acc[1]), cvtpk(acc[2], acc[3]), cvtpk(acc[4], acc[5]), cvtpk(acc[6], acc[7])};
        u32x4 hi = {cvtpk(acc[8], acc[9]), cvtpk(acc[10], acc[11]), cvtpk(acc[12], acc[13]), cvtpk(acc[14], acc[15])};
        if (!isv) {
            st16_dev(rs, tile + lane * 16, lo);
            if (h == 0) st8_dev(p.ws + tile + 1024 + r32 * 8, (u32x2){hi[0], hi[1]});
            else { const float lg = __uint_as_float(cvtpk(acc[8], 0.f) << 16);
                   st4_dev(p.ws + WS_G + (rt * 32 + r32) * 4, __uint_as_float(cvtpk(1.f / (1.f + __expf(-lg)), 0.f) << 16)); }
        } else {
            if (r32 == 20) { lo = (u32x4){0x3f803f80u, 0x3f803f80u, 0x3f803f80u, 0x3f803f80u}; hi = lo; }
            if (r32 <= 20) { st16_dev(rs, tile + 1280 + h * 336 + r32 * 16, lo); st16_dev(rs, tile + 1952 + h * 336 + r32 * 16, hi); }
        }
        STAMP(0, 6);
        WAIT_VM(0);
        STAMP(0, 7);
        if (lane < 16) flag_set(p.ws, WS_KVF + lane * 1024 + (rt * 2 + isv) * 8, tag);
    }
}

#define AE(b, i) "v_exp_f32 v[" #b "+" #i "], v[" #b "+" #i "]\n"
#define AC(pb, j, b, i) "v_cvt_pk_bf16_f32 v[" #pb "+" #j "], v[" #b "+" #i "], v[" #b "+" #i "+1]\n"
#define AG2(MA, MB, X, XI, PX, PJ) MA AE(X, XI) AE(X, XI+1) AE(X, XI+2) AE(X, XI+3) AC(PX, PJ, X, XI) AC(PX, PJ+1, X, XI+2) \
    MB AE(X, XI+4) AE(X, XI+5) AE(X, XI+6) AE(X, XI+7) AC(PX, PJ+2, X, XI+4) AC(PX, PJ+3, X, XI+6)
#define AHALF(X, PX, Y, Q1, Q2, O, PR, BUF, R1, R2, R3, R4) \
    AG2("v_mfma_f32_32x32x16_bf16 v[" #Y ":" #Y "+15], v[" #BUF ":" #BUF "+3], " Q1 ", 0\n" R1, \
        "v_mfma_f32_32x32x16_bf16 v[" #Y ":" #Y "+15], v[" #BUF "+4:" #BUF "+7], " Q2 ", v[" #Y ":" #Y "+15]\n" R2, X, 0, PX, 0) \
    AG2("v_mfma_f32_32x32x16_bf16 " O ", v[" #BUF "+8:" #BUF "+11], v[" #PR ":" #PR "+3], " O "\n" R3, \
        "v_mfma_f32_32x32x16_bf16 " O ", v[" #BUF "+12:" #BUF "+15], v[" #PR "+4:" #PR "+7], " O "\n" R4, X, 8, PX, 4)
#define ARK1(NB, sl, tl) "ds_read_b128 v[" #NB ":" #NB "+3], %[k1a] offset:4096+(" #sl ")*12288+" #tl "*2624\n"
#define ARK2(NB, sl, tl) "ds_read_b64 v[" #NB "+4:" #NB "+5], %[k2a] offset:4096+(" #sl ")*12288+" #tl "*2624\n"
#define ARV0(NB, sl, tl) "ds_read_b128 v[" #NB "+8:" #NB "+11], %[va] offset:4096+(" #sl ")*12288+" #tl "*2624\n"
#define ARV1(NB, sl, tl) "ds_read_b128 v[" #NB "+12:" #NB "+15], %[va] offset:4096+(" #sl ")*12288+" #tl "*2624+672\n"
#define AH1(PREV, R1, R2, R3, R4) AHALF(128, 160, 144, "%[qb1]", "%[qb2]", "%[ob]", 168, PREV, R1, R2, R3, R4)
#define AH2(CUR, PREV, sl, tl) AHALF(144, 168, 128, "%[qa1]", "%[qa2]", "%[oa]", 160, CUR, ARK1(PREV, sl, tl), ARK2(PREV, sl, tl), ARV0(PREV, sl, tl), ARV1(PREV, sl, tl))
#define AWL "s_waitcnt lgkmcnt(0)\n"
#define ASYNC "s_waitcnt vmcnt(6) lgkmcnt(0)\ns_barrier\n"
#define ADMA0(fs) "s_mul_i32 %[of], %[ck], 10496\n s_add_u32 %[ck], %[ck], 1\n s_add_u32 m0, %[ldst], " #fs "*12288\n" \
                  "v_add_u32 v208, %[of], %[kvo]\n"
#define ADMA1 "global_load_lds_dwordx4 v208, %[img] sc1\n"
#define ADMA2 "global_load_lds_dwordx4 v208, %[img] offset:1024 sc1\n"
#define ADMA3 "global_load_lds_dwordx4 v208, %[img] offset:2048 sc1\n"
#define ACHUNK(sl, nsl) \
    AH1(192, "", "", "", "") AWL AH2(176, 192, sl, 2) \
    AH1(176, "", "", "", "") AWL AH2(192, 176, sl, 3) \
    AH1(192, "", "", "", "") ASYNC AH2(176, 192, nsl, 0) \
    AH1(176, ADMA0(sl), ADMA1, ADMA2, ADMA3) AWL AH2(192, 176, nsl, 1)
#define ACHUNKT(sl, nsl, W) \
    AH1(192, "", "", "", "") AWL AH2(176, 192, sl, 2) \
    AH1(176, "", "", "", "") AWL AH2(192, 176, sl, 3) \
    AH1(192, "", "", "", "") "s_waitcnt vmcnt(" #W ") lgkmcnt(0)\ns_barrier\n" AH2(176, 192, nsl, 0) \
    AH1(176, "", "", "", "") AWL AH2(192, 176, nsl, 1)
#define ASP1(NB) "ds_read_b128 v[" #NB "+8:" #NB "+11], %[va]\n"
#define ASP2(NB) "ds_read_b128 v[" #NB "+12:" #NB "+15], %[va] offset:672\n"
#define ACHUNKL(sl) \
    AH1(192, "", "", "", "") AWL AH2(176, 192, sl, 2) \
    AH1(176, "", "", "", "") AWL AH2(192, 176, sl, 3) \
    AH1(192, "", "", "", "") AWL AHALF(144, 168, 128, "%[qa1]", "%[qa2]", "%[oa]", 160, 176, ASP1(192), ASP2(192), "", "") \
    AH1(176, "", "", "", "") AWL AHALF(144, 168, 128, "%[qa1]", "%[qa2]", "%[oa]", 160, 192, "", "", "", "")
#define AMOV(d, s) "v_mov_b32 v" #d ", " s "\n"
#define ATTN_LOOP_ASM \
    "s_setprio 2\n s_mov_b32 %[m0s], m0\n s_mov_b32 %[it], 3\n s_mov_b32 %[ck], 4\n" \
    ARK1(176, 1, 1) ARK2(176, 1, 1) ARV0(176, 1, 1) ARV1(176, 1, 1) \
    "v_mov_b64 v[192:193], %[k0a]\n v_mov_b64 v[194:195], %[k0b]\n" \
    "v_mov_b64 v[196:197], %[k02]\n" AMOV(198, "%[kone]") AMOV(199, "0") AMOV(182, "%[kone]") AMOV(183, "0") \
    "v_mfma_f32_32x32x16_bf16 v[128:143], v[192:195], %[qa1], 0\n" \
    AMOV(200, "0") AMOV(201, "0") AMOV(202, "0") AMOV(203, "0") AMOV(204, "0") AMOV(205, "0") AMOV(206, "0") AMOV(207, "0") \
    AMOV(168, "0") AMOV(169, "0") AMOV(170, "0") AMOV(171, "0") AMOV(172, "0") AMOV(173, "0") AMOV(174, "0") AMOV(175, "0") \
    "v_mfma_f32_32x32x16_bf16 v[128:143], v[196:199], %[qa2], v[128:143]\n" \
    "s_nop 7\n s_nop 7\n" \
    "1:\n" \
    ACHUNK(1, 2) ACHUNK(2, 3) ACHUNK(3, 0) ACHUNK(0, 1) \
    "s_sub_u32 %[it], %[it], 1\n s_cmp_lg_u32 %[it], 0\n s_cbranch_scc1 1b\n" \
    ACHUNKT(1, 2, 6) ACHUNKT(2, 3, 3) ACHUNKT(3, 0, 0) ACHUNKL(0) "s_nop 1\n" \
    "v_mfma_f32_32x32x16_bf16 %[ob], v[200:203], v[168:171], %[ob]\n" \
    "v_mfma_f32_32x32x16_bf16 %[ob], v[204:207], v[172:175], %[ob]\n" \
    "s_setprio 3\n s_mov_b32 m0, %[m0s]\n s_nop 7\n s_nop 7\n"
#define ATTN_CLOBBERS "v128","v129","v130","v131","v132","v133","v134","v135","v136","v137","v138","v139","v140","v141","v142","v143", \
    "v144","v145","v146","v147","v148","v149","v150","v151","v152","v153","v154","v155","v156","v157","v158","v159", \
    "v160","v161","v162","v163","v164","v165","v166","v167","v168","v169","v170","v171","v172","v173","v174","v175", \
    "v176","v177","v178","v179","v180","v181","v182","v183","v184","v185","v186","v187","v188","v189","v190","v191", \
    "v192","v193","v194","v195","v196","v197","v198","v199","v200","v201","v202","v203","v204","v205","v206","v207","v208"

DI void attn_wg(const Params& p, int rb, int c, u64 ep, char* lds) {
    const int tid = threadIdx.x, lane = tid & 63, w = __builtin_amdgcn_readfirstlane(tid >> 6);
    const int r32 = lane & 31, h = lane >> 5;
    const unsigned lds0 = (unsigned)(uintptr_t)lds;
    const char* kvimg = p.ws + WS_KV;
    STAMP(1, 0);
    constexpr int RB = 32768;
    const unsigned kvoff = (unsigned)(w * 3072 + lane * 16);
#define KV_DMA(chunk) do { const char* s_ = kvimg + (size_t)(chunk) * KV_CHUNK_B; const unsigned d_ = lds0 + RB + (((chunk) + 1) & 3) * KV_SLOT_B + w * 3072; \
        glds16_dev(s_, kvoff, d_); glds16_dev(s_, kvoff + 1024, d_ + 1024); glds16_dev(s_, kvoff + 2048, d_ + 2048); } while (0)
    constexpr int QS_B = 18432;
    unsigned qoff[5]; unsigned qdst[5];
#pragma unroll
    for (int j = 0; j < 5; ++j) {
        int i = w + 4 * j; if (i >= 18) i -= 2;
        if (j < 2) { const int row = 8 * i + (lane >> 3), pos = lane & 7, ch = pos ^ ((row >> 1) & 7); qoff[j] = row * (D_ * 2) + ch * 16; }
        else { const int idx = (i - 8) * 64 + lane, kr = idx / 10, ch = idx % 10; qoff[j] = kr * (D_ * 2) + ch * 16; }
        qdst[j] = lds0 + i * 1024;
    }
    const u16* xsrc = p.x + (size_t)rb * 64 * D_;
    const u16* wsrc = p.wq + c * 80;
#define Q_DMA(kt) do { const unsigned d_ = ((kt) & 3) * QS_B; const u16* xs_ = xsrc + (kt) * 64; const u16* ws_ = wsrc + (size_t)(kt) * 64 * D_; \
        glds16(xs_, qoff[0], d_ + qdst[0]); glds16(xs_, qoff[1], d_ + qdst[1]); glds16(ws_, qoff[2], d_ + qdst[2]); glds16(ws_, qoff[3], d_ + qdst[3]); glds16(ws_, qoff[4], d_ + qdst[4]); } while (0)
#define Q_PIECE(kt, j) glds16((j) < 2 ? xsrc + (kt) * 64 : wsrc + (size_t)(kt) * 64 * D_, qoff[j], ((kt) & 3) * QS_B + qdst[j])
    Q_DMA(0); Q_DMA(1); Q_DMA(2); Q_DMA(3);
    const u64 tag = epoch_tag(ep);
    f32x16 qa = zero16(), qb = zero16();
    {
        unsigned xa[4];
#pragma unroll
        for (int ks = 0; ks < 4; ++ks) xa[ks] = r32 * 128 + (((2 * ks + h) ^ ((r32 >> 1) & 7)) << 4);
        const unsigned wa = 8192 + (8 * h + ((lane & 15) >> 2)) * 160 + w * 40 + ((lane >> 4) & 1) * 32 + (lane & 3) * 8;
        bf16x8 wfA[4], x0A[4], x1A[4], wfB[4], x0B[4], x1B[4];
#define Q_RD(S, kt, ks) do { const lds_cptr sb_ = (lds_cptr)lds + ((kt) & 3) * QS_B; wf##S[ks] = cat8(trread(sb_ + wa + (ks) * 2560), trread(sb_ + wa + (ks) * 2560 + 640)); \
            x0##S[ks] = ldsread16(sb_ + xa[ks]); x1##S[ks] = ldsread16(sb_ + xa[ks] + 4096); } while (0)
#define Q_STEP(C, N, kt) do { \
            if ((kt) == 7 && w == 0) { flags_wait<2, 4>(p.ws + WS_KVF + (blockIdx.x & 15) * 1024, 128, tag, lane); STAMP(1, 5); }     \
            if ((kt) < 7) { WAIT_BAR(10); } else if ((kt) == 7) { WAIT_BAR(5); } else if ((kt) == 8) { WAIT_BAR(9); } \
            if ((kt) == 7) { KV_DMA(0); KV_DMA(1); KV_DMA(2); } \
            _Pragma("unroll") for (int ks = 0; ks < 4; ++ks) { qa = MFMA(wf##C[ks], x0##C[ks], qa); if ((kt) + 1 < 10) Q_RD(N, (kt) + 1, ks); SBAR(); qb = MFMA(wf##C[ks], x1##C[ks], qb); \
                if ((kt) + 4 < 10) { Q_PIECE((kt) + 4, ks); if (ks == 3) Q_PIECE((kt) + 4, 4); } SBAR(); } } while (0)
        WAIT_BAR(15);
        STAMP(3, 0);
        if (tid == 0) flag_set(p.ws, WS_SEEN + blockIdx.x * 8, tag);
#pragma unroll
        for (int ks = 0; ks < 4; ++ks) Q_RD(A, 0, ks);
#pragma unroll
        for (int kt = 0; kt < 10; kt += 2) { Q_STEP(A, B, kt); STAMP(3, 1 + kt); Q_STEP(B, A, kt + 1); }
    }
    STAMP(1, 1);
    constexpr float C2 = 0.22360679774997896f * 1.4426950408889634f;
    u32x4 qa1, qa2, qb1, qb2;
    qa1 = (u32x4){cvtpk(qa[0] * C2, qa[1] * C2), cvtpk(qa[2] * C2, qa[3] * C2), cvtpk(qa[4] * C2, qa[5] * C2), cvtpk(qa[6] * C2, qa[7] * C2)};
    qb1 = (u32x4){cvtpk(qb[0] * C2, qb[1] * C2), cvtpk(qb[2] * C2, qb[3] * C2), cvtpk(qb[4] * C2, qb[5] * C2), cvtpk(qb[6] * C2, qb[7] * C2)};
    qa2 = (u32x4){h ? 0u : cvtpk(qa[8] * C2, qa[9] * C2), h ? 0u : cvtpk(qa[10] * C2, qa[11] * C2), 0u, 0u};
    qb2 = (u32x4){h ? 0u : cvtpk(qb[8] * C2, qb[9] * C2), h ? 0u : cvtpk(qb[10] * C2, qb[11] * C2), 0u, 0u};
    WAIT_BAR(6);
    glds16_dev(kvimg, (unsigned)(w * 1024 + lane * 16), lds0 + RB - 4096 + w * 1024);
    KV_DMA(3);
    const unsigned k1a = lane * 16, k2a = 1024 + r32 * 8;
    STAMP(1, 6);
    const bf16x8 k01 = ldsread16((lds_cptr)lds + RB + KV_SLOT_B + k1a);
    const u32x2 k02 = *(const LAS u32x2*)((lds_cptr)lds + RB + KV_SLOT_B + k2a);
    const unsigned va = 1280 + h * 336 + (r32 <= 20 ? r32 : (r32 < 28 ? 20 : 16)) * 16;
    const unsigned kone = h ? 0u : 0x3f80u;
    auto kfrag2 = [](u32x2 v, unsigned one) { return __builtin_bit_cast(bf16x8, (u32x4){v[0], v[1], one, 0u}); };
    {
        f32x16 sa = MFMA(k01, BC(qa1), zero16()); sa = MFMA(kfrag2(k02, kone), BC(qa2), sa);
        f32x16 sb = MFMA(k01, BC(qb1), zero16()); sb = MFMA(kfrag2(k02, kone), BC(qb2), sb);
        float ma = sa[0], mb = sb[0];
#pragma unroll
        for (int r = 1; r < 16; ++r) { ma = __builtin_fmaxf(ma, sa[r]); mb = __builtin_fmaxf(mb, sb[r]); }
        ma = xhalf_max(ma); mb = xhalf_max(mb);
        qa2[2] = h ? 0u : (cvtpk(-ma, 0.f) & 0xffffu);
        qb2[2] = h ? 0u : (cvtpk(-mb, 0.f) & 0xffffu);
    }
    f32x16 oa = zero16(), ob = zero16();
    float ga, gb;
    asm volatile("global_load_dword %0, %2, %3 sc1\n\tglobal_load_dword %1, %2, %3 offset:128 sc1" : "=&v"(ga), "=&v"(gb) : "v"(r32 * 4), "s"(p.ws + WS_G + rb * 256) : "memory");
    STAMP(1, 2);
    {
        unsigned t_m0, t_it, t_ck, t_of;
        const u32x4 k0u = __builtin_bit_cast(u32x4, k01); const u32x2 k0a = {k0u[0], k0u[1]}, k0b = {k0u[2], k0u[3]};
        asm volatile(ATTN_LOOP_ASM
            : [oa] "+v"(oa), [ob] "+v"(ob), "+v"(ga), "+v"(gb), [m0s] "=&s"(t_m0), [it] "=&s"(t_it), [ck] "=&s"(t_ck), [of] "=&s"(t_of)
            : [qa1] "v"(qa1), [qa2] "v"(qa2), [qb1] "v"(qb1), [qb2] "v"(qb2), [k1a] "v"(lds0 + RB - 4096 + k1a), [k2a] "v"(lds0 + RB - 4096 + k2a), [va] "v"(lds0 + RB - 4096 + va),
              [kvo] "v"(kvoff), [img] "s"(kvimg), [ldst] "s"(lds0 + RB + w * 3072), [k0a] "v"(k0a), [k0b] "v"(k0b), [k02] "v"(k02), [kone] "v"(kone)
            : ATTN_CLOBBERS, "scc", "memory");
    }
    STAMP(1, 3);
    {
        float chk = 0.f;
#pragma unroll
        for (int r = 0; r < 8; ++r) chk += oa[r] * 0.f + ob[r] * 0.f;
        if (h == 0) { chk += (oa[8] + oa[9] + oa[10] + oa[11] + ob[8] + ob[9] + ob[10] + ob[11]) * 0.f; } else { chk += (oa[8] + ob[8]) * 0.f; }
        if (h == 1 && !(oa[8] > 1e-30f && ob[8] > 1e-30f)) chk = 1.f;
        if (__builtin_expect(__any(!(chk == 0.f)), 0)) {
            __builtin_amdgcn_fence(__ATOMIC_ACQUIRE, "agent");
            qa2[2] = 0u; qb2[2] = 0u;
            oa = zero16(); ob = zero16();
            float ma = -INFINITY, mb = -INFINITY;
            for (int t = 0; t < 64; ++t) {
                const char* tb = kvimg + (size_t)t * KV_TILE_B;
                const char* kb = kvimg + (size_t)((t + 63) & 63) * KV_TILE_B;
                const bf16x8 k1 = *(const bf16x8*)(kb + k1a), k2 = kfrag2(*(const u32x2*)(kb + k2a), kone);
                const bf16x8 v0 = *(const bf16x8*)(tb + va), v1 = *(const bf16x8*)(tb + va + 672);
                f32x16 sa = MFMA(k1, BC(qa1), zero16()); sa = MFMA(k2, BC(qa2), sa);
                f32x16 sb = MFMA(k1, BC(qb1), zero16()); sb = MFMA(k2, BC(qb2), sb);
                float ta = ma, tb_ = mb;
#pragma unroll
                for (int r = 0; r < 16; ++r) { ta = __builtin_fmaxf(ta, sa[r]); tb_ = __builtin_fmaxf(tb_, sb[r]); }
                ta = xhalf_max(ta); tb_ = xhalf_max(tb_);
                const float fa = __builtin_amdgcn_exp2f(ma - ta), fb = __builtin_amdgcn_exp2f(mb - tb_);
                ma = ta; mb = tb_;
#pragma unroll
                for (int r = 0; r < 16; ++r) { oa[r] *= fa; ob[r] *= fb; sa[r] = __builtin_amdgcn_exp2f(sa[r] - ma); sb[r] = __builtin_amdgcn_exp2f(sb[r] - mb); }
                u32x4 pa0, pa1, pb0, pb1;
#pragma unroll
                for (int j = 0; j < 4; ++j) { pa0[j] = cvtpk(sa[2 * j], sa[2 * j + 1]); pa1[j] = cvtpk(sa[8 + 2 * j], sa[9 + 2 * j]); pb0[j] = cvtpk(sb[2 * j], sb[2 * j + 1]); pb1[j] = cvtpk(sb[8 + 2 * j], sb[9 + 2 * j]); }
                oa = MFMA(v0, BC(pa0), oa); oa = MFMA(v1, BC(pa1), oa);
                ob = MFMA(v0, BC(pb0), ob); ob = MFMA(v1, BC(pb1), ob);
            }
        }
    }
    const float la = __shfl(oa[8], r32 + 32), lb = __shfl(ob[8], r32 + 32);
    const float fa = ga * __builtin_amdgcn_rcpf(la), fb = gb * __builtin_amdgcn_rcpf(lb);
    const int head = 4 * c + w;
    const __amdgpu_buffer_rsrc_t rs = __builtin_amdgcn_make_buffer_rsrc(p.ws, 0, (int)WS_END, 0x00020000);
    const unsigned ob0 = (unsigned)WS_O + (unsigned)(rb * 80 + (head >> 3) * 10) * 1024;
    st16_dev(rs, ob0 + (head & 7) * 1024 + lane * 16, (u32x4){cvtpk(oa[0] * fa, oa[1] * fa), cvtpk(oa[2] * fa, oa[3] * fa), cvtpk(oa[4] * fa, oa[5] * fa), cvtpk(oa[6] * fa, oa[7] * fa)});
    st16_dev(rs, ob0 + 40960 + (head & 7) * 1024 + lane * 16, (u32x4){cvtpk(ob[0] * fb, ob[1] * fb), cvtpk(ob[2] * fb, ob[3] * fb), cvtpk(ob[4] * fb, ob[5] * fb), cvtpk(ob[6] * fb, ob[7] * fb)});
    {
        const u32x2 ua = {cvtpk(oa[8] * fa, oa[9] * fa), cvtpk(oa[10] * fa, oa[11] * fa)}, ub = {cvtpk(ob[8] * fb, ob[9] * fb), cvtpk(ob[10] * fb, ob[11] * fb)};
        const unsigned s0 = (unsigned)__shfl((int)ub[0], r32), s1 = (unsigned)__shfl((int)ub[1], r32);
        const u32x2 uv = {h ? s0 : ua[0], h ? s1 : ua[1]};
        st8_dev(p.ws + ob0 + h * 40960 + (8 + ((head >> 2) & 1)) * 1024 + ((head & 1) * 32 + r32) * 16 + ((head >> 1) & 1) * 8, uv);
    }
    WAIT_VM(0);
    if (lane == 0) flag_set(p.ws, WS_OF + (rb * 4 + (head >> 3)) * 128 + (head & 7) * 8, tag);
    STAMP(1, 4);
    if (blockIdx.x == 0 && w == 0) {
        flags_wait<8, 32>(p.ws + WS_SEEN, 512, tag, lane);
        if (lane == 0) flag_set(p.ws, WS_EPOCH, tag);
    }
#undef KV_DMA
#undef Q_DMA
}

DI u64 ffn_up_wg(const Params& p, int rb2, int fs, u64 ep, char* lds) {
    const int tid = threadIdx.x, lane = tid & 63, w = __builtin_amdgcn_readfirstlane(tid >> 6);
    const int r32 = lane & 31, h = lane >> 5;
    const unsigned lds0 = (unsigned)(uintptr_t)lds;
    constexpr int XS_B = 16384, WS_B = 12288, WS_0 = 2 * XS_B;
    const int xrow = 8 * w + (lane >> 3);
    const unsigned xo = xrow * (D_ * 2) + (((lane & 7) ^ ((xrow >> 1) & 7)) << 4);
    unsigned wo_[3];
#pragma unroll
    for (int j = 0; j < 3; ++j) { const int idx = (w + 4 * j) * 64 + lane, r = idx % 384; wo_[j] = (r / 12) * (F_ * 2) + ((r % 12) ^ (idx >= 384 ? 2 : 0)) * 16; }
    const u16* xsrc = p.x + (size_t)rb2 * 128 * D_;
    const u16* gsrc = p.wg + fs * 96;
    const u16* usrc = p.wu + fs * 96;
    const u16* w1src = (w < 2) ? gsrc : usrc;
    const unsigned dw = lds0 + w * 1024;
#define UP_X(kb, m) glds16(xsrc + (kb) * 64 + (m) * 32 * D_, xo, dw + ((kb) & 1) * XS_B + (m) * 4096)
#define UP_W0(s) glds16(gsrc + (size_t)(s) * 32 * F_, wo_[0], dw + WS_0 + ((s) & 3) * WS_B)
#define UP_W1(s) glds16(w1src + (size_t)(s) * 32 * F_, wo_[1], dw + WS_0 + ((s) & 3) * WS_B + 4096)
#define UP_W2(s) glds16(usrc + (size_t)(s) * 32 * F_, wo_[2], dw + WS_0 + ((s) & 3) * WS_B + 8192)
#define UP_XA(kb) do { UP_X(kb, 0); UP_X(kb, 1); UP_X(kb, 2); UP_X(kb, 3); } while (0)
#define UP_WA(s) do { UP_W0(s); UP_W1(s); UP_W2(s); } while (0)
#define X_UPPRIO 1
#define X_UPPRIO0 3
    __builtin_amdgcn_s_setprio(X_UPPRIO0);
    STAMP(2, 1);
    UP_XA(0); UP_WA(0); UP_WA(1); UP_XA(1); UP_WA(2);
    __builtin_amdgcn_s_setprio(X_UPPRIO);
    const u64 tag = epoch_tag(ep);
    f32x16 acc[6];
#pragma unroll
    for (int i = 0; i < 6; ++i) acc[i] = zero16();
    const int wr = w >> 1, wc = w & 1, row = 64 * wr + r32, blk = (lane >> 4) & 1;
    lds_cptr xa[4], wa[3];
#pragma unroll
    for (int kk = 0; kk < 4; ++kk) xa[kk] = (lds_cptr)lds + row * 128 + (((2 * kk + h) ^ ((row >> 1) & 7)) << 4);
#pragma unroll
    for (int i = 0; i < 3; ++i) wa[i] = (lds_cptr)lds + WS_0 + blk * 6144 + (8 * h + ((lane & 15) >> 2)) * 192 + ((3 * wc + i) ^ blk) * 32 + (lane & 3) * 8;
    bf16x8 xf0[2], xf1[2], wf0[3], wf1[3];
#define UP_RX(q, rt) ldsread16(xa[(q) & 3] + (((q) >> 2) & 1) * XS_B + (rt) * 4096)
#define UP_RW(q, i) cat8(trread(wa[i] + (((q) >> 1) & 3) * WS_B + ((q) & 1) * 3072), trread(wa[i] + (((q) >> 1) & 3) * WS_B + ((q) & 1) * 3072 + 768))
#define UP_STEP(C, N, q, D0, D1, D2, D3, D4, D5) do { \
        acc[0] = MFMA(wf##C[0], xf##C[0], acc[0]); xf##N[0] = UP_RX(q, 0); D0; SBAR(); PACE(); \
        acc[3] = MFMA(wf##C[0], xf##C[1], acc[3]); wf##N[0] = UP_RW(q, 0); D1; SBAR(); PACE(); \
        acc[1] = MFMA(wf##C[1], xf##C[0], acc[1]); xf##N[1] = UP_RX(q, 1); D2; SBAR(); PACE(); \
        acc[4] = MFMA(wf##C[1], xf##C[1], acc[4]); wf##N[1] = UP_RW(q, 1); D3; SBAR(); PACE(); \
        acc[2] = MFMA(wf##C[2], xf##C[0], acc[2]); wf##N[2] = UP_RW(q, 2); D4; SBAR(); PACE(); \
        acc[5] = MFMA(wf##C[2], xf##C[1], acc[5]); D5; SBAR(); PACE(); } while (0)
#define NOP_ do {} while (0)
    WAIT_BAR(10);
    STAMP(2, 9);
    if (tid == 0) flag_set(p.ws, WS_SEEN + blockIdx.x * 8, tag);
    xf0[0] = UP_RX(0, 0); xf0[1] = UP_RX(0, 1);
#pragma unroll
    for (int i = 0; i < 3; ++i) wf0[i] = UP_RW(0, i);
    for (int it = 0; it < 4; ++it) {
        const int s0 = 4 * it, kb = 2 * it;
        UP_STEP(0, 1, 1, NOP_, UP_W0(s0 + 3), NOP_, UP_W1(s0 + 3), NOP_, UP_W2(s0 + 3));
        WAIT_BAR(10);
        UP_STEP(1, 0, 2, UP_W0(s0 + 4), NOP_, NOP_, UP_W1(s0 + 4), NOP_, NOP_);
        UP_STEP(0, 1, 3, UP_W2(s0 + 4), NOP_, NOP_, NOP_, NOP_, NOP_);
        WAIT_BAR(6);
        UP_STEP(1, 0, 4, UP_X(kb + 2, 0), UP_X(kb + 2, 1), NOP_, UP_X(kb + 2, 2), NOP_, UP_X(kb + 2, 3));
        UP_STEP(0, 1, 5, NOP_, UP_W0(s0 + 5), NOP_, UP_W1(s0 + 5), NOP_, UP_W2(s0 + 5));
        WAIT_BAR(10);
        UP_STEP(1, 0, 6, UP_W0(s0 + 6), NOP_, NOP_, UP_W1(s0 + 6), NOP_, NOP_);
        UP_STEP(0, 1, 7, UP_W2(s0 + 6), NOP_, NOP_, NOP_, NOP_, NOP_);
        WAIT_BAR(6);
        UP_STEP(1, 0, 0, UP_X(kb + 3, 0), UP_X(kb + 3, 1), NOP_, UP_X(kb + 3, 2), NOP_, UP_X(kb + 3, 3));
        STAMP(2, 10 + it);
    }
    UP_STEP(0, 1, 1, UP_W0(19), UP_W1(19), UP_W2(19), NOP_, NOP_, NOP_);
    WAIT_BAR(3);
    UP_STEP(1, 0, 2, NOP_, NOP_, NOP_, NOP_, NOP_, NOP_);
    UP_STEP(0, 1, 3, NOP_, NOP_, NOP_, NOP_, NOP_, NOP_);
    UP_STEP(1, 0, 4, NOP_, NOP_, NOP_, NOP_, NOP_, NOP_);
    UP_STEP(0, 1, 5, NOP_, NOP_, NOP_, NOP_, NOP_, NOP_);
    WAIT_BAR(0);
    UP_STEP(1, 0, 6, NOP_, NOP_, NOP_, NOP_, NOP_, NOP_);
    UP_STEP(0, 1, 7, NOP_, NOP_, NOP_, NOP_, NOP_, NOP_);
#pragma unroll
    for (int i = 0; i < 6; ++i) acc[i] = MFMA(wf1[i % 3], xf1[i / 3], acc[i]);
    __builtin_amdgcn_s_setprio(3);
    STAMP(2, 2);
    const __amdgpu_buffer_rsrc_t rs = __builtin_amdgcn_make_buffer_rsrc(p.ws, 0, (int)WS_END, 0x00020000);
    const unsigned ho = (unsigned)WS_H + (unsigned)((rb2 * 4 + 2 * wr) * 96 + fs * 6 + 3 * wc) * 1024 + lane * 16;
#pragma unroll
    for (int j = 0; j < 6; ++j) {
        float hv[8];
#pragma unroll
        for (int r = 0; r < 8; ++r) hv[r] = __builtin_fmaxf(acc[j][r], 0.f) * acc[j][8 + r];
        st16_dev(rs, ho + (j / 3) * 96 * 1024 + (j % 3) * 1024, (u32x4){cvtpk(hv[0], hv[1]), cvtpk(hv[2], hv[3]), cvtpk(hv[4], hv[5]), cvtpk(hv[6], hv[7])});
    }
    WAIT_BAR(0);
    if (tid == 0) flag_set(p.ws, WS_HF + (rb2 * 4 + (fs >> 2)) * 128 + (fs & 3) * 8, tag);
    STAMP(2, 3);
    return tag;
}

#define MFMA16(a, b, c) __builtin_amdgcn_mfma_f32_16x16x32_bf16((a), (b), (c), 0, 0, 0)
DI void ld16_dev(bf16x8& d0, bf16x8& d1, const char* sbase, unsigned voff) {
    asm volatile("global_load_dwordx4 %0, %2, %3 sc1\n\tglobal_load_dwordx4 %1, %2, %3 offset:256 sc1" : "=&v"(d0), "=&v"(d1) : "v"(voff), "s"(sbase) : "memory");
}
DI void out_wg(const Params& p, int rb, int cn, u64 tag, char* lds) {
    const int tid = threadIdx.x, lane = tid & 63, w = __builtin_amdgcn_readfirstlane(tid >> 6);
    const int r16 = lane & 15, g = lane >> 4;
    const unsigned lds0 = (unsigned)(uintptr_t)lds + w * 20480;
    unsigned wo_[5], wm_[5], wl_[5];
#pragma unroll
    for (int j = 0; j < 5; ++j) {
        const int idx = j * 64 + lane, r = idx / 10, ch = (idx % 10) * 16;
        wo_[j] = r * (D_ * 2) + ch; wm_[j] = ((r >> 4) * 20 + (r & 15)) * (D_ * 2) + ch; wl_[j] = ((r >> 2) * 20 + 16 + (r & 3)) * (D_ * 2) + ch;
    }
    const u16* dsrc = p.wd + (size_t)w * 384 * D_ + cn * 80;
    const u16* osrc = p.wo + (size_t)w * 160 * D_ + cn * 80;
    const char* hsrc = p.ws + WS_H + ((size_t)(rb * 2) * 48 + w * 12) * 2048;
    const char* asrc = p.ws + WS_O + ((size_t)(rb * 2) * 40 + w * 10) * 1024;
    const unsigned lo16 = (g >> 1) * 1024 + (g & 1) * 512 + r16 * 16;
#define W_PIECES(s_, o_, d_) do { glds16(s_, o_[0], d_); glds16(s_, o_[1], d_ + 1024); glds16(s_, o_[2], d_ + 2048); glds16(s_, o_[3], d_ + 3072); glds16(s_, o_[4], d_ + 4096); } while (0)
#define W_DMA(u) do { const unsigned d_ = lds0 + ((u) & 3) * 5120; \
        if ((u) < 12) W_PIECES(dsrc + (size_t)(u) * 32 * D_, wo_, d_); else if ((u) < 16) W_PIECES(osrc + (size_t)((u) - 12) * 40 * D_, wm_, d_); else W_PIECES(osrc, wl_, d_); } while (0)
    asm volatile("" :: "v"(wo_[0]), "v"(wo_[1]), "v"(wo_[2]), "v"(wo_[3]), "v"(wo_[4]), "v"(wm_[0]), "v"(wm_[1]), "v"(wm_[2]), "v"(wm_[3]), "v"(wm_[4]), "v"(wl_[0]), "v"(wl_[1]), "v"(wl_[2]), "v"(wl_[3]), "v"(wl_[4]));
    __builtin_amdgcn_s_setprio(1);
    W_DMA(0); W_DMA(1); W_DMA(2); W_DMA(3);
    f32x4 acc[4][5];
#pragma unroll
    for (int i = 0; i < 4; ++i)
#pragma unroll
        for (int j = 0; j < 5; ++j) acc[i][j] = (f32x4){0.f, 0.f, 0.f, 0.f};
    const lds_cptr wbase = (lds_cptr)lds + w * 20480 + (16 * (g >> 1) + 4 * (g & 1) + (r16 >> 2)) * 160 + (lane & 3) * 8;
    bf16x8 wf[2][5];
    bf16x8 af[5][4];
#define W_RD(u, i) wf[(u) & 1][i] = cat8(trread(wbase + ((u) & 3) * 5120 + (i) * 32), trread(wbase + ((u) & 3) * 5120 + (i) * 32 + 1280))
#define H_LD(u) do { ld16_dev(af[(u) & 3][0], af[(u) & 3][1], hsrc + (u) * 2048, lo16); ld16_dev(af[(u) & 3][2], af[(u) & 3][3], hsrc + 48 * 2048 + (u) * 2048, lo16); } while (0)
#define W_PIECE(u, i) glds16((u) < 12 ? (const void*)(dsrc + (size_t)(u) * 32 * D_) : (const void*)(osrc + (size_t)((u) - 12) * 40 * D_), (u) < 12 ? wo_[i] : wm_[i], lds0 + ((u) & 3) * 5120 + (i) * 1024)
#define UNIT_D(u, A) do { _Pragma("unroll") for (int i = 0; i < 5; ++i) { \
        acc[0][i] = MFMA16(wf[(u) & 1][i], A[0], acc[0][i]); acc[1][i] = MFMA16(wf[(u) & 1][i], A[1], acc[1][i]); W_PIECE((u) + 4, i); SBAR(); \
        acc[2][i] = MFMA16(wf[(u) & 1][i], A[2], acc[2][i]); acc[3][i] = MFMA16(wf[(u) & 1][i], A[3], acc[3][i]); W_RD((u) + 1, i); \
        if ((u) + 3 < 12 && i == 1) ld16_dev(af[((u) + 3) & 3][0], af[((u) + 3) & 3][1], hsrc + ((u) + 3) * 2048, lo16); \
        if ((u) + 3 < 12 && i == 3) ld16_dev(af[((u) + 3) & 3][2], af[((u) + 3) & 3][3], hsrc + 48 * 2048 + ((u) + 3) * 2048, lo16); \
        SBAR(); } } while (0)
#define UNIT(u, A) do { _Pragma("unroll") for (int i = 0; i < 5; ++i) { \
        _Pragma("unroll") for (int rg = 0; rg < 4; ++rg) acc[rg][i] = MFMA16(wf[(u) & 1][i], A[rg], acc[rg][i]); \
        if ((u) + 1 < 17) W_RD((u) + 1, i); SBAR(); } } while (0)
#define PINA(A) "+v"(A[0]), "+v"(A[1]), "+v"(A[2]), "+v"(A[3])
    flags_wait<1, 32>(p.ws + WS_KVF + (blockIdx.x & 15) * 1024 + (rb * 4) * 8, 4, tag, lane);
    flags_wait<1, 16>(p.ws + WS_HF + ((rb >> 1) * 4 + w) * 128, 4, tag, lane);
    STAMP(2, 4);
    float gm[4];
#pragma unroll
    for (int rg = 0; rg < 4; ++rg) gm[rg] = __uint_as_float(cvtpk(1.f - ld4_dev(p.ws + WS_G + (rb * 64 + rg * 16 + r16) * 4), 0.f) << 16);
    asm volatile("s_waitcnt vmcnt(0)" :: "v"(gm[0]), "v"(gm[1]), "v"(gm[2]), "v"(gm[3]) : "memory");
    H_LD(0); H_LD(1); H_LD(2);
#pragma unroll
    for (int i = 0; i < 5; ++i) W_RD(0, i);
    __builtin_amdgcn_s_setprio(1);
#pragma unroll
    for (int u = 0; u < 12; ++u) {
        if (u == 0) asm volatile("s_waitcnt vmcnt(8) lgkmcnt(0)" : PINA(af[0]) :: "memory");
        else if (u == 1) asm volatile("s_waitcnt vmcnt(13) lgkmcnt(0)" : PINA(af[1]) :: "memory");
        else if (u <= 9) asm volatile("s_waitcnt vmcnt(18) lgkmcnt(0)" : PINA(af[u & 3]) :: "memory");
        else if (u == 10) asm volatile("s_waitcnt vmcnt(14) lgkmcnt(0)" : PINA(af[u & 3]) :: "memory");
        else asm volatile("s_waitcnt vmcnt(10) lgkmcnt(0)" : PINA(af[u & 3]) :: "memory");
        SBAR();
        UNIT_D(u, af[u & 3]);
    }
    __builtin_amdgcn_s_setprio(3);
    asm volatile("s_waitcnt lgkmcnt(0)" ::: "memory");
    W_DMA(16);
#pragma unroll
    for (int rg = 0; rg < 4; ++rg)
#pragma unroll
        for (int i = 0; i < 5; ++i) acc[rg][i] *= gm[rg];
    STAMP(2, 5);
    flags_wait<1, 6>(p.ws + WS_OF + (rb * 4 + w) * 128, 8, tag, lane);
    STAMP(2, 6);
#pragma unroll
    for (int v = 0; v < 5; ++v) { ld16_dev(af[v][0], af[v][1], asrc + v * 2048, lo16); ld16_dev(af[v][2], af[v][3], asrc + 40 * 1024 + v * 2048, lo16); }
    asm volatile("s_waitcnt vmcnt(16)" : PINA(af[0]) :: "memory"); SBAR(); UNIT(12, af[0]);
    asm volatile("s_waitcnt vmcnt(12)" : PINA(af[1]) :: "memory"); SBAR(); UNIT(13, af[1]);
    asm volatile("s_waitcnt vmcnt(8)" : PINA(af[2]) :: "memory"); SBAR(); UNIT(14, af[2]);
    asm volatile("s_waitcnt vmcnt(4)" : PINA(af[3]) :: "memory"); SBAR(); UNIT(15, af[3]);
    asm volatile("s_waitcnt vmcnt(0)" : PINA(af[4]) :: "memory"); SBAR(); UNIT(16, af[4]);
    STAMP(2, 7);
    LAS f32x4* mine = (LAS f32x4*)((lds_ptr)lds + w * 20480);
#pragma unroll
    for (int rg = 0; rg < 4; ++rg)
#pragma unroll
        for (int i = 0; i < 5; ++i) mine[(rg * 5 + i) * 64 + lane] = acc[rg][i];
    WAIT_BAR(0);
    const LAS f32x4* all = (const LAS f32x4*)lds + w * 5 * 64 + lane;
    u16* orow = p.out + (size_t)(rb * 64 + w * 16 + r16) * D_ + cn * 80 + 4 * g;
#pragma unroll
    for (int i = 0; i < 5; ++i) {
        const f32x4 s = (all[i * 64] + all[1280 + i * 64]) + (all[2560 + i * 64] + all[3840 + i * 64]);
        st8_dev(orow + 16 * i, (u32x2){cvtpk(s[0], s[1]), cvtpk(s[2], s[3])});
    }
    STAMP(2, 8);
}

__global__ __launch_bounds__(256, 2) void k_fused(Params p) {
    extern __shared__ __attribute__((aligned(16))) char lds[];
    const int b = blockIdx.x, j = b & 7, i = (b >> 3) & 31;
    const u64 ep = epoch_load(p.ws + WS_EPOCH);
    if (b < 256) {
#define X_APRIO 2
        __builtin_amdgcn_s_setprio(X_APRIO);
        attn_wg(p, 16 * (j >> 2) + (i >> 1), 2 * (j & 3) + (i & 1), ep, lds);
    } else {
        STAMP(2, 0);
        if (i < 16) { __builtin_amdgcn_s_setprio(3); kv_job(p, 8 * j + (i >> 1), i & 1, ep, lds); __builtin_amdgcn_s_setprio(1); }
        const u64 tag = ffn_up_wg(p, 8 * (j >> 2) + (i >> 2), 4 * (j & 3) + (i & 3), ep, lds);
        out_wg(p, 4 * j + (i >> 3), i & 7, tag, lds);
    }
}

extern "C" void kernel_launch(void* const* d_in, const int* in_sizes, int n_in, void* d_out, int out_size, void* d_ws, size_t ws_size,
                              hipStream_t stream) {
    Params p;
    p.x = (const u16*)d_in[0]; p.wq = (const u16*)d_in[1]; p.wk = (const u16*)d_in[2]; p.wv = (const u16*)d_in[3]; p.wo = (const u16*)d_in[4];
    p.wg = (const u16*)d_in[6]; p.wu = (const u16*)d_in[7]; p.wd = (const u16*)d_in[8]; p.gate_w = (const u16*)d_in[9];
    p.out = (u16*)d_out; p.ws = (char*)d_ws;
    (void)hipFuncSetAttribute((const void*)k_fused, hipFuncAttributeMaxDynamicSharedMemorySize, LDS_BYTES);
    k_fused<<<512, 256, LDS_BYTES, stream>>>(p);
}
```

```cpp
#include <hip/hip_runtime.h>
#include <hip/hip_bf16.h>
#include <stdint.h>

typedef __attribute__((ext_vector_type(8))) short bf16x8;
typedef __attribute__((ext_vector_type(4))) short s16x4;
typedef __attribute__((ext_vector_type(16))) float f32x16;
typedef __attribute__((ext_vector_type(4))) float f32x4;
typedef __attribute__((ext_vector_type(2))) float f32x2;
typedef __attribute__((ext_vector_type(4))) unsigned u32x4;
typedef __attribute__((ext_vector_type(2))) unsigned u32x2;
typedef __attribute__((ext_vector_type(2))) __bf16 bf16x2_t;
typedef short v4i16_t __attribute__((ext_vector_type(4)));
typedef unsigned short u16;
typedef unsigned long long u64;
#define LAS __attribute__((address_space(3)))
#define GAS __attribute__((address_space(1)))
typedef LAS const char* lds_cptr;
typedef LAS char* lds_ptr;
#define DI __device__ __forceinline__

constexpr int D_ = 640, F_ = 1536;
constexpr int KV_TILE_B = 2624;
constexpr int KV_CHUNK_B = 4 * KV_TILE_B;
constexpr int KV_SLOT_B = 12288;
constexpr size_t WS_EPOCH = 0, WS_SEEN = 1024, WS_HF = 8192, WS_KVF = 16384, WS_OF = 32768;
constexpr size_t WS_KV = 64 * 1024;
constexpr size_t WS_G = 240 * 1024;
constexpr size_t WS_H = 256 * 1024;
constexpr size_t WS_O = WS_H + (size_t)64 * 48 * 2048;
constexpr size_t WS_END = WS_O + (size_t)64 * 40 * 1024;
constexpr int LDS_BYTES = 80 * 1024;
constexpr unsigned SPIN_CAP = 1u << 18;

#define MFMA(a, b, c) __builtin_amdgcn_mfma_f32_32x32x16_bf16((a), (b), (c), 0, 0, 0)
#define SBAR() __builtin_amdgcn_sched_barrier(0)
#define WAIT_VM(N) asm volatile("s_waitcnt vmcnt(" #N ")" ::: "memory")
#define WAIT_BAR(N) asm volatile("s_waitcnt vmcnt(" #N ") lgkmcnt(0)\n\ts_barrier" ::: "memory")
#define RLX_AGENT __ATOMIC_RELAXED, __HIP_MEMORY_SCOPE_AGENT
#define BC(x) __builtin_bit_cast(bf16x8, x)

DI unsigned cvtpk(float lo, float hi) {
    f32x2 v = {lo, hi};
    return __builtin_bit_cast(unsigned, __builtin_convertvector(v, bf16x2_t));
}
DI void glds16(const void* sbase, unsigned voff, unsigned lds_base) {
    unsigned sv;
    asm volatile("s_mov_b32 %0, m0\n\ts_mov_b32 m0, %3\n\ts_nop 0\n\tglobal_load_lds_dwordx4 %1, %2\n\ts_mov_b32 m0, %0"
                 : "=&s"(sv) : "v"(voff), "s"(sbase), "s"(lds_base) : "memory");
}
DI void glds16v(const void* ptr, unsigned lds_base) {
    unsigned sv;
    asm volatile("s_mov_b32 %0, m0\n\ts_mov_b32 m0, %2\n\ts_nop 0\n\tglobal_load_lds_dwordx4 %1, off\n\ts_mov_b32 m0, %0"
                 : "=&s"(sv) : "v"(ptr), "s"(lds_base) : "memory");
}
DI void glds16_dev(const void* sbase, unsigned voff, unsigned lds_base) {
    unsigned sv;
    asm volatile("s_mov_b32 %0, m0\n\ts_mov_b32 m0, %3\n\ts_nop 0\n\tglobal_load_lds_dwordx4 %1, %2 sc1\n\ts_mov_b32 m0, %0"
                 : "=&s"(sv) : "v"(voff), "s"(sbase), "s"(lds_base) : "memory");
}
DI s16x4 trread(lds_cptr p) { return __builtin_bit_cast(s16x4, __builtin_amdgcn_ds_read_tr16_b64_v4i16((LAS v4i16_t*)p)); }
DI bf16x8 cat8(s16x4 lo, s16x4 hi) { return __builtin_shufflevector(lo, hi, 0, 1, 2, 3, 4, 5, 6, 7); }
DI bf16x8 ldsread16(lds_cptr p) { return *(const LAS bf16x8*)p; }
DI float xhalf_max(float m) {
    auto rr = __builtin_amdgcn_permlane32_swap(__float_as_uint(m), __float_as_uint(m), false, false);
    return __builtin_fmaxf(__uint_as_float(rr[0]), __uint_as_float(rr[1]));
}
DI f32x16 zero16() { f32x16 z; for (int i = 0; i < 16; ++i) z[i] = 0.f; return z; }
DI void st16_dev(__amdgpu_buffer_rsrc_t r, unsigned off, u32x4 v) { __builtin_amdgcn_raw_buffer_store_b128(v, r, off, 0, 16); }
DI void st8_dev(void* p, u32x2 v) { __hip_atomic_store((GAS u64*)p, (u64)v[0] | ((u64)v[1] << 32), RLX_AGENT); }
DI void st4_dev(void* p, float v) { __hip_atomic_store((GAS unsigned*)p, __float_as_uint(v), RLX_AGENT); }
DI u64 ld8_dev(const void* p) { return __hip_atomic_load((const GAS u64*)p, RLX_AGENT); }
DI float ld4_dev(const void* p) { return __uint_as_float(__hip_atomic_load((const GAS unsigned*)p, RLX_AGENT)); }
DI void flag_set(char* ws, size_t off, u64 tag) { __hip_atomic_store((GAS u64*)(ws + off), tag, RLX_AGENT); }
DI u64 epoch_load(const char* ws) { u64 e; asm volatile("s_load_dwordx2 %0, %1, 0x0" : "=s"(e) : "s"(ws) : "memory"); return e; }
DI u64 epoch_tag(u64 e) { u64 r; asm volatile("s_waitcnt lgkmcnt(0)\n\ts_mov_b64 %0, %1" : "=s"(r) : "s"(e) : "memory"); return r * 6364136223846793005ull + 1442695040888963407ull; }
template <int K, int SLEEP> DI void flags_wait(const char* f, int n, u64 tag, int lane) {
#pragma unroll 1
    for (unsigned spin = 0; spin < SPIN_CAP; ++spin) {
        bool ok = true;
#pragma unroll
        for (int k = 0; k < K; ++k) { const int i = lane + 64 * k; const u64 v = ld8_dev(f + 8 * (i < n ? i : 0)); ok &= (v == tag); }
        if (__all(ok)) break;
        __builtin_amdgcn_s_sleep(SLEEP);
    }
}

struct Params {
    const u16* x; const u16* wq; const u16* wk; const u16* wv; const u16* wo;
    const u16* wg; const u16* wu; const u16* wd; const u16* gate_w;
    u16* out; char* ws;
};

DI void kv_job(const Params& p, int rt, int isv, u64 ep, char* lds) {
    const int tid = threadIdx.x, lane = tid & 63, w = __builtin_amdgcn_readfirstlane(tid >> 6);
    const int r32 = lane & 31, h = lane >> 5;
    const unsigned lds0 = (unsigned)(uintptr_t)lds;
    const unsigned xoff = w * 17408, woff = xoff + 10240;
    const u16* xs = p.x + (size_t)rt * 32 * D_ + w * 160;
#pragma unroll
    for (int i = 0; i < 10; ++i) {
        const int idx = i * 64 + lane, row = idx / 20, ch = (idx % 20) ^ ((row >> 2) & 3);
        glds16(xs, (unsigned)(row * (D_ * 2) + ch * 16), lds0 + xoff + i * 1024);
    }
    const u16* wsrc = (isv ? p.wv : p.wk);
#pragma unroll
    for (int i = 0; i < 6; ++i) glds16(wsrc, (unsigned)(w * 6400 + i * 1024 + lane * 16), lds0 + woff + i * 1024);
    glds16v(lane < 16 ? (const char*)wsrc + w * 6400 + 6144 + lane * 16 : (const char*)p.gate_w + w * 320 + (lane < 36 ? lane - 16 : 0) * 16, lds0 + woff + 6144);
    const u64 tag = epoch_tag(ep);
    WAIT_VM(0);
    const lds_cptr xb0 = (lds_cptr)lds + xoff + r32 * 320 + ((h ^ ((r32 >> 2) & 3)) << 4), xb1 = (lds_cptr)lds + xoff + r32 * 320 + (((2 + h) ^ ((r32 >> 2) & 3)) << 4);
    const lds_cptr wb = (lds_cptr)lds + woff + (8 * h + ((lane & 15) >> 2)) * 40 + ((lane >> 4) & 1) * 32 + (lane & 3) * 8;
    const lds_cptr gb = (lds_cptr)lds + woff + 6400 + h * 16;
    bf16x8 xf[10], wf[10], gf[10];
#pragma unroll
    for (int ks = 0; ks < 10; ++ks) { xf[ks] = ldsread16(((ks & 1) ? xb1 : xb0) + (ks >> 1) * 64); wf[ks] = cat8(trread(wb + ks * 640), trread(wb + ks * 640 + 160)); gf[ks] = ldsread16(gb + ks * 32); }
    SBAR();
    f32x16 acc = zero16();
    if (isv) {
#pragma unroll
        for (int ks = 0; ks < 10; ++ks) acc = MFMA(xf[ks], wf[ks], acc);
    } else {
#pragma unroll
        for (int ks = 0; ks < 10; ++ks) acc = MFMA((r32 == 20) ? gf[ks] : wf[ks], xf[ks], acc);
    }
    LAS f32x4* red = (LAS f32x4*)((lds_ptr)lds + 69632);
    if (w > 0) {
#pragma unroll
        for (int g = 0; g < 4; ++g) red[((w - 1) * 4 + g) * 64 + lane] = (f32x4){acc[4 * g], acc[4 * g + 1], acc[4 * g + 2], acc[4 * g + 3]};
    }
    WAIT_BAR(0);
    if (w == 0) {
#pragma unroll
        for (int ww = 0; ww < 3; ++ww)
#pragma unroll
            for (int g = 0; g < 4; ++g) { const f32x4 t = red[(ww * 4 + g) * 64 + lane]; acc[4 * g] += t[0]; acc[4 * g + 1] += t[1]; acc[4 * g + 2] += t[2]; acc[4 * g + 3] += t[3]; }
    }
    WAIT_BAR(0);
    if (w == 0) {
        const __amdgpu_buffer_rsrc_t rs = __builtin_amdgcn_make_buffer_rsrc(p.ws, 0, (int)WS_END, 0x00020000);
        const unsigned tile = (unsigned)WS_KV + (unsigned)(isv ? rt : ((rt + 63) & 63)) * KV_TILE_B;
        u32x4 lo = {cvtpk(acc[0], acc[1]), cvtpk(acc[2], acc[3]), cvtpk(acc[4], acc[5]), cvtpk(acc[6], acc[7])};
        u32x4 hi = {cvtpk(acc[8], acc[9]), cvtpk(acc[10], acc[11]), cvtpk(acc[12], acc[13]), cvtpk(acc[14], acc[15])};
        if (!isv) {
            st16_dev(rs, tile + lane * 16, lo);
            if (h == 0) st8_dev(p.ws + tile + 1024 + r32 * 8, (u32x2){hi[0], hi[1]});
            else { const float lg = __uint_as_float(cvtpk(acc[8], 0.f) << 16);
                   st4_dev(p.ws + WS_G + (rt * 32 + r32) * 4, __uint_as_float(cvtpk(1.f / (1.f + __expf(-lg)), 0.f) << 16)); }
        } else {
            if (r32 == 20) { lo = (u32x4){0x3f803f80u, 0x3f803f80u, 0x3f803f80u, 0x3f803f80u}; hi = lo; }
            if (r32 <= 20) { st16_dev(rs, tile + 1280 + h * 336 + r32 * 16, lo); st16_dev(rs, tile + 1952 + h * 336 + r32 * 16, hi); }
        }
        WAIT_VM(0);
        if (lane < 16) flag_set(p.ws, WS_KVF + lane * 1024 + (rt * 2 + isv) * 8, tag);
    }
}


#define AE(b, i) "v_exp_f32 v[" #b "+" #i "], v[" #b "+" #i "]\n"
#define AC(pb, j, b, i) "v_cvt_pk_bf16_f32 v[" #pb "+" #j "], v[" #b "+" #i "], v[" #b "+" #i "+1]\n"
#define AG2(MA, MB, X, XI, PX, PJ) MA AE(X, XI) AE(X, XI+1) AE(X, XI+2) AE(X, XI+3) AC(PX, PJ, X, XI) AC(PX, PJ+1, X, XI+2) \
    MB AE(X, XI+4) AE(X, XI+5) AE(X, XI+6) AE(X, XI+7) AC(PX, PJ+2, X, XI+4) AC(PX, PJ+3, X, XI+6)
#define AHALF(X, PX, Y, Q1, Q2, O, PR, BUF, R1, R2, R3, R4) \
    AG2("v_mfma_f32_32x32x16_bf16 v[" #Y ":" #Y "+15], v[" #BUF ":" #BUF "+3], " Q1 ", 0\n" R1, \
        "v_mfma_f32_32x32x16_bf16 v[" #Y ":" #Y "+15], v[" #BUF "+4:" #BUF "+7], " Q2 ", v[" #Y ":" #Y "+15]\n" R2, X, 0, PX, 0) \
    AG2("v_mfma_f32_32x32x16_bf16 " O ", v[" #BUF "+8:" #BUF "+11], v[" #PR ":" #PR "+3], " O "\n" R3, \
        "v_mfma_f32_32x32x16_bf16 " O ", v[" #BUF "+12:" #BUF "+15], v[" #PR "+4:" #PR "+7], " O "\n" R4, X, 8, PX, 4)
#define ARK1(NB, sl, tl) "ds_read_b128 v[" #NB ":" #NB "+3], %[k1a] offset:4096+(" #sl ")*12288+" #tl "*2624\n"
#define ARK2(NB, sl, tl) "ds_read_b64 v[" #NB "+4:" #NB "+5], %[k2a] offset:4096+(" #sl ")*12288+" #tl "*2624\n"
#define ARV0(NB, sl, tl) "ds_read_b128 v[" #NB "+8:" #NB "+11], %[va] offset:4096+(" #sl ")*12288+" #tl "*2624\n"
#define ARV1(NB, sl, tl) "ds_read_b128 v[" #NB "+12:" #NB "+15], %[va] offset:4096+(" #sl ")*12288+" #tl "*2624+672\n"
#define AH1(PREV, R1, R2, R3, R4) AHALF(128, 160, 144, "%[qb1]", "%[qb2]", "%[ob]", 168, PREV, R1, R2, R3, R4)
#define AH2(CUR, PREV, sl, tl) AHALF(144, 168, 128, "%[qa1]", "%[qa2]", "%[oa]", 160, CUR, ARK1(PREV, sl, tl), ARK2(PREV, sl, tl), ARV0(PREV, sl, tl), ARV1(PREV, sl, tl))
#define AWL "s_waitcnt lgkmcnt(0)\n"
#define ASYNC "s_waitcnt vmcnt(6) lgkmcnt(0)\ns_barrier\n"
#define ADMA0(fs) "s_mul_i32 %[of], %[ck], 10496\n s_add_u32 %[ck], %[ck], 1\n s_add_u32 m0, %[ldst], " #fs "*12288\n" \
                  "v_add_u32 v208, %[of], %[kvo]\n"
#define ADMA1 "global_load_lds_dwordx4 v208, %[img] sc1\n"
#define ADMA2 "global_load_lds_dwordx4 v208, %[img] offset:1024 sc1\n"
#define ADMA3 "global_load_lds_dwordx4 v208, %[img] offset:2048 sc1\n"
#define ACHUNK(sl, nsl) \
    AH1(192, "", "", "", "") AWL AH2(176, 192, sl, 2) \
    AH1(176, "", "", "", "") AWL AH2(192, 176, sl, 3) \
    AH1(192, "", "", "", "") ASYNC AH2(176, 192, nsl, 0) \
    AH1(176, ADMA0(sl), ADMA1, ADMA2, ADMA3) AWL AH2(192, 176, nsl, 1)
#define ACHUNKT(sl, nsl, W) \
    AH1(192, "", "", "", "") AWL AH2(176, 192, sl, 2) \
    AH1(176, "", "", "", "") AWL AH2(192, 176, sl, 3) \
    AH1(192, "", "", "", "") "s_waitcnt vmcnt(" #W ") lgkmcnt(0)\ns_barrier\n" AH2(176, 192, nsl, 0) \
    AH1(176, "", "", "", "") AWL AH2(192, 176, nsl, 1)
#define ASP1(NB) "ds_read_b128 v[" #NB "+8:" #NB "+11], %[va]\n"
#define ASP2(NB) "ds_read_b128 v[" #NB "+12:" #NB "+15], %[va] offset:672\n"
#define ACHUNKL(sl) \
    AH1(192, "", "", "", "") AWL AH2(176, 192, sl, 2) \
    AH1(176, "", "", "", "") AWL AH2(192, 176, sl, 3) \
    AH1(192, "", "", "", "") AWL AHALF(144, 168, 128, "%[qa1]", "%[qa2]", "%[oa]", 160, 176, ASP1(192), ASP2(192), "", "") \
    AH1(176, "", "", "", "") AWL AHALF(144, 168, 128, "%[qa1]", "%[qa2]", "%[oa]", 160, 192, "", "", "", "")
#define AMOV(d, s) "v_mov_b32 v" #d ", " s "\n"
#define ATTN_LOOP_ASM \
    "s_setprio 2\n s_mov_b32 %[m0s], m0\n s_mov_b32 %[it], 3\n s_mov_b32 %[ck], 4\n" \
    ARK1(176, 1, 1) ARK2(176, 1, 1) ARV0(176, 1, 1) ARV1(176, 1, 1) \
    "v_mov_b64 v[192:193], %[k0a]\n v_mov_b64 v[194:195], %[k0b]\n" \
    "v_mov_b64 v[196:197], %[k02]\n" AMOV(198, "%[kone]") AMOV(199, "0") AMOV(182, "%[kone]") AMOV(183, "0") \
    "v_mfma_f32_32x32x16_bf16 v[128:143], v[192:195], %[qa1], 0\n" \
    AMOV(200, "0") AMOV(201, "0") AMOV(202, "0") AMOV(203, "0") AMOV(204, "0") AMOV(205, "0") AMOV(206, "0") AMOV(207, "0") \
    AMOV(168, "0") AMOV(169, "0") AMOV(170, "0") AMOV(171, "0") AMOV(172, "0") AMOV(173, "0") AMOV(174, "0") AMOV(175, "0") \
    "v_mfma_f32_32x32x16_bf16 v[128:143], v[196:199], %[qa2], v[128:143]\n" \
    "s_nop 7\n s_nop 7\n" \
    "1:\n" \
    ACHUNK(1, 2) ACHUNK(2, 3) ACHUNK(3, 0) ACHUNK(0, 1) \
    "s_sub_u32 %[it], %[it], 1\n s_cmp_lg_u32 %[it], 0\n s_cbranch_scc1 1b\n" \
    ACHUNKT(1, 2, 6) ACHUNKT(2, 3, 3) ACHUNKT(3, 0, 0) ACHUNKL(0) "s_nop 1\n" \
    "v_mfma_f32_32x32x16_bf16 %[ob], v[200:203], v[168:171], %[ob]\n" \
    "v_mfma_f32_32x32x16_bf16 %[ob], v[204:207], v[172:175], %[ob]\n" \
    "s_setprio 3\n s_mov_b32 m0, %[m0s]\n s_nop 7\n s_nop 7\n"
#define ATTN_CLOBBERS "v128","v129","v130","v131","v132","v133","v134","v135","v136","v137","v138","v139","v140","v141","v142","v143", \
    "v144","v145","v146","v147","v148","v149","v150","v151","v152","v153","v154","v155","v156","v157","v158","v159", \
    "v160","v161","v162","v163","v164","v165","v166","v167","v168","v169","v170","v171","v172","v173","v174","v175", \
    "v176","v177","v178","v179","v180","v181","v182","v183","v184","v185","v186","v187","v188","v189","v190","v191", \
    "v192","v193","v194","v195","v196","v197","v198","v199","v200","v201","v202","v203","v204","v205","v206","v207","v208"

DI void attn_wg(const Params& p, int rb, int c, u64 ep, char* lds) {
    const int tid = threadIdx.x, lane = tid & 63, w = __builtin_amdgcn_readfirstlane(tid >> 6);
    const int r32 = lane & 31, h = lane >> 5;
    const unsigned lds0 = (unsigned)(uintptr_t)lds;
    const char* kvimg = p.ws + WS_KV;
    constexpr int RB = 32768;
    const unsigned kvoff = (unsigned)(w * 3072 + lane * 16);
#define KV_DMA(chunk) do { const char* s_ = kvimg + (size_t)(chunk) * KV_CHUNK_B; const unsigned d_ = lds0 + RB + (((chunk) + 1) & 3) * KV_SLOT_B + w * 3072; \
        glds16_dev(s_, kvoff, d_); glds16_dev(s_, kvoff + 1024, d_ + 1024); glds16_dev(s_, kvoff + 2048, d_ + 2048); } while (0)
    constexpr int QS_B = 18432;
    unsigned qoff[5]; unsigned qdst[5];
#pragma unroll
    for (int j = 0; j < 5; ++j) {
        int i = w + 4 * j; if (i >= 18) i -= 2;
        if (j < 2) { const int row = 8 * i + (lane >> 3), pos = lane & 7, ch = pos ^ ((row >> 1) & 7); qoff[j] = row * (D_ * 2) + ch * 16; }
        else { const int idx = (i - 8) * 64 + lane, kr = idx / 10, ch = idx % 10; qoff[j] = kr * (D_ * 2) + ch * 16; }
        qdst[j] = lds0 + i * 1024;
    }
    const u16* xsrc = p.x + (size_t)rb * 64 * D_;
    const u16* wsrc = p.wq + c * 80;
#define Q_DMA(kt) do { const unsigned d_ = ((kt) & 3) * QS_B; const u16* xs_ = xsrc + (kt) * 64; const u16* ws_ = wsrc + (size_t)(kt) * 64 * D_; \
        glds16(xs_, qoff[0], d_ + qdst[0]); glds16(xs_, qoff[1], d_ + qdst[1]); glds16(ws_, qoff[2], d_ + qdst[2]); glds16(ws_, qoff[3], d_ + qdst[3]); glds16(ws_, qoff[4], d_ + qdst[4]); } while (0)
#define Q_PIECE(kt, j) glds16((j) < 2 ? xsrc + (kt) * 64 : wsrc + (size_t)(kt) * 64 * D_, qoff[j], ((kt) & 3) * QS_B + qdst[j])
    Q_DMA(0); Q_DMA(1); Q_DMA(2); Q_DMA(3);
    const u64 tag = epoch_tag(ep);
    f32x16 qa = zero16(), qb = zero16();
    {
        unsigned xa[4];
#pragma unroll
        for (int ks = 0; ks < 4; ++ks) xa[ks] = r32 * 128 + (((2 * ks + h) ^ ((r32 >> 1) & 7)) << 4);
        const unsigned wa = 8192 + (8 * h + ((lane & 15) >> 2)) * 160 + w * 40 + ((lane >> 4) & 1) * 32 + (lane & 3) * 8;
        bf16x8 wfA[4], x0A[4], x1A[4], wfB[4], x0B[4], x1B[4];
#define Q_RD(S, kt, ks) do { const lds_cptr sb_ = (lds_cptr)lds + ((kt) & 3) * QS_B; wf##S[ks] = cat8(trread(sb_ + wa + (ks) * 2560), trread(sb_ + wa + (ks) * 2560 + 640)); \
            x0##S[ks] = ldsread16(sb_ + xa[ks]); x1##S[ks] = ldsread16(sb_ + xa[ks] + 4096); } while (0)
#define Q_STEP(C, N, kt) do { \
            if ((kt) == 7 && w == 0) { flags_wait<2, 4>(p.ws + WS_KVF + (blockIdx.x & 15) * 1024, 128, tag, lane); }     \
            if ((kt) < 7) { WAIT_BAR(10); } else if ((kt) == 7) { WAIT_BAR(5); } else if ((kt) == 8) { WAIT_BAR(9); } \
            if ((kt) == 7) { KV_DMA(0); KV_DMA(1); KV_DMA(2); } \
            _Pragma("unroll") for (int ks = 0; ks < 4; ++ks) { qa = MFMA(wf##C[ks], x0##C[ks], qa); if ((kt) + 1 < 10) Q_RD(N, (kt) + 1, ks); SBAR(); qb = MFMA(wf##C[ks], x1##C[ks], qb); \
                if ((kt) + 4 < 10) { Q_PIECE((kt) + 4, ks); if (ks == 3) Q_PIECE((kt) + 4, 4); } SBAR(); } } while (0)
        WAIT_BAR(15);
        if (tid == 0) flag_set(p.ws, WS_SEEN + blockIdx.x * 8, tag);
#pragma unroll
        for (int ks = 0; ks < 4; ++ks) Q_RD(A, 0, ks);
#pragma unroll
        for (int kt = 0; kt < 10; kt += 2) { Q_STEP(A, B, kt); Q_STEP(B, A, kt + 1); }
    }
    constexpr float C2 = 0.22360679774997896f * 1.4426950408889634f;
    u32x4 qa1, qa2, qb1, qb2;
    qa1 = (u32x4){cvtpk(qa[0] * C2, qa[1] * C2), cvtpk(qa[2] * C2, qa[3] * C2), cvtpk(qa[4] * C2, qa[5] * C2), cvtpk(qa[6] * C2, qa[7] * C2)};
    qb1 = (u32x4){cvtpk(qb[0] * C2, qb[1] * C2), cvtpk(qb[2] * C2, qb[3] * C2), cvtpk(qb[4] * C2, qb[5] * C2), cvtpk(qb[6] * C2, qb[7] * C2)};
    qa2 = (u32x4){h ? 0u : cvtpk(qa[8] * C2, qa[9] * C2), h ? 0u : cvtpk(qa[10] * C2, qa[11] * C2), 0u, 0u};
    qb2 = (u32x4){h ? 0u : cvtpk(qb[8] * C2, qb[9] * C2), h ? 0u : cvtpk(qb[10] * C2, qb[11] * C2), 0u, 0u};
    WAIT_BAR(6);
    glds16_dev(kvimg, (unsigned)(w * 1024 + lane * 16), lds0 + RB - 4096 + w * 1024);
    KV_DMA(3);
    const unsigned k1a = lane * 16, k2a = 1024 + r32 * 8;
    const bf16x8 k01 = ldsread16((lds_cptr)lds + RB + KV_SLOT_B + k1a);
    const u32x2 k02 = *(const LAS u32x2*)((lds_cptr)lds + RB + KV_SLOT_B + k2a);
    const unsigned va = 1280 + h * 336 + (r32 <= 20 ? r32 : (r32 < 28 ? 20 : 16)) * 16;
    const unsigned kone = h ? 0u : 0x3f80u;
    auto kfrag2 = [](u32x2 v, unsigned one) { return __builtin_bit_cast(bf16x8, (u32x4){v[0], v[1], one, 0u}); };
    {
        f32x16 sa = MFMA(k01, BC(qa1), zero16()); sa = MFMA(kfrag2(k02, kone), BC(qa2), sa);
        f32x16 sb = MFMA(k01, BC(qb1), zero16()); sb = MFMA(kfrag2(k02, kone), BC(qb2), sb);
        float ma = sa[0], mb = sb[0];
#pragma unroll
        for (int r = 1; r < 16; ++r) { ma = __builtin_fmaxf(ma, sa[r]); mb = __builtin_fmaxf(mb, sb[r]); }
        ma = xhalf_max(ma); mb = xhalf_max(mb);
        qa2[2] = h ? 0u : (cvtpk(-ma, 0.f) & 0xffffu);
        qb2[2] = h ? 0u : (cvtpk(-mb, 0.f) & 0xffffu);
    }
    f32x16 oa = zero16(), ob = zero16();
    float ga, gb;
    asm volatile("global_load_dword %0, %2, %3 sc1\n\tglobal_load_dword %1, %2, %3 offset:128 sc1" : "=&v"(ga), "=&v"(gb) : "v"(r32 * 4), "s"(p.ws + WS_G + rb * 256) : "memory");
    {
        unsigned t_m0, t_it, t_ck, t_of;
        const u32x4 k0u = __builtin_bit_cast(u32x4, k01); const u32x2 k0a = {k0u[0], k0u[1]}, k0b = {k0u[2], k0u[3]};
        asm volatile(ATTN_LOOP_ASM
            : [oa] "+v"(oa), [ob] "+v"(ob), "+v"(ga), "+v"(gb), [m0s] "=&s"(t_m0), [it] "=&s"(t_it), [ck] "=&s"(t_ck), [of] "=&s"(t_of)
            : [qa1] "v"(qa1), [qa2] "v"(qa2), [qb1] "v"(qb1), [qb2] "v"(qb2), [k1a] "v"(lds0 + RB - 4096 + k1a), [k2a] "v"(lds0 + RB - 4096 + k2a), [va] "v"(lds0 + RB - 4096 + va),
              [kvo] "v"(kvoff), [img] "s"(kvimg), [ldst] "s"(lds0 + RB + w * 3072), [k0a] "v"(k0a), [k0b] "v"(k0b), [k02] "v"(k02), [kone] "v"(kone)
            : ATTN_CLOBBERS, "scc", "memory");
    }
    {
        float chk = 0.f;
#pragma unroll
        for (int r = 0; r < 8; ++r) chk += oa[r] * 0.f + ob[r] * 0.f;
        if (h == 0) { chk += (oa[8] + oa[9] + oa[10] + oa[11] + ob[8] + ob[9] + ob[10] + ob[11]) * 0.f; } else { chk += (oa[8] + ob[8]) * 0.f; }
        if (h == 1 && !(oa[8] > 1e-30f && ob[8] > 1e-30f && oa[8] < 1e30f && ob[8] < 1e30f)) chk = 1.f;
        if (__builtin_expect(__any(!(chk == 0.f)), 0)) {
            __builtin_amdgcn_fence(__ATOMIC_ACQUIRE, "agent");
            qa2[2] = 0u; qb2[2] = 0u;
            oa = zero16(); ob = zero16();
            float ma = -INFINITY, mb = -INFINITY;
            for (int t = 0; t < 64; ++t) {
                const char* tb = kvimg + (size_t)t * KV_TILE_B;
                const char* kb = kvimg + (size_t)((t + 63) & 63) * KV_TILE_B;
                const bf16x8 k1 = *(const bf16x8*)(kb + k1a), k2 = kfrag2(*(const u32x2*)(kb + k2a), kone);
                const bf16x8 v0 = *(const bf16x8*)(tb + va), v1 = *(const bf16x8*)(tb + va + 672);
                f32x16 sa = MFMA(k1, BC(qa1), zero16()); sa = MFMA(k2, BC(qa2), sa);
                f32x16 sb = MFMA(k1, BC(qb1), zero16()); sb = MFMA(k2, BC(qb2), sb);
                float ta = ma, tb_ = mb;
#pragma unroll
                for (int r = 0; r < 16; ++r) { ta = __builtin_fmaxf(ta, sa[r]); tb_ = __builtin_fmaxf(tb_, sb[r]); }
                ta = xhalf_max(ta); tb_ = xhalf_max(tb_);
                const float fa = __builtin_amdgcn_exp2f(ma - ta), fb = __builtin_amdgcn_exp2f(mb - tb_);
                ma = ta; mb = tb_;
#pragma unroll
                for (int r = 0; r < 16; ++r) { oa[r] *= fa; ob[r] *= fb; sa[r] = __builtin_amdgcn_exp2f(sa[r] - ma); sb[r] = __builtin_amdgcn_exp2f(sb[r] - mb); }
                u32x4 pa0, pa1, pb0, pb1;
#pragma unroll
                for (int j = 0; j < 4; ++j) { pa0[j] = cvtpk(sa[2 * j], sa[2 * j + 1]); pa1[j] = cvtpk(sa[8 + 2 * j], sa[9 + 2 * j]); pb0[j] = cvtpk(sb[2 * j], sb[2 * j + 1]); pb1[j] = cvtpk(sb[8 + 2 * j], sb[9 + 2 * j]); }
                oa = MFMA(v0, BC(pa0), oa); oa = MFMA(v1, BC(pa1), oa);
                ob = MFMA(v0, BC(pb0), ob); ob = MFMA(v1, BC(pb1), ob);
            }
        }
    }
    const float la = __shfl(oa[8], r32 + 32), lb = __shfl(ob[8], r32 + 32);
    const float fa = ga * __builtin_amdgcn_rcpf(la), fb = gb * __builtin_amdgcn_rcpf(lb);
    const int head = 4 * c + w;
    const __amdgpu_buffer_rsrc_t rs = __builtin_amdgcn_make_buffer_rsrc(p.ws, 0, (int)WS_END, 0x00020000);
    const unsigned ob0 = (unsigned)WS_O + (unsigned)(rb * 80 + (head >> 3) * 10) * 1024;
    st16_dev(rs, ob0 + (head & 7) * 1024 + lane * 16, (u32x4){cvtpk(oa[0] * fa, oa[1] * fa), cvtpk(oa[2] * fa, oa[3] * fa), cvtpk(oa[4] * fa, oa[5] * fa), cvtpk(oa[6] * fa, oa[7] * fa)});
    st16_dev(rs, ob0 + 40960 + (head & 7) * 1024 + lane * 16, (u32x4){cvtpk(ob[0] * fb, ob[1] * fb), cvtpk(ob[2] * fb, ob[3] * fb), cvtpk(ob[4] * fb, ob[5] * fb), cvtpk(ob[6] * fb, ob[7] * fb)});
    {
        const u32x2 ua = {cvtpk(oa[8] * fa, oa[9] * fa), cvtpk(oa[10] * fa, oa[11] * fa)}, ub = {cvtpk(ob[8] * fb, ob[9] * fb), cvtpk(ob[10] * fb, ob[11] * fb)};
        const unsigned s0 = (unsigned)__shfl((int)ub[0], r32), s1 = (unsigned)__shfl((int)ub[1], r32);
        const u32x2 uv = {h ? s0 : ua[0], h ? s1 : ua[1]};
        st8_dev(p.ws + ob0 + h * 40960 + (8 + ((head >> 2) & 1)) * 1024 + ((head & 1) * 32 + r32) * 16 + ((head >> 1) & 1) * 8, uv);
    }
    WAIT_VM(0);
    if (lane == 0) flag_set(p.ws, WS_OF + (rb * 4 + (head >> 3)) * 128 + (head & 7) * 8, tag);
    if (blockIdx.x == 0 && w == 0) {
        flags_wait<8, 32>(p.ws + WS_SEEN, 512, tag, lane);
        if (lane == 0) flag_set(p.ws, WS_EPOCH, tag);
    }
#undef KV_DMA
#undef Q_DMA
}

DI u64 ffn_up_wg(const Params& p, int rb2, int fs, u64 ep, char* lds) {
    const int tid = threadIdx.x, lane = tid & 63, w = __builtin_amdgcn_readfirstlane(tid >> 6);
    const int r32 = lane & 31, h = lane >> 5;
    const unsigned lds0 = (unsigned)(uintptr_t)lds;
    constexpr int XS_B = 16384, WS_B = 12288, WS_0 = 2 * XS_B;
    const int xrow = 8 * w + (lane >> 3);
    const unsigned xo = xrow * (D_ * 2) + (((lane & 7) ^ ((xrow >> 1) & 7)) << 4);
    unsigned wo_[3];
#pragma unroll
    for (int j = 0; j < 3; ++j) { const int idx = (w + 4 * j) * 64 + lane, r = idx % 384; wo_[j] = (r / 12) * (F_ * 2) + ((r % 12) ^ (idx >= 384 ? 2 : 0)) * 16; }
    const u16* xsrc = p.x + (size_t)rb2 * 128 * D_;
    const u16* gsrc = p.wg + fs * 96;
    const u16* usrc = p.wu + fs * 96;
    const u16* w1src = (w < 2) ? gsrc : usrc;
    const unsigned dw = lds0 + w * 1024;
#define UP_X(kb, m) glds16(xsrc + (kb) * 64 + (m) * 32 * D_, xo, dw + ((kb) & 1) * XS_B + (m) * 4096)
#define UP_W0(s) glds16(gsrc + (size_t)(s) * 32 * F_, wo_[0], dw + WS_0 + ((s) & 3) * WS_B)
#define UP_W1(s) glds16(w1src + (size_t)(s) * 32 * F_, wo_[1], dw + WS_0 + ((s) & 3) * WS_B + 4096)
#define UP_W2(s) glds16(usrc + (size_t)(s) * 32 * F_, wo_[2], dw + WS_0 + ((s) & 3) * WS_B + 8192)
#define UP_XA(kb) do { UP_X(kb, 0); UP_X(kb, 1); UP_X(kb, 2); UP_X(kb, 3); } while (0)
#define UP_WA(s) do { UP_W0(s); UP_W1(s); UP_W2(s); } while (0)
    __builtin_amdgcn_s_setprio(3);
    UP_XA(0); UP_WA(0); UP_WA(1); UP_XA(1); UP_WA(2);
    __builtin_amdgcn_s_setprio(1);
    const u64 tag = epoch_tag(ep);
    f32x16 acc[6];
#pragma unroll
    for (int i = 0; i < 6; ++i) acc[i] = zero16();
    const int wr = w >> 1, wc = w & 1, row = 64 * wr + r32, blk = (lane >> 4) & 1;
    lds_cptr xa[4], wa[3];
#pragma unroll
    for (int kk = 0; kk < 4; ++kk) xa[kk] = (lds_cptr)lds + row * 128 + (((2 * kk + h) ^ ((row >> 1) & 7)) << 4);
#pragma unroll
    for (int i = 0; i < 3; ++i) wa[i] = (lds_cptr)lds + WS_0 + blk * 6144 + (8 * h + ((lane & 15) >> 2)) * 192 + ((3 * wc + i) ^ blk) * 32 + (lane & 3) * 8;
    bf16x8 xf0[2], xf1[2], wf0[3], wf1[3];
#define UP_RX(q, rt) ldsread16(xa[(q) & 3] + (((q) >> 2) & 1) * XS_B + (rt) * 4096)
#define UP_RW(q, i) cat8(trread(wa[i] + (((q) >> 1) & 3) * WS_B + ((q) & 1) * 3072), trread(wa[i] + (((q) >> 1) & 3) * WS_B + ((q) & 1) * 3072 + 768))
#define UP_STEP(C, N, q, D0, D1, D2, D3, D4, D5) do { \
        acc[0] = MFMA(wf##C[0], xf##C[0], acc[0]); xf##N[0] = UP_RX(q, 0); D0; SBAR(); \
        acc[3] = MFMA(wf##C[0], xf##C[1], acc[3]); wf##N[0] = UP_RW(q, 0); D1; SBAR(); \
        acc[1] = MFMA(wf##C[1], xf##C[0], acc[1]); xf##N[1] = UP_RX(q, 1); D2; SBAR(); \
        acc[4] = MFMA(wf##C[1], xf##C[1], acc[4]); wf##N[1] = UP_RW(q, 1); D3; SBAR(); \
        acc[2] = MFMA(wf##C[2], xf##C[0], acc[2]); wf##N[2] = UP_RW(q, 2); D4; SBAR(); \
        acc[5] = MFMA(wf##C[2], xf##C[1], acc[5]); D5; SBAR(); } while (0)
#define NOP_ do {} while (0)
    WAIT_BAR(10);
    if (tid == 0) flag_set(p.ws, WS_SEEN + blockIdx.x * 8, tag);
    xf0[0] = UP_RX(0, 0); xf0[1] = UP_RX(0, 1);
#pragma unroll
    for (int i = 0; i < 3; ++i) wf0[i] = UP_RW(0, i);
    for (int it = 0; it < 4; ++it) {
        const int s0 = 4 * it, kb = 2 * it;
        UP_STEP(0, 1, 1, NOP_, UP_W0(s0 + 3), NOP_, UP_W1(s0 + 3), NOP_, UP_W2(s0 + 3));
        WAIT_BAR(10);
        UP_STEP(1, 0, 2, UP_W0(s0 + 4), NOP_, NOP_, UP_W1(s0 + 4), NOP_, NOP_);
        UP_STEP(0, 1, 3, UP_W2(s0 + 4), NOP_, NOP_, NOP_, NOP_, NOP_);
        WAIT_BAR(6);
        UP_STEP(1, 0, 4, UP_X(kb + 2, 0), UP_X(kb + 2, 1), NOP_, UP_X(kb + 2, 2), NOP_, UP_X(kb + 2, 3));
        UP_STEP(0, 1, 5, NOP_, UP_W0(s0 + 5), NOP_, UP_W1(s0 + 5), NOP_, UP_W2(s0 + 5));
        WAIT_BAR(10);
        UP_STEP(1, 0, 6, UP_W0(s0 + 6), NOP_, NOP_, UP_W1(s0 + 6), NOP_, NOP_);
        UP_STEP(0, 1, 7, UP_W2(s0 + 6), NOP_, NOP_, NOP_, NOP_, NOP_);
        WAIT_BAR(6);
        UP_STEP(1, 0, 0, UP_X(kb + 3, 0), UP_X(kb + 3, 1), NOP_, UP_X(kb + 3, 2), NOP_, UP_X(kb + 3, 3));
    }
    UP_STEP(0, 1, 1, UP_W0(19), UP_W1(19), UP_W2(19), NOP_, NOP_, NOP_);
    WAIT_BAR(3);
    UP_STEP(1, 0, 2, NOP_, NOP_, NOP_, NOP_, NOP_, NOP_);
    UP_STEP(0, 1, 3, NOP_, NOP_, NOP_, NOP_, NOP_, NOP_);
    UP_STEP(1, 0, 4, NOP_, NOP_, NOP_, NOP_, NOP_, NOP_);
    UP_STEP(0, 1, 5, NOP_, NOP_, NOP_, NOP_, NOP_, NOP_);
    WAIT_BAR(0);
    UP_STEP(1, 0, 6, NOP_, NOP_, NOP_, NOP_, NOP_, NOP_);
    UP_STEP(0, 1, 7, NOP_, NOP_, NOP_, NOP_, NOP_, NOP_);
#pragma unroll
    for (int i = 0; i < 6; ++i) acc[i] = MFMA(wf1[i % 3], xf1[i / 3], acc[i]);
    __builtin_amdgcn_s_setprio(3);
    const __amdgpu_buffer_rsrc_t rs = __builtin_amdgcn_make_buffer_rsrc(p.ws, 0, (int)WS_END, 0x00020000);
    const unsigned ho = (unsigned)WS_H + (unsigned)((rb2 * 4 + 2 * wr) * 96 + fs * 6 + 3 * wc) * 1024 + lane * 16;
#pragma unroll
    for (int j = 0; j < 6; ++j) {
        float hv[8];
#pragma unroll
        for (int r = 0; r < 8; ++r) hv[r] = __builtin_fmaxf(acc[j][r], 0.f) * acc[j][8 + r];
        st16_dev(rs, ho + (j / 3) * 96 * 1024 + (j % 3) * 1024, (u32x4){cvtpk(hv[0], hv[1]), cvtpk(hv[2], hv[3]), cvtpk(hv[4], hv[5]), cvtpk(hv[6], hv[7])});
    }
    WAIT_BAR(0);
    if (tid == 0) flag_set(p.ws, WS_HF + (rb2 * 4 + (fs >> 2)) * 128 + (fs & 3) * 8, tag);
    return tag;
}

#define MFMA16(a, b, c) __builtin_amdgcn_mfma_f32_16x16x32_bf16((a), (b), (c), 0, 0, 0)
DI void ld16_dev(bf16x8& d0, bf16x8& d1, const char* sbase, unsigned voff) {
    asm volatile("global_load_dwordx4 %0, %2, %3 sc1\n\tglobal_load_dwordx4 %1, %2, %3 offset:256 sc1" : "=&v"(d0), "=&v"(d1) : "v"(voff), "s"(sbase) : "memory");
}
DI void out_wg(const Params& p, int rb, int cn, u64 tag, char* lds) {
    const int tid = threadIdx.x, lane = tid & 63, w = __builtin_amdgcn_readfirstlane(tid >> 6);
    const int r16 = lane & 15, g = lane >> 4;
    const unsigned lds0 = (unsigned)(uintptr_t)lds + w * 20480;
    unsigned wo_[5], wm_[5], wl_[5];
#pragma unroll
    for (int j = 0; j < 5; ++j) {
        const int idx = j * 64 + lane, r = idx / 10, ch = (idx % 10) * 16;
        wo_[j] = r * (D_ * 2) + ch; wm_[j] = ((r >> 4) * 20 + (r & 15)) * (D_ * 2) + ch; wl_[j] = ((r >> 2) * 20 + 16 + (r & 3)) * (D_ * 2) + ch;
    }
    const u16* dsrc = p.wd + (size_t)w * 384 * D_ + cn * 80;
    const u16* osrc = p.wo + (size_t)w * 160 * D_ + cn * 80;
    const char* hsrc = p.ws + WS_H + ((size_t)(rb * 2) * 48 + w * 12) * 2048;
    const char* asrc = p.ws + WS_O + ((size_t)(rb * 2) * 40 + w * 10) * 1024;
    const unsigned lo16 = (g >> 1) * 1024 + (g & 1) * 512 + r16 * 16;
#define W_PIECES(s_, o_, d_) do { glds16(s_, o_[0], d_); glds16(s_, o_[1], d_ + 1024); glds16(s_, o_[2], d_ + 2048); glds16(s_, o_[3], d_ + 3072); glds16(s_, o_[4], d_ + 4096); } while (0)
#define W_DMA(u) do { const unsigned d_ = lds0 + ((u) & 3) * 5120; \
        if ((u) < 12) W_PIECES(dsrc + (size_t)(u) * 32 * D_, wo_, d_); else if ((u) < 16) W_PIECES(osrc + (size_t)((u) - 12) * 40 * D_, wm_, d_); else W_PIECES(osrc, wl_, d_); } while (0)
    asm volatile("" :: "v"(wo_[0]), "v"(wo_[1]), "v"(wo_[2]), "v"(wo_[3]), "v"(wo_[4]), "v"(wm_[0]), "v"(wm_[1]), "v"(wm_[2]), "v"(wm_[3]), "v"(wm_[4]), "v"(wl_[0]), "v"(wl_[1]), "v"(wl_[2]), "v"(wl_[3]), "v"(wl_[4]));
    __builtin_amdgcn_s_setprio(1);
    W_DMA(0); W_DMA(1); W_DMA(2); W_DMA(3);
    f32x4 acc[4][5];
#pragma unroll
    for (int i = 0; i < 4; ++i)
#pragma unroll
        for (int j = 0; j < 5; ++j) acc[i][j] = (f32x4){0.f, 0.f, 0.f, 0.f};
    const lds_cptr wbase = (lds_cptr)lds + w * 20480 + (16 * (g >> 1) + 4 * (g & 1) + (r16 >> 2)) * 160 + (lane & 3) * 8;
    bf16x8 wf[2][5];
    bf16x8 af[5][4];
#define W_RD(u, i) wf[(u) & 1][i] = cat8(trread(wbase + ((u) & 3) * 5120 + (i) * 32), trread(wbase + ((u) & 3) * 5120 + (i) * 32 + 1280))
#define H_LD(u) do { ld16_dev(af[(u) & 3][0], af[(u) & 3][1], hsrc + (u) * 2048, lo16); ld16_dev(af[(u) & 3][2], af[(u) & 3][3], hsrc + 48 * 2048 + (u) * 2048, lo16); } while (0)
#define W_PIECE(u, i) glds16((u) < 12 ? (const void*)(dsrc + (size_t)(u) * 32 * D_) : (const void*)(osrc + (size_t)((u) - 12) * 40 * D_), (u) < 12 ? wo_[i] : wm_[i], lds0 + ((u) & 3) * 5120 + (i) * 1024)
#define UNIT_D(u, A) do { _Pragma("unroll") for (int i = 0; i < 5; ++i) { \
        acc[0][i] = MFMA16(wf[(u) & 1][i], A[0], acc[0][i]); acc[1][i] = MFMA16(wf[(u) & 1][i], A[1], acc[1][i]); W_PIECE((u) + 4, i); SBAR(); \
        acc[2][i] = MFMA16(wf[(u) & 1][i], A[2], acc[2][i]); acc[3][i] = MFMA16(wf[(u) & 1][i], A[3], acc[3][i]); W_RD((u) + 1, i); \
        if ((u) + 3 < 12 && i == 1) ld16_dev(af[((u) + 3) & 3][0], af[((u) + 3) & 3][1], hsrc + ((u) + 3) * 2048, lo16); \
        if ((u) + 3 < 12 && i == 3) ld16_dev(af[((u) + 3) & 3][2], af[((u) + 3) & 3][3], hsrc + 48 * 2048 + ((u) + 3) * 2048, lo16); \
        SBAR(); } } while (0)
#define UNIT(u, A) do { _Pragma("unroll") for (int i = 0; i < 5; ++i) { \
        _Pragma("unroll") for (int rg = 0; rg < 4; ++rg) acc[rg][i] = MFMA16(wf[(u) & 1][i], A[rg], acc[rg][i]); \
        if ((u) + 1 < 17) W_RD((u) + 1, i); SBAR(); } } while (0)
#define PINA(A) "+v"(A[0]), "+v"(A[1]), "+v"(A[2]), "+v"(A[3])
    flags_wait<1, 32>(p.ws + WS_KVF + (blockIdx.x & 15) * 1024 + (rb * 4) * 8, 4, tag, lane);
    flags_wait<1, 16>(p.ws + WS_HF + ((rb >> 1) * 4 + w) * 128, 4, tag, lane);
    float gm[4];
#pragma unroll
    for (int rg = 0; rg < 4; ++rg) gm[rg] = __uint_as_float(cvtpk(1.f - ld4_dev(p.ws + WS_G + (rb * 64 + rg * 16 + r16) * 4), 0.f) << 16);
    asm volatile("s_waitcnt vmcnt(0)" :: "v"(gm[0]), "v"(gm[1]), "v"(gm[2]), "v"(gm[3]) : "memory");
    H_LD(0); H_LD(1); H_LD(2);
#pragma unroll
    for (int i = 0; i < 5; ++i) W_RD(0, i);
    __builtin_amdgcn_s_setprio(1);
#pragma unroll
    for (int u = 0; u < 12; ++u) {
        if (u == 0) asm volatile("s_waitcnt vmcnt(8) lgkmcnt(0)" : PINA(af[0]) :: "memory");
        else if (u == 1) asm volatile("s_waitcnt vmcnt(13) lgkmcnt(0)" : PINA(af[1]) :: "memory");
        else if (u <= 9) asm volatile("s_waitcnt vmcnt(18) lgkmcnt(0)" : PINA(af[u & 3]) :: "memory");
        else if (u == 10) asm volatile("s_waitcnt vmcnt(14) lgkmcnt(0)" : PINA(af[u & 3]) :: "memory");
        else asm volatile("s_waitcnt vmcnt(10) lgkmcnt(0)" : PINA(af[u & 3]) :: "memory");
        SBAR();
        UNIT_D(u, af[u & 3]);
    }
    __builtin_amdgcn_s_setprio(3);
    asm volatile("s_waitcnt lgkmcnt(0)" ::: "memory");
    W_DMA(16);
#pragma unroll
    for (int rg = 0; rg < 4; ++rg)
#pragma unroll
        for (int i = 0; i < 5; ++i) acc[rg][i] *= gm[rg];
    flags_wait<1, 6>(p.ws + WS_OF + (rb * 4 + w) * 128, 8, tag, lane);
#pragma unroll
    for (int v = 0; v < 5; ++v) { ld16_dev(af[v][0], af[v][1], asrc + v * 2048, lo16); ld16_dev(af[v][2], af[v][3], asrc + 40 * 1024 + v * 2048, lo16); }
    asm volatile("s_waitcnt vmcnt(16)" : PINA(af[0]) :: "memory"); SBAR(); UNIT(12, af[0]);
    asm volatile("s_waitcnt vmcnt(12)" : PINA(af[1]) :: "memory"); SBAR(); UNIT(13, af[1]);
    asm volatile("s_waitcnt vmcnt(8)" : PINA(af[2]) :: "memory"); SBAR(); UNIT(14, af[2]);
    asm volatile("s_waitcnt vmcnt(4)" : PINA(af[3]) :: "memory"); SBAR(); UNIT(15, af[3]);
    asm volatile("s_waitcnt vmcnt(0)" : PINA(af[4]) :: "memory"); SBAR(); UNIT(16, af[4]);
    LAS f32x4* mine = (LAS f32x4*)((lds_ptr)lds + w * 20480);
#pragma unroll
    for (int rg = 0; rg < 4; ++rg)
#pragma unroll
        for (int i = 0; i < 5; ++i) mine[(rg * 5 + i) * 64 + lane] = acc[rg][i];
    WAIT_BAR(0);
    const LAS f32x4* all = (const LAS f32x4*)lds + w * 5 * 64 + lane;
    u16* orow = p.out + (size_t)(rb * 64 + w * 16 + r16) * D_ + cn * 80 + 4 * g;
#pragma unroll
    for (int i = 0; i < 5; ++i) {
        const f32x4 s = (all[i * 64] + all[1280 + i * 64]) + (all[2560 + i * 64] + all[3840 + i * 64]);
        st8_dev(orow + 16 * i, (u32x2){cvtpk(s[0], s[1]), cvtpk(s[2], s[3])});
    }
}

__global__ __launch_bounds__(256, 2) void k_fused(Params p) {
    extern __shared__ __attribute__((aligned(16))) char lds[];
    const int b = blockIdx.x, j = b & 7, i = (b >> 3) & 31;
    const u64 ep = epoch_load(p.ws + WS_EPOCH);
    if (b < 256) {
        __builtin_amdgcn_s_setprio(2);
        attn_wg(p, 16 * (j >> 2) + (i >> 1), 2 * (j & 3) + (i & 1), ep, lds);
    } else {
        if (i < 16) { __builtin_amdgcn_s_setprio(3); kv_job(p, 8 * j + (i >> 1), i & 1, ep, lds); __builtin_amdgcn_s_setprio(1); }
        const u64 tag = ffn_up_wg(p, 8 * (j >> 2) + (i >> 2), 4 * (j & 3) + (i & 3), ep, lds);
        out_wg(p, 4 * j + (i >> 3), i & 7, tag, lds);
    }
}

extern "C" void kernel_launch(void* const* d_in, const int* in_sizes, int n_in, void* d_out, int out_size, void* d_ws, size_t ws_size,
                              hipStream_t stream) {
    Params p;
    p.x = (const u16*)d_in[0]; p.wq = (const u16*)d_in[1]; p.wk = (const u16*)d_in[2]; p.wv = (const u16*)d_in[3]; p.wo = (const u16*)d_in[4];
    p.wg = (const u16*)d_in[6]; p.wu = (const u16*)d_in[7]; p.wd = (const u16*)d_in[8]; p.gate_w = (const u16*)d_in[9];
    p.out = (u16*)d_out; p.ws = (char*)d_ws;
    (void)hipFuncSetAttribute((const void*)k_fused, hipFuncAttributeMaxDynamicSharedMemorySize, LDS_BYTES);
    k_fused<<<512, 256, LDS_BYTES, stream>>>(p);
}
```

```cpp
#include <hip/hip_runtime.h>
#include <hip/hip_bf16.h>
#include <stdint.h>

typedef __attribute__((ext_vector_type(8))) short bf16x8;
typedef __attribute__((ext_vector_type(4))) short s16x4;
typedef __attribute__((ext_vector_type(16))) float f32x16;
typedef __attribute__((ext_vector_type(4))) float f32x4;
typedef __attribute__((ext_vector_type(2))) float f32x2;
typedef __attribute__((ext_vector_type(4))) unsigned u32x4;
typedef __attribute__((ext_vector_type(2))) unsigned u32x2;
typedef __attribute__((ext_vector_type(2))) __bf16 bf16x2_t;
typedef short v4i16_t __attribute__((ext_vector_type(4)));
typedef unsigned short u16;
typedef unsigned long long u64;
#define LAS __attribute__((address_space(3)))
#define GAS __attribute__((address_space(1)))
typedef LAS const char* lds_cptr;
typedef LAS char* lds_ptr;
#define DI __device__ __forceinline__

constexpr int D_ = 640, F_ = 1536;
constexpr int KV_TILE_B = 2624;
constexpr int KV_CHUNK_B = 4 * KV_TILE_B;
constexpr int KV_SLOT_B = 12288;
constexpr size_t WS_EPOCH = 0, WS_SEEN = 1024, WS_HF = 8192, WS_KVF = 16384, WS_OF = 32768;
constexpr size_t WS_KV = 64 * 1024;
constexpr size_t WS_G = 240 * 1024;
constexpr size_t WS_H = 256 * 1024;
constexpr size_t WS_O = WS_H + (size_t)64 * 48 * 2048;
constexpr size_t WS_END = WS_O + (size_t)64 * 40 * 1024;
constexpr int LDS_BYTES = 80 * 1024;
constexpr unsigned SPIN_CAP = 1u << 18;

#define MFMA(a, b, c) __builtin_amdgcn_mfma_f32_32x32x16_bf16((a), (b), (c), 0, 0, 0)
#define SBAR() __builtin_amdgcn_sched_barrier(0)
#define WAIT_VM(N) asm volatile("s_waitcnt vmcnt(" #N ")" ::: "memory")
#define WAIT_BAR(N) asm volatile("s_waitcnt vmcnt(" #N ") lgkmcnt(0)\n\ts_barrier" ::: "memory")
#define RLX_AGENT __ATOMIC_RELAXED, __HIP_MEMORY_SCOPE_AGENT
#define BC(x) __builtin_bit_cast(bf16x8, x)

DI unsigned cvtpk(float lo, float hi) {
    f32x2 v = {lo, hi};
    return __builtin_bit_cast(unsigned, __builtin_convertvector(v, bf16x2_t));
}
DI void glds16(const void* sbase, unsigned voff, unsigned lds_base) {
    unsigned sv;
    asm volatile("s_mov_b32 %0, m0\n\ts_mov_b32 m0, %3\n\ts_nop 0\n\tglobal_load_lds_dwordx4 %1, %2\n\ts_mov_b32 m0, %0"
                 : "=&s"(sv) : "v"(voff), "s"(sbase), "s"(lds_base) : "memory");
}
DI void glds16v(const void* ptr, unsigned lds_base) {
    unsigned sv;
    asm volatile("s_mov_b32 %0, m0\n\ts_mov_b32 m0, %2\n\ts_nop 0\n\tglobal_load_lds_dwordx4 %1, off\n\ts_mov_b32 m0, %0"
                 : "=&s"(sv) : "v"(ptr), "s"(lds_base) : "memory");
}
DI void glds16_dev(const void* sbase, unsigned voff, unsigned lds_base) {
    unsigned sv;
    asm volatile("s_mov_b32 %0, m0\n\ts_mov_b32 m0, %3\n\ts_nop 0\n\tglobal_load_lds_dwordx4 %1, %2 sc1\n\ts_mov_b32 m0, %0"
                 : "=&s"(sv) : "v"(voff), "s"(sbase), "s"(lds_base) : "memory");
}
DI s16x4 trread(lds_cptr p) { return __builtin_bit_cast(s16x4, __builtin_amdgcn_ds_read_tr16_b64_v4i16((LAS v4i16_t*)p)); }
DI bf16x8 cat8(s16x4 lo, s16x4 hi) { return __builtin_shufflevector(lo, hi, 0, 1, 2, 3, 4, 5, 6, 7); }
DI bf16x8 ldsread16(lds_cptr p) { return *(const LAS bf16x8*)p; }
DI float xhalf_max(float m) {
    auto rr = __builtin_amdgcn_permlane32_swap(__float_as_uint(m), __float_as_uint(m), false, false);
    return __builtin_fmaxf(__uint_as_float(rr[0]), __uint_as_float(rr[1]));
}
DI f32x16 zero16() { f32x16 z; for (int i = 0; i < 16; ++i) z[i] = 0.f; return z; }
DI void st16_dev(__amdgpu_buffer_rsrc_t r, unsigned off, u32x4 v) { __builtin_amdgcn_raw_buffer_store_b128(v, r, off, 0, 16); }
DI void st8_dev(void* p, u32x2 v) { __hip_atomic_store((GAS u64*)p, (u64)v[0] | ((u64)v[1] << 32), RLX_AGENT); }
DI void st4_dev(void* p, float v) { __hip_atomic_store((GAS unsigned*)p, __float_as_uint(v), RLX_AGENT); }
DI u64 ld8_dev(const void* p) { return __hip_atomic_load((const GAS u64*)p, RLX_AGENT); }
DI float ld4_dev(const void* p) { return __uint_as_float(__hip_atomic_load((const GAS unsigned*)p, RLX_AGENT)); }
DI void flag_set(char* ws, size_t off, u64 tag) { __hip_atomic_store((GAS u64*)(ws + off), tag, RLX_AGENT); }
DI u64 epoch_load(const char* ws) { u64 e; asm volatile("s_load_dwordx2 %0, %1, 0x0" : "=s"(e) : "s"(ws) : "memory"); return e; }
DI u64 epoch_tag(u64 e) { u64 r; asm volatile("s_waitcnt lgkmcnt(0)\n\ts_mov_b64 %0, %1" : "=s"(r) : "s"(e) : "memory"); return r * 6364136223846793005ull + 1442695040888963407ull; }
template <int K, int SLEEP> DI void flags_wait(const char* f, int n, u64 tag, int lane) {
#pragma unroll 1
    for (unsigned spin = 0; spin < SPIN_CAP; ++spin) {
        bool ok = true;
#pragma unroll
        for (int k = 0; k < K; ++k) { const int i = lane + 64 * k; const u64 v = ld8_dev(f + 8 * (i < n ? i : 0)); ok &= (v == tag); }
        if (__all(ok)) break;
        __builtin_amdgcn_s_sleep(SLEEP);
    }
}

struct Params {
    const u16* x; const u16* wq; const u16* wk; const u16* wv; const u16* wo;
    const u16* wg; const u16* wu; const u16* wd; const u16* gate_w;
    u16* out; char* ws;
};

DI void kv_job(const Params& p, int rt, int isv, u64 ep, char* lds) {
    const int tid = threadIdx.x, lane = tid & 63, w = __builtin_amdgcn_readfirstlane(tid >> 6);
    const int r32 = lane & 31, h = lane >> 5;
    const unsigned lds0 = (unsigned)(uintptr_t)lds;
    const unsigned xoff = w * 17408, woff = xoff + 10240;
    const u16* xs = p.x + (size_t)rt * 32 * D_ + w * 160;
#pragma unroll
    for (int i = 0; i < 10; ++i) {
        const int idx = i * 64 + lane, row = idx / 20, ch = (idx % 20) ^ ((row >> 2) & 3);
        glds16(xs, (unsigned)(row * (D_ * 2) + ch * 16), lds0 + xoff + i * 1024);
    }
    const u16* wsrc = (isv ? p.wv : p.wk);
#pragma unroll
    for (int i = 0; i < 6; ++i) glds16(wsrc, (unsigned)(w * 6400 + i * 1024 + lane * 16), lds0 + woff + i * 1024);
    glds16v(lane < 16 ? (const char*)wsrc + w * 6400 + 6144 + lane * 16 : (const char*)p.gate_w + w * 320 + (lane < 36 ? lane - 16 : 0) * 16, lds0 + woff + 6144);
    const u64 tag = epoch_tag(ep);
    WAIT_VM(0);
    const lds_cptr xb0 = (lds_cptr)lds + xoff + r32 * 320 + ((h ^ ((r32 >> 2) & 3)) << 4), xb1 = (lds_cptr)lds + xoff + r32 * 320 + (((2 + h) ^ ((r32 >> 2) & 3)) << 4);
    const lds_cptr wb = (lds_cptr)lds + woff + (8 * h + ((lane & 15) >> 2)) * 40 + ((lane >> 4) & 1) * 32 + (lane & 3) * 8;
    const lds_cptr gb = (lds_cptr)lds + woff + 6400 + h * 16;
    bf16x8 xf[10], wf[10], gf[10];
#pragma unroll
    for (int ks = 0; ks < 10; ++ks) { xf[ks] = ldsread16(((ks & 1) ? xb1 : xb0) + (ks >> 1) * 64); wf[ks] = cat8(trread(wb + ks * 640), trread(wb + ks * 640 + 160)); gf[ks] = ldsread16(gb + ks * 32); }
    SBAR();
    f32x16 acc = zero16();
    if (isv) {
#pragma unroll
        for (int ks = 0; ks < 10; ++ks) acc = MFMA(xf[ks], wf[ks], acc);
    } else {
#pragma unroll
        for (int ks = 0; ks < 10; ++ks) acc = MFMA((r32 == 20) ? gf[ks] : wf[ks], xf[ks], acc);
    }
    LAS f32x4* red = (LAS f32x4*)((lds_ptr)lds + 69632);
    if (w > 0) {
#pragma unroll
        for (int g = 0; g < 4; ++g) red[((w - 1) * 4 + g) * 64 + lane] = (f32x4){acc[4 * g], acc[4 * g + 1], acc[4 * g + 2], acc[4 * g + 3]};
    }
    WAIT_BAR(0);
    if (w == 0) {
#pragma unroll
        for (int ww = 0; ww < 3; ++ww)
#pragma unroll
            for (int g = 0; g < 4; ++g) { const f32x4 t = red[(ww * 4 + g) * 64 + lane]; acc[4 * g] += t[0]; acc[4 * g + 1] += t[1]; acc[4 * g + 2] += t[2]; acc[4 * g + 3] += t[3]; }
    }
    WAIT_BAR(0);
    if (w == 0) {
        const __amdgpu_buffer_rsrc_t rs = __builtin_amdgcn_make_buffer_rsrc(p.ws, 0, (int)WS_END, 0x00020000);
        const unsigned tile = (unsigned)WS_KV + (unsigned)(isv ? rt : ((rt + 63) & 63)) * KV_TILE_B;
        u32x4 lo = {cvtpk(acc[0], acc[1]), cvtpk(acc[2], acc[3]), cvtpk(acc[4], acc[5]), cvtpk(acc[6], acc[7])};
        u32x4 hi = {cvtpk(acc[8], acc[9]), cvtpk(acc[10], acc[11]), cvtpk(acc[12], acc[13]), cvtpk(acc[14], acc[15])};
        if (!isv) {
            st16_dev(rs, tile + lane * 16, lo);
            if (h == 0) st8_dev(p.ws + tile + 1024 + r32 * 8, (u32x2){hi[0], hi[1]});
            else { const float lg = __uint_as_float(cvtpk(acc[8], 0.f) << 16);
                   st4_dev(p.ws + WS_G + (rt * 32 + r32) * 4, __uint_as_float(cvtpk(1.f / (1.f + __expf(-lg)), 0.f) << 16)); }
        } else {
            if (r32 == 20) { lo = (u32x4){0x3f803f80u, 0x3f803f80u, 0x3f803f80u, 0x3f803f80u}; hi = lo; }
            if (r32 <= 20) { st16_dev(rs, tile + 1280 + h * 336 + r32 * 16, lo); st16_dev(rs, tile + 1952 + h * 336 + r32 * 16, hi); }
        }
        WAIT_VM(0);
        if (lane < 16) flag_set(p.ws, WS_KVF + lane * 1024 + (rt * 2 + isv) * 8, tag);
    }
}


#define AE(b, i) "v_exp_f32 v[" #b "+" #i "], v[" #b "+" #i "]\n"
#define AC(pb, j, b, i) "v_cvt_pk_bf16_f32 v[" #pb "+" #j "], v[" #b "+" #i "], v[" #b "+" #i "+1]\n"
#define AG2(MA, MB, X, XI, PX, PJ) MA AE(X, XI) AE(X, XI+1) AE(X, XI+2) AE(X, XI+3) AC(PX, PJ, X, XI) AC(PX, PJ+1, X, XI+2) \
    MB AE(X, XI+4) AE(X, XI+5) AE(X, XI+6) AE(X, XI+7) AC(PX, PJ+2, X, XI+4) AC(PX, PJ+3, X, XI+6)
#define AHALF(X, PX, Y, Q1, Q2, O, PR, BUF, R1, R2, R3, R4) \
    AG2("v_mfma_f32_32x32x16_bf16 v[" #Y ":" #Y "+15], v[" #BUF ":" #BUF "+3], " Q1 ", 0\n" R1, \
        "v_mfma_f32_32x32x16_bf16 v[" #Y ":" #Y "+15], v[" #BUF "+4:" #BUF "+7], " Q2 ", v[" #Y ":" #Y "+15]\n" R2, X, 0, PX, 0) \
    AG2("v_mfma_f32_32x32x16_bf16 " O ", v[" #BUF "+8:" #BUF "+11], v[" #PR ":" #PR "+3], " O "\n" R3, \
        "v_mfma_f32_32x32x16_bf16 " O ", v[" #BUF "+12:" #BUF "+15], v[" #PR "+4:" #PR "+7], " O "\n" R4, X, 8, PX, 4)
#define ARK1(NB, sl, tl) "ds_read_b128 v[" #NB ":" #NB "+3], %[k1a] offset:4096+(" #sl ")*12288+" #tl "*2624\n"
#define ARK2(NB, sl, tl) "ds_read_b64 v[" #NB "+4:" #NB "+5], %[k2a] offset:4096+(" #sl ")*12288+" #tl "*2624\n"
#define ARV0(NB, sl, tl) "ds_read_b128 v[" #NB "+8:" #NB "+11], %[va] offset:4096+(" #sl ")*12288+" #tl "*2624\n"
#define ARV1(NB, sl, tl) "ds_read_b128 v[" #NB "+12:" #NB "+15], %[va] offset:4096+(" #sl ")*12288+" #tl "*2624+672\n"
#define AH1(PREV, R1, R2, R3, R4) AHALF(128, 160, 144, "%[qb1]", "%[qb2]", "%[ob]", 168, PREV, R1, R2, R3, R4)
#define AH2(CUR, PREV, sl, tl) AHALF(144, 168, 128, "%[qa1]", "%[qa2]", "%[oa]", 160, CUR, ARK1(PREV, sl, tl), ARK2(PREV, sl, tl), ARV0(PREV, sl, tl), ARV1(PREV, sl, tl))
#define AWL "s_waitcnt lgkmcnt(0)\n"
#define ASYNC "s_waitcnt vmcnt(6) lgkmcnt(0)\ns_barrier\n"
#define ADMA0(fs) "s_mul_i32 %[of], %[ck], 10496\n s_add_u32 %[ck], %[ck], 1\n s_add_u32 m0, %[ldst], " #fs "*12288\n" \
                  "v_add_u32 v208, %[of], %[kvo]\n"
#define ADMA1 "global_load_lds_dwordx4 v208, %[img] sc1\n"
#define ADMA2 "global_load_lds_dwordx4 v208, %[img] offset:1024 sc1\n"
#define ADMA3 "global_load_lds_dwordx4 v208, %[img] offset:2048 sc1\n"
#define ACHUNK(sl, nsl) \
    AH1(192, "", "", "", "") AWL AH2(176, 192, sl, 2) \
    AH1(176, "", "", "", "") AWL AH2(192, 176, sl, 3) \
    AH1(192, "", "", "", "") ASYNC AH2(176, 192, nsl, 0) \
    AH1(176, ADMA0(sl), ADMA1, ADMA2, ADMA3) AWL AH2(192, 176, nsl, 1)
#define ACHUNKT(sl, nsl, W) \
    AH1(192, "", "", "", "") AWL AH2(176, 192, sl, 2) \
    AH1(176, "", "", "", "") AWL AH2(192, 176, sl, 3) \
    AH1(192, "", "", "", "") "s_waitcnt vmcnt(" #W ") lgkmcnt(0)\ns_barrier\n" AH2(176, 192, nsl, 0) \
    AH1(176, "", "", "", "") AWL AH2(192, 176, nsl, 1)
#define ASP1(NB) "ds_read_b128 v[" #NB "+8:" #NB "+11], %[va]\n"
#define ASP2(NB) "ds_read_b128 v[" #NB "+12:" #NB "+15], %[va] offset:672\n"
#define ACHUNKL(sl) \
    AH1(192, "", "", "", "") AWL AH2(176, 192, sl, 2) \
    AH1(176, "", "", "", "") AWL AH2(192, 176, sl, 3) \
    AH1(192, "", "", "", "") AWL AHALF(144, 168, 128, "%[qa1]", "%[qa2]", "%[oa]", 160, 176, ASP1(192), ASP2(192), "", "") \
    AH1(176, "", "", "", "") AWL AHALF(144, 168, 128, "%[qa1]", "%[qa2]", "%[oa]", 160, 192, "", "", "", "")
#define AMOV(d, s) "v_mov_b32 v" #d ", " s "\n"
#define ATTN_LOOP_ASM \
    "s_setprio 2\n s_mov_b32 %[m0s], m0\n s_mov_b32 %[it], 3\n s_mov_b32 %[ck], 4\n" \
    ARK1(176, 1, 1) ARK2(176, 1, 1) ARV0(176, 1, 1) ARV1(176, 1, 1) \
    "v_mov_b64 v[192:193], %[k0a]\n v_mov_b64 v[194:195], %[k0b]\n" \
    "v_mov_b64 v[196:197], %[k02]\n" AMOV(198, "%[kone]") AMOV(199, "0") AMOV(182, "%[kone]") AMOV(183, "0") \
    "v_mfma_f32_32x32x16_bf16 v[128:143], v[192:195], %[qa1], 0\n" \
    AMOV(200, "0") AMOV(201, "0") AMOV(202, "0") AMOV(203, "0") AMOV(204, "0") AMOV(205, "0") AMOV(206, "0") AMOV(207, "0") \
    AMOV(168, "0") AMOV(169, "0") AMOV(170, "0") AMOV(171, "0") AMOV(172, "0") AMOV(173, "0") AMOV(174, "0") AMOV(175, "0") \
    "v_mfma_f32_32x32x16_bf16 v[128:143], v[196:199], %[qa2], v[128:143]\n" \
    "s_nop 7\n s_nop 7\n" \
    "1:\n" \
    ACHUNK(1, 2) ACHUNK(2, 3) ACHUNK(3, 0) ACHUNK(0, 1) \
    "s_sub_u32 %[it], %[it], 1\n s_cmp_lg_u32 %[it], 0\n s_cbranch_scc1 1b\n" \
    ACHUNKT(1, 2, 6) ACHUNKT(2, 3, 3) ACHUNKT(3, 0, 0) ACHUNKL(0) "s_nop 1\n" \
    "v_mfma_f32_32x32x16_bf16 %[ob], v[200:203], v[168:171], %[ob]\n" \
    "v_mfma_f32_32x32x16_bf16 %[ob], v[204:207], v[172:175], %[ob]\n" \
    "s_setprio 3\n s_mov_b32 m0, %[m0s]\n s_nop 7\n s_nop 7\n"
#define ATTN_CLOBBERS "v128","v129","v130","v131","v132","v133","v134","v135","v136","v137","v138","v139","v140","v141","v142","v143", \
    "v144","v145","v146","v147","v148","v149","v150","v151","v152","v153","v154","v155","v156","v157","v158","v159", \
    "v160","v161","v162","v163","v164","v165","v166","v167","v168","v169","v170","v171","v172","v173","v174","v175", \
    "v176","v177","v178","v179","v180","v181","v182","v183","v184","v185","v186","v187","v188","v189","v190","v191", \
    "v192","v193","v194","v195","v196","v197","v198","v199","v200","v201","v202","v203","v204","v205","v206","v207","v208"

DI void attn_wg(const Params& p, int rb, int c, u64 ep, char* lds) {
    const int tid = threadIdx.x, lane = tid & 63, w = __builtin_amdgcn_readfirstlane(tid >> 6);
    const int r32 = lane & 31, h = lane >> 5;
    const unsigned lds0 = (unsigned)(uintptr_t)lds;
    const char* kvimg = p.ws + WS_KV;
    constexpr int RB = 32768;
    const unsigned kvoff = (unsigned)(w * 3072 + lane * 16);
#define KV_DMA(chunk) do { const char* s_ = kvimg + (size_t)(chunk) * KV_CHUNK_B; const unsigned d_ = lds0 + RB + (((chunk) + 1) & 3) * KV_SLOT_B + w * 3072; \
        glds16_dev(s_, kvoff, d_); glds16_dev(s_, kvoff + 1024, d_ + 1024); glds16_dev(s_, kvoff + 2048, d_ + 2048); } while (0)
#define KV_PIECE(chunk, j) glds16_dev(kvimg + (size_t)(chunk) * KV_CHUNK_B, kvoff + (j) * 1024, lds0 + RB + (((chunk) + 1) & 3) * KV_SLOT_B + w * 3072 + (j) * 1024)
    float ga, gb;
#define G_LOAD() asm volatile("global_load_dword %0, %2, %3 sc1\n\tglobal_load_dword %1, %2, %3 offset:128 sc1" : "=&v"(ga), "=&v"(gb) : "v"(r32 * 4), "s"(p.ws + WS_G + rb * 256) : "memory")
    constexpr int QS_B = 18432;
    unsigned qoff[5]; unsigned qdst[5];
#pragma unroll
    for (int j = 0; j < 5; ++j) {
        int i = w + 4 * j; if (i >= 18) i -= 2;
        if (j < 2) { const int row = 8 * i + (lane >> 3), pos = lane & 7, ch = pos ^ ((row >> 1) & 7); qoff[j] = row * (D_ * 2) + ch * 16; }
        else { const int idx = (i - 8) * 64 + lane, kr = idx / 10, ch = idx % 10; qoff[j] = kr * (D_ * 2) + ch * 16; }
        qdst[j] = lds0 + i * 1024;
    }
    const u16* xsrc = p.x + (size_t)rb * 64 * D_;
    const u16* wsrc = p.wq + c * 80;
#define Q_DMA(kt) do { const unsigned d_ = ((kt) & 3) * QS_B; const u16* xs_ = xsrc + (kt) * 64; const u16* ws_ = wsrc + (size_t)(kt) * 64 * D_; \
        glds16(xs_, qoff[0], d_ + qdst[0]); glds16(xs_, qoff[1], d_ + qdst[1]); glds16(ws_, qoff[2], d_ + qdst[2]); glds16(ws_, qoff[3], d_ + qdst[3]); glds16(ws_, qoff[4], d_ + qdst[4]); } while (0)
#define Q_PIECE(kt, j) glds16((j) < 2 ? xsrc + (kt) * 64 : wsrc + (size_t)(kt) * 64 * D_, qoff[j], ((kt) & 3) * QS_B + qdst[j])
    Q_DMA(0); Q_DMA(1); Q_DMA(2); Q_DMA(3);
    const u64 tag = epoch_tag(ep);
    f32x16 qa = zero16(), qb = zero16();
    {
        unsigned xa[4];
#pragma unroll
        for (int ks = 0; ks < 4; ++ks) xa[ks] = r32 * 128 + (((2 * ks + h) ^ ((r32 >> 1) & 7)) << 4);
        const unsigned wa = 8192 + (8 * h + ((lane & 15) >> 2)) * 160 + w * 40 + ((lane >> 4) & 1) * 32 + (lane & 3) * 8;
        bf16x8 wfA[4], x0A[4], x1A[4], wfB[4], x0B[4], x1B[4];
#define Q_RD(S, kt, ks) do { const lds_cptr sb_ = (lds_cptr)lds + ((kt) & 3) * QS_B; wf##S[ks] = cat8(trread(sb_ + wa + (ks) * 2560), trread(sb_ + wa + (ks) * 2560 + 640)); \
            x0##S[ks] = ldsread16(sb_ + xa[ks]); x1##S[ks] = ldsread16(sb_ + xa[ks] + 4096); } while (0)
#define Q_STEP(C, N, kt) do { \
            if ((kt) == 7 && w == 0) { flags_wait<2, 4>(p.ws + WS_KVF + (blockIdx.x & 15) * 1024, 128, tag, lane); }     \
            if ((kt) < 7) { WAIT_BAR(10); } else if ((kt) == 7) { WAIT_BAR(5); } else if ((kt) == 8) { WAIT_BAR(5); } \
            _Pragma("unroll") for (int ks = 0; ks < 4; ++ks) { qa = MFMA(wf##C[ks], x0##C[ks], qa); if ((kt) + 1 < 10) Q_RD(N, (kt) + 1, ks); SBAR(); qb = MFMA(wf##C[ks], x1##C[ks], qb); \
                if ((kt) + 4 < 10) { Q_PIECE((kt) + 4, ks); if (ks == 3) Q_PIECE((kt) + 4, 4); } \
                if ((kt) == 7) { if (ks < 3) KV_PIECE(0, ks); else G_LOAD(); } \
                if ((kt) == 8) KV_PIECE(1 + ks / 3, ks % 3); \
                if ((kt) == 9 && ks < 2) KV_PIECE(2, ks + 1); \
                SBAR(); } } while (0)
        WAIT_BAR(15);
        if (tid == 0) flag_set(p.ws, WS_SEEN + blockIdx.x * 8, tag);
#pragma unroll
        for (int ks = 0; ks < 4; ++ks) Q_RD(A, 0, ks);
#pragma unroll
        for (int kt = 0; kt < 10; kt += 2) { Q_STEP(A, B, kt); Q_STEP(B, A, kt + 1); }
    }
    constexpr float C2 = 0.22360679774997896f * 1.4426950408889634f;
    u32x4 qa1, qa2, qb1, qb2;
    qa1 = (u32x4){cvtpk(qa[0] * C2, qa[1] * C2), cvtpk(qa[2] * C2, qa[3] * C2), cvtpk(qa[4] * C2, qa[5] * C2), cvtpk(qa[6] * C2, qa[7] * C2)};
    qb1 = (u32x4){cvtpk(qb[0] * C2, qb[1] * C2), cvtpk(qb[2] * C2, qb[3] * C2), cvtpk(qb[4] * C2, qb[5] * C2), cvtpk(qb[6] * C2, qb[7] * C2)};
    qa2 = (u32x4){h ? 0u : cvtpk(qa[8] * C2, qa[9] * C2), h ? 0u : cvtpk(qa[10] * C2, qa[11] * C2), 0u, 0u};
    qb2 = (u32x4){h ? 0u : cvtpk(qb[8] * C2, qb[9] * C2), h ? 0u : cvtpk(qb[10] * C2, qb[11] * C2), 0u, 0u};
    f32x16 oa = zero16(), ob = zero16();
    const unsigned k1a = lane * 16, k2a = 1024 + r32 * 8;
    const unsigned va = 1280 + h * 336 + (r32 <= 20 ? r32 : (r32 < 28 ? 20 : 16)) * 16;
    const unsigned kone = h ? 0u : 0x3f80u;
    asm volatile("" : "+v"(oa), "+v"(ob), "+v"(qa1), "+v"(qa2), "+v"(qb1), "+v"(qb2));
    WAIT_BAR(8);
    const bf16x8 k01 = ldsread16((lds_cptr)lds + RB + KV_SLOT_B + k1a);
    const u32x2 k02 = *(const LAS u32x2*)((lds_cptr)lds + RB + KV_SLOT_B + k2a);
    glds16_dev(kvimg, (unsigned)(w * 1024 + lane * 16), lds0 + RB - 4096 + w * 1024);
    auto kfrag2 = [](u32x2 v, unsigned one) { return __builtin_bit_cast(bf16x8, (u32x4){v[0], v[1], one, 0u}); };
    {
        f32x16 sa = MFMA(k01, BC(qa1), zero16()); KV_PIECE(3, 0); SBAR();
        f32x16 sb = MFMA(k01, BC(qb1), zero16()); KV_PIECE(3, 1); SBAR();
        sa = MFMA(kfrag2(k02, kone), BC(qa2), sa); sb = MFMA(kfrag2(k02, kone), BC(qb2), sb); KV_PIECE(3, 2); SBAR();
        float ma = sa[0], mb = sb[0];
#pragma unroll
        for (int r = 1; r < 16; ++r) { ma = __builtin_fmaxf(ma, sa[r]); mb = __builtin_fmaxf(mb, sb[r]); }
        ma = xhalf_max(ma); mb = xhalf_max(mb);
        qa2[2] = h ? 0u : (cvtpk(-ma, 0.f) & 0xffffu);
        qb2[2] = h ? 0u : (cvtpk(-mb, 0.f) & 0xffffu);
    }
    {
        unsigned t_m0, t_it, t_ck, t_of;
        const u32x4 k0u = __builtin_bit_cast(u32x4, k01); const u32x2 k0a = {k0u[0], k0u[1]}, k0b = {k0u[2], k0u[3]};
        asm volatile(ATTN_LOOP_ASM
            : [oa] "+v"(oa), [ob] "+v"(ob), "+v"(ga), "+v"(gb), [m0s] "=&s"(t_m0), [it] "=&s"(t_it), [ck] "=&s"(t_ck), [of] "=&s"(t_of)
            : [qa1] "v"(qa1), [qa2] "v"(qa2), [qb1] "v"(qb1), [qb2] "v"(qb2), [k1a] "v"(lds0 + RB - 4096 + k1a), [k2a] "v"(lds0 + RB - 4096 + k2a), [va] "v"(lds0 + RB - 4096 + va),
              [kvo] "v"(kvoff), [img] "s"(kvimg), [ldst] "s"(lds0 + RB + w * 3072), [k0a] "v"(k0a), [k0b] "v"(k0b), [k02] "v"(k02), [kone] "v"(kone)
            : ATTN_CLOBBERS, "scc", "memory");
    }
    {
        float chk = 0.f;
#pragma unroll
        for (int r = 0; r < 8; ++r) chk += oa[r] * 0.f + ob[r] * 0.f;
        if (h == 0) { chk += (oa[8] + oa[9] + oa[10] + oa[11] + ob[8] + ob[9] + ob[10] + ob[11]) * 0.f; } else { chk += (oa[8] + ob[8]) * 0.f; }
        if (h == 1 && !(oa[8] > 1e-30f && ob[8] > 1e-30f && oa[8] < 1e30f && ob[8] < 1e30f)) chk = 1.f;
        if (__builtin_expect(__any(!(chk == 0.f)), 0)) {
            __builtin_amdgcn_fence(__ATOMIC_ACQUIRE, "agent");
            qa2[2] = 0u; qb2[2] = 0u;
            oa = zero16(); ob = zero16();
            float ma = -INFINITY, mb = -INFINITY;
            for (int t = 0; t < 64; ++t) {
                const char* tb = kvimg + (size_t)t * KV_TILE_B;
                const char* kb = kvimg + (size_t)((t + 63) & 63) * KV_TILE_B;
                const bf16x8 k1 = *(const bf16x8*)(kb + k1a), k2 = kfrag2(*(const u32x2*)(kb + k2a), kone);
                const bf16x8 v0 = *(const bf16x8*)(tb + va), v1 = *(const bf16x8*)(tb + va + 672);
                f32x16 sa = MFMA(k1, BC(qa1), zero16()); sa = MFMA(k2, BC(qa2), sa);
                f32x16 sb = MFMA(k1, BC(qb1), zero16()); sb = MFMA(k2, BC(qb2), sb);
                float ta = ma, tb_ = mb;
#pragma unroll
                for (int r = 0; r < 16; ++r) { ta = __builtin_fmaxf(ta, sa[r]); tb_ = __builtin_fmaxf(tb_, sb[r]); }
                ta = xhalf_max(ta); tb_ = xhalf_max(tb_);
                const float fa = __builtin_amdgcn_exp2f(ma - ta), fb = __builtin_amdgcn_exp2f(mb - tb_);
                ma = ta; mb = tb_;
#pragma unroll
                for (int r = 0; r < 16; ++r) { oa[r] *= fa; ob[r] *= fb; sa[r] = __builtin_amdgcn_exp2f(sa[r] - ma); sb[r] = __builtin_amdgcn_exp2f(sb[r] - mb); }
                u32x4 pa0, pa1, pb0, pb1;
#pragma unroll
                for (int j = 0; j < 4; ++j) { pa0[j] = cvtpk(sa[2 * j], sa[2 * j + 1]); pa1[j] = cvtpk(sa[8 + 2 * j], sa[9 + 2 * j]); pb0[j] = cvtpk(sb[2 * j], sb[2 * j + 1]); pb1[j] = cvtpk(sb[8 + 2 * j], sb[9 + 2 * j]); }
                oa = MFMA(v0, BC(pa0), oa); oa = MFMA(v1, BC(pa1), oa);
                ob = MFMA(v0, BC(pb0), ob); ob = MFMA(v1, BC(pb1), ob);
            }
        }
    }
    const float la = __uint_as_float(__builtin_amdgcn_permlane32_swap(__float_as_uint(oa[8]), __float_as_uint(oa[8]), false, false)[1]);
    const float lb = __uint_as_float(__builtin_amdgcn_permlane32_swap(__float_as_uint(ob[8]), __float_as_uint(ob[8]), false, false)[1]);
    const float fa = ga * __builtin_amdgcn_rcpf(la), fb = gb * __builtin_amdgcn_rcpf(lb);
    const int head = 4 * c + w;
    const __amdgpu_buffer_rsrc_t rs = __builtin_amdgcn_make_buffer_rsrc(p.ws, 0, (int)WS_END, 0x00020000);
    const unsigned ob0 = (unsigned)WS_O + (unsigned)(rb * 80 + (head >> 3) * 10) * 1024;
    st16_dev(rs, ob0 + (head & 7) * 1024 + lane * 16, (u32x4){cvtpk(oa[0] * fa, oa[1] * fa), cvtpk(oa[2] * fa, oa[3] * fa), cvtpk(oa[4] * fa, oa[5] * fa), cvtpk(oa[6] * fa, oa[7] * fa)});
    st16_dev(rs, ob0 + 40960 + (head & 7) * 1024 + lane * 16, (u32x4){cvtpk(ob[0] * fb, ob[1] * fb), cvtpk(ob[2] * fb, ob[3] * fb), cvtpk(ob[4] * fb, ob[5] * fb), cvtpk(ob[6] * fb, ob[7] * fb)});
    {
        const u32x2 ua = {cvtpk(oa[8] * fa, oa[9] * fa), cvtpk(oa[10] * fa, oa[11] * fa)}, ub = {cvtpk(ob[8] * fb, ob[9] * fb), cvtpk(ob[10] * fb, ob[11] * fb)};
        const u32x2 uv = {__builtin_amdgcn_permlane32_swap(ua[0], ub[0], false, false)[0], __builtin_amdgcn_permlane32_swap(ua[1], ub[1], false, false)[0]};
        st8_dev(p.ws + ob0 + h * 40960 + (8 + ((head >> 2) & 1)) * 1024 + ((head & 1) * 32 + r32) * 16 + ((head >> 1) & 1) * 8, uv);
    }
    WAIT_VM(0);
    if (lane == 0) flag_set(p.ws, WS_OF + (rb * 4 + (head >> 3)) * 128 + (head & 7) * 8, tag);
    if (blockIdx.x == 0 && w == 0) {
        flags_wait<8, 32>(p.ws + WS_SEEN, 512, tag, lane);
        if (lane == 0) flag_set(p.ws, WS_EPOCH, tag);
    }
#undef KV_DMA
#undef Q_DMA
}

DI u64 ffn_up_wg(const Params& p, int rb2, int fs, u64 ep, char* lds) {
    const int tid = threadIdx.x, lane = tid & 63, w = __builtin_amdgcn_readfirstlane(tid >> 6);
    const int r32 = lane & 31, h = lane >> 5;
    const unsigned lds0 = (unsigned)(uintptr_t)lds;
    constexpr int XS_B = 16384, WS_B = 12288, WS_0 = 2 * XS_B;
    const int xrow = 8 * w + (lane >> 3);
    const unsigned xo = xrow * (D_ * 2) + (((lane & 7) ^ ((xrow >> 1) & 7)) << 4);
    unsigned wo_[3];
#pragma unroll
    for (int j = 0; j < 3; ++j) { const int idx = (w + 4 * j) * 64 + lane, r = idx % 384; wo_[j] = (r / 12) * (F_ * 2) + ((r % 12) ^ (idx >= 384 ? 2 : 0)) * 16; }
    const u16* xsrc = p.x + (size_t)rb2 * 128 * D_;
    const u16* gsrc = p.wg + fs * 96;
    const u16* usrc = p.wu + fs * 96;
    const u16* w1src = (w < 2) ? gsrc : usrc;
    const unsigned dw = lds0 + w * 1024;
#define UP_X(kb, m) glds16(xsrc + (kb) * 64 + (m) * 32 * D_, xo, dw + ((kb) & 1) * XS_B + (m) * 4096)
#define UP_W0(s) glds16(gsrc + (size_t)(s) * 32 * F_, wo_[0], dw + WS_0 + ((s) & 3) * WS_B)
#define UP_W1(s) glds16(w1src + (size_t)(s) * 32 * F_, wo_[1], dw + WS_0 + ((s) & 3) * WS_B + 4096)
#define UP_W2(s) glds16(usrc + (size_t)(s) * 32 * F_, wo_[2], dw + WS_0 + ((s) & 3) * WS_B + 8192)
#define UP_XA(kb) do { UP_X(kb, 0); UP_X(kb, 1); UP_X(kb, 2); UP_X(kb, 3); } while (0)
#define UP_WA(s) do { UP_W0(s); UP_W1(s); UP_W2(s); } while (0)
    __builtin_amdgcn_s_setprio(3);
    UP_XA(0); UP_WA(0); UP_WA(1); UP_XA(1); UP_WA(2);
    __builtin_amdgcn_s_setprio(1);
    const u64 tag = epoch_tag(ep);
    f32x16 acc[6];
#pragma unroll
    for (int i = 0; i < 6; ++i) acc[i] = zero16();
    const int wr = w >> 1, wc = w & 1, row = 64 * wr + r32, blk = (lane >> 4) & 1;
    lds_cptr xa[4], wa[3];
#pragma unroll
    for (int kk = 0; kk < 4; ++kk) xa[kk] = (lds_cptr)lds + row * 128 + (((2 * kk + h) ^ ((row >> 1) & 7)) << 4);
#pragma unroll
    for (int i = 0; i < 3; ++i) wa[i] = (lds_cptr)lds + WS_0 + blk * 6144 + (8 * h + ((lane & 15) >> 2)) * 192 + ((3 * wc + i) ^ blk) * 32 + (lane & 3) * 8;
    bf16x8 xf0[2], xf1[2], wf0[3], wf1[3];
#define UP_RX(q, rt) ldsread16(xa[(q) & 3] + (((q) >> 2) & 1) * XS_B + (rt) * 4096)
#define UP_RW(q, i) cat8(trread(wa[i] + (((q) >> 1) & 3) * WS_B + ((q) & 1) * 3072), trread(wa[i] + (((q) >> 1) & 3) * WS_B + ((q) & 1) * 3072 + 768))
#define UP_STEP(C, N, q, D0, D1, D2, D3, D4, D5) do { \
        acc[0] = MFMA(wf##C[0], xf##C[0], acc[0]); xf##N[0] = UP_RX(q, 0); D0; SBAR(); \
        acc[3] = MFMA(wf##C[0], xf##C[1], acc[3]); wf##N[0] = UP_RW(q, 0); D1; SBAR(); \
        acc[1] = MFMA(wf##C[1], xf##C[0], acc[1]); xf##N[1] = UP_RX(q, 1); D2; SBAR(); \
        acc[4] = MFMA(wf##C[1], xf##C[1], acc[4]); wf##N[1] = UP_RW(q, 1); D3; SBAR(); \
        acc[2] = MFMA(wf##C[2], xf##C[0], acc[2]); wf##N[2] = UP_RW(q, 2); D4; SBAR(); \
        acc[5] = MFMA(wf##C[2], xf##C[1], acc[5]); D5; SBAR(); } while (0)
#define NOP_ do {} while (0)
    WAIT_BAR(10);
    if (tid == 0) flag_set(p.ws, WS_SEEN + blockIdx.x * 8, tag);
    xf0[0] = UP_RX(0, 0); xf0[1] = UP_RX(0, 1);
#pragma unroll
    for (int i = 0; i < 3; ++i) wf0[i] = UP_RW(0, i);
    for (int it = 0; it < 4; ++it) {
        const int s0 = 4 * it, kb = 2 * it;
        UP_STEP(0, 1, 1, NOP_, UP_W0(s0 + 3), NOP_, UP_W1(s0 + 3), NOP_, UP_W2(s0 + 3));
        WAIT_BAR(10);
        UP_STEP(1, 0, 2, UP_W0(s0 + 4), NOP_, NOP_, UP_W1(s0 + 4), NOP_, NOP_);
        UP_STEP(0, 1, 3, UP_W2(s0 + 4), NOP_, NOP_, NOP_, NOP_, NOP_);
        WAIT_BAR(6);
        UP_STEP(1, 0, 4, UP_X(kb + 2, 0), UP_X(kb + 2, 1), NOP_, UP_X(kb + 2, 2), NOP_, UP_X(kb + 2, 3));
        UP_STEP(0, 1, 5, NOP_, UP_W0(s0 + 5), NOP_, UP_W1(s0 + 5), NOP_, UP_W2(s0 + 5));
        WAIT_BAR(10);
        UP_STEP(1, 0, 6, UP_W0(s0 + 6), NOP_, NOP_, UP_W1(s0 + 6), NOP_, NOP_);
        UP_STEP(0, 1, 7, UP_W2(s0 + 6), NOP_, NOP_, NOP_, NOP_, NOP_);
        WAIT_BAR(6);
        UP_STEP(1, 0, 0, UP_X(kb + 3, 0), UP_X(kb + 3, 1), NOP_, UP_X(kb + 3, 2), NOP_, UP_X(kb + 3, 3));
    }
    UP_STEP(0, 1, 1, UP_W0(19), UP_W1(19), UP_W2(19), NOP_, NOP_, NOP_);
    WAIT_BAR(3);
    UP_STEP(1, 0, 2, NOP_, NOP_, NOP_, NOP_, NOP_, NOP_);
    UP_STEP(0, 1, 3, NOP_, NOP_, NOP_, NOP_, NOP_, NOP_);
    UP_STEP(1, 0, 4, NOP_, NOP_, NOP_, NOP_, NOP_, NOP_);
    UP_STEP(0, 1, 5, NOP_, NOP_, NOP_, NOP_, NOP_, NOP_);
    WAIT_BAR(0);
    UP_STEP(1, 0, 6, NOP_, NOP_, NOP_, NOP_, NOP_, NOP_);
    UP_STEP(0, 1, 7, NOP_, NOP_, NOP_, NOP_, NOP_, NOP_);
#pragma unroll
    for (int i = 0; i < 6; ++i) acc[i] = MFMA(wf1[i % 3], xf1[i / 3], acc[i]);
    __builtin_amdgcn_s_setprio(3);
    const __amdgpu_buffer_rsrc_t rs = __builtin_amdgcn_make_buffer_rsrc(p.ws, 0, (int)WS_END, 0x00020000);
    const unsigned ho = (unsigned)WS_H + (unsigned)((rb2 * 4 + 2 * wr) * 96 + fs * 6 + 3 * wc) * 1024 + lane * 16;
#pragma unroll
    for (int j = 0; j < 6; ++j) {
        float hv[8];
#pragma unroll
        for (int r = 0; r < 8; ++r) hv[r] = __builtin_fmaxf(acc[j][r], 0.f) * acc[j][8 + r];
        st16_dev(rs, ho + (j / 3) * 96 * 1024 + (j % 3) * 1024, (u32x4){cvtpk(hv[0], hv[1]), cvtpk(hv[2], hv[3]), cvtpk(hv[4], hv[5]), cvtpk(hv[6], hv[7])});
    }
    WAIT_BAR(0);
    if (tid == 0) flag_set(p.ws, WS_HF + (rb2 * 4 + (fs >> 2)) * 128 + (fs & 3) * 8, tag);
    return tag;
}

#define MFMA16(a, b, c) __builtin_amdgcn_mfma_f32_16x16x32_bf16((a), (b), (c), 0, 0, 0)
DI void ld16_dev(bf16x8& d0, bf16x8& d1, const char* sbase, unsigned voff) {
    asm volatile("global_load_dwordx4 %0, %2, %3 sc1\n\tglobal_load_dwordx4 %1, %2, %3 offset:256 sc1" : "=&v"(d0), "=&v"(d1) : "v"(voff), "s"(sbase) : "memory");
}
struct OutOffs { unsigned wo_[5], wm_[5], wl_[5]; };
DI OutOffs out_offsets() {
    const int lane = threadIdx.x & 63;
    OutOffs o;
#pragma unroll
    for (int j = 0; j < 5; ++j) {
        const int idx = j * 64 + lane, r = idx / 10, ch = (idx % 10) * 16;
        o.wo_[j] = r * (D_ * 2) + ch; o.wm_[j] = ((r >> 4) * 20 + (r & 15)) * (D_ * 2) + ch; o.wl_[j] = ((r >> 2) * 20 + 16 + (r & 3)) * (D_ * 2) + ch;
        asm volatile("" : "+v"(o.wo_[j]), "+v"(o.wm_[j]), "+v"(o.wl_[j]));
    }
    return o;
}
DI void out_wg(const Params& p, int rb, int cn, u64 tag, const OutOffs& oo, char* lds) {
    const int tid = threadIdx.x, lane = tid & 63, w = __builtin_amdgcn_readfirstlane(tid >> 6);
    const int r16 = lane & 15, g = lane >> 4;
    const unsigned lds0 = (unsigned)(uintptr_t)lds + w * 20480;
    const unsigned (&wo_)[5] = oo.wo_; const unsigned (&wm_)[5] = oo.wm_; const unsigned (&wl_)[5] = oo.wl_;
    const u16* dsrc = p.wd + (size_t)w * 384 * D_ + cn * 80;
    const u16* osrc = p.wo + (size_t)w * 160 * D_ + cn * 80;
    const char* hsrc = p.ws + WS_H + ((size_t)(rb * 2) * 48 + w * 12) * 2048;
    const char* asrc = p.ws + WS_O + ((size_t)(rb * 2) * 40 + w * 10) * 1024;
    const unsigned lo16 = (g >> 1) * 1024 + (g & 1) * 512 + r16 * 16;
#define W_PIECES(s_, o_, d_) do { glds16(s_, o_[0], d_); glds16(s_, o_[1], d_ + 1024); glds16(s_, o_[2], d_ + 2048); glds16(s_, o_[3], d_ + 3072); glds16(s_, o_[4], d_ + 4096); } while (0)
#define W_DMA(u) do { const unsigned d_ = lds0 + ((u) & 3) * 5120; \
        if ((u) < 12) W_PIECES(dsrc + (size_t)(u) * 32 * D_, wo_, d_); else if ((u) < 16) W_PIECES(osrc + (size_t)((u) - 12) * 40 * D_, wm_, d_); else W_PIECES(osrc, wl_, d_); } while (0)
    __builtin_amdgcn_s_setprio(1);
    W_DMA(0); W_DMA(1);
    f32x4 acc[4][5];
    const f32x4 z4 = {0.f, 0.f, 0.f, 0.f};
#define ACC(u, rg, i) ((u) == 0 ? z4 : acc[rg][i])
    const lds_cptr wbase = (lds_cptr)lds + w * 20480 + (16 * (g >> 1) + 4 * (g & 1) + (r16 >> 2)) * 160 + (lane & 3) * 8;
    bf16x8 wf[2][5];
    bf16x8 af[5][4];
#define W_RD(u, i) wf[(u) & 1][i] = cat8(trread(wbase + ((u) & 3) * 5120 + (i) * 32), trread(wbase + ((u) & 3) * 5120 + (i) * 32 + 1280))
#define H_LD(u) do { ld16_dev(af[(u) & 3][0], af[(u) & 3][1], hsrc + (u) * 2048, lo16); ld16_dev(af[(u) & 3][2], af[(u) & 3][3], hsrc + 48 * 2048 + (u) * 2048, lo16); } while (0)
#define W_PIECE(u, i) glds16((u) < 12 ? (const void*)(dsrc + (size_t)(u) * 32 * D_) : (const void*)(osrc + (size_t)((u) - 12) * 40 * D_), (u) < 12 ? wo_[i] : wm_[i], lds0 + ((u) & 3) * 5120 + (i) * 1024)
#define UNIT_D(u, A) do { _Pragma("unroll") for (int i = 0; i < 5; ++i) { \
        acc[0][i] = MFMA16(wf[(u) & 1][i], A[0], ACC(u, 0, i)); acc[1][i] = MFMA16(wf[(u) & 1][i], A[1], ACC(u, 1, i)); W_PIECE((u) + 4, i); SBAR(); \
        acc[2][i] = MFMA16(wf[(u) & 1][i], A[2], ACC(u, 2, i)); acc[3][i] = MFMA16(wf[(u) & 1][i], A[3], ACC(u, 3, i)); W_RD((u) + 1, i); \
        if ((u) + 3 < 12 && i == 1) ld16_dev(af[((u) + 3) & 3][0], af[((u) + 3) & 3][1], hsrc + ((u) + 3) * 2048, lo16); \
        if ((u) + 3 < 12 && i == 3) ld16_dev(af[((u) + 3) & 3][2], af[((u) + 3) & 3][3], hsrc + 48 * 2048 + ((u) + 3) * 2048, lo16); \
        SBAR(); } } while (0)
#define UNIT(u, A) do { _Pragma("unroll") for (int i = 0; i < 5; ++i) { \
        _Pragma("unroll") for (int rg = 0; rg < 4; ++rg) acc[rg][i] = MFMA16(wf[(u) & 1][i], A[rg], acc[rg][i]); \
        if ((u) + 1 < 17) W_RD((u) + 1, i); SBAR(); } } while (0)
#define PINA(A) "+v"(A[0]), "+v"(A[1]), "+v"(A[2]), "+v"(A[3])
    {
        const char* fp = p.ws + ((lane & 4) ? WS_KVF + (blockIdx.x & 15) * 1024 + (rb * 4) * 8 : WS_HF + ((rb >> 1) * 4 + w) * 128) + (lane & 3) * 8;
#pragma unroll 1
        for (unsigned spin = 0; spin < SPIN_CAP; ++spin) { if (__all(ld8_dev(fp) == tag)) break; __builtin_amdgcn_s_sleep(12); }
    }
    WAIT_VM(0);
    float gv[4];
    asm volatile("global_load_dword %0, %4, %5 sc1\n\tglobal_load_dword %1, %4, %5 offset:64 sc1\n\tglobal_load_dword %2, %4, %5 offset:128 sc1\n\tglobal_load_dword %3, %4, %5 offset:192 sc1"
                 : "=&v"(gv[0]), "=&v"(gv[1]), "=&v"(gv[2]), "=&v"(gv[3]) : "v"(r16 * 4), "s"(p.ws + WS_G + rb * 256) : "memory");
    H_LD(0); W_DMA(2); H_LD(1); W_DMA(3); H_LD(2);
#pragma unroll
    for (int i = 0; i < 5; ++i) W_RD(0, i);
    __builtin_amdgcn_s_setprio(1);
#pragma unroll
    for (int u = 0; u < 12; ++u) {
        if (u == 0) asm volatile("s_waitcnt vmcnt(18) lgkmcnt(0)" : PINA(af[0]), PINA(gv) :: "memory");
        else if (u <= 9) asm volatile("s_waitcnt vmcnt(18) lgkmcnt(0)" : PINA(af[u & 3]) :: "memory");
        else if (u == 10) asm volatile("s_waitcnt vmcnt(14) lgkmcnt(0)" : PINA(af[u & 3]) :: "memory");
        else asm volatile("s_waitcnt vmcnt(10) lgkmcnt(0)" : PINA(af[u & 3]) :: "memory");
        SBAR();
        UNIT_D(u, af[u & 3]);
    }
    __builtin_amdgcn_s_setprio(3);
    asm volatile("s_waitcnt lgkmcnt(0)" ::: "memory");
    W_DMA(16);
#pragma unroll
    for (int rg = 0; rg < 4; ++rg) {
        const float gm = __uint_as_float(cvtpk(1.f - gv[rg], 0.f) << 16);
#pragma unroll
        for (int i = 0; i < 5; ++i) acc[rg][i] *= gm;
    }
    flags_wait<1, 6>(p.ws + WS_OF + (rb * 4 + w) * 128, 8, tag, lane);
#pragma unroll
    for (int v = 0; v < 5; ++v) { ld16_dev(af[v][0], af[v][1], asrc + v * 2048, lo16); ld16_dev(af[v][2], af[v][3], asrc + 40 * 1024 + v * 2048, lo16); }
    asm volatile("s_waitcnt vmcnt(16)" : PINA(af[0]) :: "memory"); SBAR(); UNIT(12, af[0]);
    asm volatile("s_waitcnt vmcnt(12)" : PINA(af[1]) :: "memory"); SBAR(); UNIT(13, af[1]);
    asm volatile("s_waitcnt vmcnt(8)" : PINA(af[2]) :: "memory"); SBAR(); UNIT(14, af[2]);
    asm volatile("s_waitcnt vmcnt(4)" : PINA(af[3]) :: "memory"); SBAR(); UNIT(15, af[3]);
    asm volatile("s_waitcnt vmcnt(0)" : PINA(af[4]) :: "memory"); SBAR(); UNIT(16, af[4]);
    LAS f32x4* mine = (LAS f32x4*)((lds_ptr)lds + w * 20480);
#pragma unroll
    for (int rg = 0; rg < 4; ++rg)
#pragma unroll
        for (int i = 0; i < 5; ++i) mine[(rg * 5 + i) * 64 + lane] = acc[rg][i];
    WAIT_BAR(0);
    const LAS f32x4* all = (const LAS f32x4*)lds + w * 5 * 64 + lane;
    u16* orow = p.out + (size_t)(rb * 64 + w * 16 + r16) * D_ + cn * 80 + 4 * g;
#pragma unroll
    for (int i = 0; i < 5; ++i) {
        const f32x4 s = (all[i * 64] + all[1280 + i * 64]) + (all[2560 + i * 64] + all[3840 + i * 64]);
        st8_dev(orow + 16 * i, (u32x2){cvtpk(s[0], s[1]), cvtpk(s[2], s[3])});
    }
}

__global__ __launch_bounds__(256, 2) void k_fused(Params p) {
    extern __shared__ __attribute__((aligned(16))) char lds[];
    const int b = blockIdx.x, j = b & 7, i = (b >> 3) & 31;
    const u64 ep = epoch_load(p.ws + WS_EPOCH);
    if (b < 256) {
        __builtin_amdgcn_s_setprio(2);
        attn_wg(p, 16 * (j >> 2) + (i >> 1), 2 * (j & 3) + (i & 1), ep, lds);
    } else {
        if (i < 16) { __builtin_amdgcn_s_setprio(3); kv_job(p, 8 * j + (i >> 1), i & 1, ep, lds); __builtin_amdgcn_s_setprio(1); }
        const OutOffs oo = out_offsets();
        const u64 tag = ffn_up_wg(p, 8 * (j >> 2) + (i >> 2), 4 * (j & 3) + (i & 3), ep, lds);
        out_wg(p, 4 * j + (i >> 3), i & 7, tag, oo, lds);
    }
}

extern "C" void kernel_launch(void* const* d_in, const int* in_sizes, int n_in, void* d_out, int out_size, void* d_ws, size_t ws_size,
                              hipStream_t stream) {
    Params p;
    p.x = (const u16*)d_in[0]; p.wq = (const u16*)d_in[1]; p.wk = (const u16*)d_in[2]; p.wv = (const u16*)d_in[3]; p.wo = (const u16*)d_in[4];
    p.wg = (const u16*)d_in[6]; p.wu = (const u16*)d_in[7]; p.wd = (const u16*)d_in[8]; p.gate_w = (const u16*)d_in[9];
    p.out = (u16*)d_out; p.ws = (char*)d_ws;
    (void)hipFuncSetAttribute((const void*)k_fused, hipFuncAttributeMaxDynamicSharedMemorySize, LDS_BYTES);
    k_fused<<<512, 256, LDS_BYTES, stream>>>(p);
}
```

```cpp
#include <hip/hip_runtime.h>
#include <hip/hip_bf16.h>
#include <stdint.h>

typedef __attribute__((ext_vector_type(8))) short bf16x8;
typedef __attribute__((ext_vector_type(4))) short s16x4;
typedef __attribute__((ext_vector_type(16))) float f32x16;
typedef __attribute__((ext_vector_type(4))) float f32x4;
typedef __attribute__((ext_vector_type(2))) float f32x2;
typedef __attribute__((ext_vector_type(4))) unsigned u32x4;
typedef __attribute__((ext_vector_type(2))) unsigned u32x2;
typedef __attribute__((ext_vector_type(2))) __bf16 bf16x2_t;
typedef short v4i16_t __attribute__((ext_vector_type(4)));
typedef unsigned short u16;
typedef unsigned long long u64;
#define LAS __attribute__((address_space(3)))
#define GAS __attribute__((address_space(1)))
typedef LAS const char* lds_cptr;
typedef LAS char* lds_ptr;
#define DI __device__ __forceinline__

constexpr int D_ = 640, F_ = 1536;
constexpr int KV_TILE_B = 2624;
constexpr int KV_CHUNK_B = 4 * KV_TILE_B;
constexpr int KV_SLOT_B = 12288;
constexpr size_t WS_EPOCH = 0, WS_SEEN = 1024, WS_HF = 8192, WS_KVF = 16384, WS_OF = 32768;
constexpr size_t WS_KV = 64 * 1024;
constexpr size_t WS_G = 240 * 1024;
constexpr size_t WS_H = 256 * 1024;
constexpr size_t WS_O = WS_H + (size_t)64 * 48 * 2048;
constexpr size_t WS_END = WS_O + (size_t)64 * 40 * 1024;
constexpr int LDS_BYTES = 80 * 1024;
constexpr unsigned SPIN_CAP = 1u << 18;

#define MFMA(a, b, c) __builtin_amdgcn_mfma_f32_32x32x16_bf16((a), (b), (c), 0, 0, 0)
#define SBAR() __builtin_amdgcn_sched_barrier(0)
#define WAIT_VM(N) asm volatile("s_waitcnt vmcnt(" #N ")" ::: "memory")
#define WAIT_BAR(N) asm volatile("s_waitcnt vmcnt(" #N ") lgkmcnt(0)\n\ts_barrier" ::: "memory")
#define RLX_AGENT __ATOMIC_RELAXED, __HIP_MEMORY_SCOPE_AGENT
#define BC(x) __builtin_bit_cast(bf16x8, x)

DI unsigned cvtpk(float lo, float hi) {
    f32x2 v = {lo, hi};
    return __builtin_bit_cast(unsigned, __builtin_convertvector(v, bf16x2_t));
}
DI void glds16(const void* sbase, unsigned voff, unsigned lds_base) {
    unsigned sv;
    asm volatile("s_mov_b32 %0, m0\n\ts_mov_b32 m0, %3\n\ts_nop 0\n\tglobal_load_lds_dwordx4 %1, %2\n\ts_mov_b32 m0, %0"
                 : "=&s"(sv) : "v"(voff), "s"(sbase), "s"(lds_base) : "memory");
}
DI void glds16v(const void* ptr, unsigned lds_base) {
    unsigned sv;
    asm volatile("s_mov_b32 %0, m0\n\ts_mov_b32 m0, %2\n\ts_nop 0\n\tglobal_load_lds_dwordx4 %1, off\n\ts_mov_b32 m0, %0"
                 : "=&s"(sv) : "v"(ptr), "s"(lds_base) : "memory");
}
DI void glds16_dev(const void* sbase, unsigned voff, unsigned lds_base) {
    unsigned sv;
    asm volatile("s_mov_b32 %0, m0\n\ts_mov_b32 m0, %3\n\ts_nop 0\n\tglobal_load_lds_dwordx4 %1, %2 sc1\n\ts_mov_b32 m0, %0"
                 : "=&s"(sv) : "v"(voff), "s"(sbase), "s"(lds_base) : "memory");
}
DI s16x4 trread(lds_cptr p) { return __builtin_bit_cast(s16x4, __builtin_amdgcn_ds_read_tr16_b64_v4i16((LAS v4i16_t*)p)); }
DI bf16x8 cat8(s16x4 lo, s16x4 hi) { return __builtin_shufflevector(lo, hi, 0, 1, 2, 3, 4, 5, 6, 7); }
DI bf16x8 ldsread16(lds_cptr p) { return *(const LAS bf16x8*)p; }
DI float xhalf_max(float m) {
    auto rr = __builtin_amdgcn_permlane32_swap(__float_as_uint(m), __float_as_uint(m), false, false);
    return __builtin_fmaxf(__uint_as_float(rr[0]), __uint_as_float(rr[1]));
}
DI f32x16 zero16() { f32x16 z; for (int i = 0; i < 16; ++i) z[i] = 0.f; return z; }
DI void st16_dev(__amdgpu_buffer_rsrc_t r, unsigned off, u32x4 v) { __builtin_amdgcn_raw_buffer_store_b128(v, r, off, 0, 16); }
DI void st8_dev(void* p, u32x2 v) { __hip_atomic_store((GAS u64*)p, (u64)v[0] | ((u64)v[1] << 32), RLX_AGENT); }
DI void st4_dev(void* p, float v) { __hip_atomic_store((GAS unsigned*)p, __float_as_uint(v), RLX_AGENT); }
DI u64 ld8_dev(const void* p) { return __hip_atomic_load((const GAS u64*)p, RLX_AGENT); }
DI void flag_set(char* ws, size_t off, u64 tag) { __hip_atomic_store((GAS u64*)(ws + off), tag, RLX_AGENT); }
DI u64 epoch_load(const char* ws) { u64 e; asm volatile("s_load_dwordx2 %0, %1, 0x0" : "=s"(e) : "s"(ws) : "memory"); return e; }
DI u64 epoch_tag(u64 e) { u64 r; asm volatile("s_waitcnt lgkmcnt(0)\n\ts_mov_b64 %0, %1" : "=s"(r) : "s"(e) : "memory"); return r * 6364136223846793005ull + 1442695040888963407ull; }
template <int K, int SLEEP> DI void flags_wait(const char* f, int n, u64 tag, int lane) {
#pragma unroll 1
    for (unsigned spin = 0; spin < SPIN_CAP; ++spin) {
        bool ok = true;
#pragma unroll
        for (int k = 0; k < K; ++k) { const int i = lane + 64 * k; const u64 v = ld8_dev(f + 8 * (i < n ? i : 0)); ok &= (v == tag); }
        if (__all(ok)) break;
        __builtin_amdgcn_s_sleep(SLEEP);
    }
}

struct Params {
    const u16* x; const u16* wq; const u16* wk; const u16* wv; const u16* wo;
    const u16* wg; const u16* wu; const u16* wd; const u16* gate_w;
    u16* out; char* ws;
};

DI void kv_job(const Params& p, int rt, int isv, u64 ep, char* lds) {
    const int tid = threadIdx.x, lane = tid & 63, w = __builtin_amdgcn_readfirstlane(tid >> 6);
    const int r32 = lane & 31, h = lane >> 5;
    const unsigned lds0 = (unsigned)(uintptr_t)lds;
    const unsigned xoff = w * 17408, woff = xoff + 10240;
    const u16* xs = p.x + (size_t)rt * 32 * D_ + w * 160;
#pragma unroll
    for (int i = 0; i < 10; ++i) {
        const int idx = i * 64 + lane, row = idx / 20, ch = (idx % 20) ^ ((row >> 2) & 3);
        glds16(xs, (unsigned)(row * (D_ * 2) + ch * 16), lds0 + xoff + i * 1024);
    }
    const u16* wsrc = (isv ? p.wv : p.wk);
#pragma unroll
    for (int i = 0; i < 6; ++i) glds16(wsrc, (unsigned)(w * 6400 + i * 1024 + lane * 16), lds0 + woff + i * 1024);
    glds16v(lane < 16 ? (const char*)wsrc + w * 6400 + 6144 + lane * 16 : (const char*)p.gate_w + w * 320 + (lane < 36 ? lane - 16 : 0) * 16, lds0 + woff + 6144);
    const u64 tag = epoch_tag(ep);
    WAIT_VM(0);
    const lds_cptr xb0 = (lds_cptr)lds + xoff + r32 * 320 + ((h ^ ((r32 >> 2) & 3)) << 4), xb1 = (lds_cptr)lds + xoff + r32 * 320 + (((2 + h) ^ ((r32 >> 2) & 3)) << 4);
    const lds_cptr wb = (lds_cptr)lds + woff + (8 * h + ((lane & 15) >> 2)) * 40 + ((lane >> 4) & 1) * 32 + (lane & 3) * 8;
    const lds_cptr gb = (lds_cptr)lds + woff + 6400 + h * 16;
    bf16x8 xf[10], wf[10], gf[10];
#pragma unroll
    for (int ks = 0; ks < 10; ++ks) { xf[ks] = ldsread16(((ks & 1) ? xb1 : xb0) + (ks >> 1) * 64); wf[ks] = cat8(trread(wb + ks * 640), trread(wb + ks * 640 + 160)); gf[ks] = ldsread16(gb + ks * 32); }
    SBAR();
    f32x16 acc = zero16();
    if (isv) {
#pragma unroll
        for (int ks = 0; ks < 10; ++ks) acc = MFMA(xf[ks], wf[ks], acc);
    } else {
#pragma unroll
        for (int ks = 0; ks < 10; ++ks) acc = MFMA((r32 == 20) ? gf[ks] : wf[ks], xf[ks], acc);
    }
    LAS f32x4* red = (LAS f32x4*)((lds_ptr)lds + 69632);
    if (w > 0) {
#pragma unroll
        for (int g = 0; g < 4; ++g) red[((w - 1) * 4 + g) * 64 + lane] = (f32x4){acc[4 * g], acc[4 * g + 1], acc[4 * g + 2], acc[4 * g + 3]};
    }
    WAIT_BAR(0);
    if (w == 0) {
#pragma unroll
        for (int ww = 0; ww < 3; ++ww)
#pragma unroll
            for (int g = 0; g < 4; ++g) { const f32x4 t = red[(ww * 4 + g) * 64 + lane]; acc[4 * g] += t[0]; acc[4 * g + 1] += t[1]; acc[4 * g + 2] += t[2]; acc[4 * g + 3] += t[3]; }
    }
    WAIT_BAR(0);
    if (w == 0) {
        const __amdgpu_buffer_rsrc_t rs = __builtin_amdgcn_make_buffer_rsrc(p.ws, 0, (int)WS_END, 0x00020000);
        const unsigned tile = (unsigned)WS_KV + (unsigned)(isv ? rt : ((rt + 63) & 63)) * KV_TILE_B;
        u32x4 lo = {cvtpk(acc[0], acc[1]), cvtpk(acc[2], acc[3]), cvtpk(acc[4], acc[5]), cvtpk(acc[6], acc[7])};
        u32x4 hi = {cvtpk(acc[8], acc[9]), cvtpk(acc[10], acc[11]), cvtpk(acc[12], acc[13]), cvtpk(acc[14], acc[15])};
        if (!isv) {
            st16_dev(rs, tile + lane * 16, lo);
            if (h == 0) st8_dev(p.ws + tile + 1024 + r32 * 8, (u32x2){hi[0], hi[1]});
            else { const float lg = __uint_as_float(cvtpk(acc[8], 0.f) << 16);
                   st4_dev(p.ws + WS_G + (rt * 32 + r32) * 4, __uint_as_float(cvtpk(1.f / (1.f + __expf(-lg)), 0.f) << 16)); }
        } else {
            if (r32 == 20) { lo = (u32x4){0x3f803f80u, 0x3f803f80u, 0x3f803f80u, 0x3f803f80u}; hi = lo; }
            if (r32 <= 20) { st16_dev(rs, tile + 1280 + h * 336 + r32 * 16, lo); st16_dev(rs, tile + 1952 + h * 336 + r32 * 16, hi); }
        }
        WAIT_VM(0);
        if (lane < 16) flag_set(p.ws, WS_KVF + lane * 1024 + (rt * 2 + isv) * 8, tag);
    }
}


#define AE(b, i) "v_exp_f32 v[" #b "+" #i "], v[" #b "+" #i "]\n"
#define AC(pb, j, b, i) "v_cvt_pk_bf16_f32 v[" #pb "+" #j "], v[" #b "+" #i "], v[" #b "+" #i "+1]\n"
#define AG2(MA, MB, X, XI, PX, PJ) MA AE(X, XI) AE(X, XI+1) AE(X, XI+2) AE(X, XI+3) AC(PX, PJ, X, XI) AC(PX, PJ+1, X, XI+2) \
    MB AE(X, XI+4) AE(X, XI+5) AE(X, XI+6) AE(X, XI+7) AC(PX, PJ+2, X, XI+4) AC(PX, PJ+3, X, XI+6)
#define AHALF(X, PX, Y, Q1, Q2, O, PR, BUF, R1, R2, R3, R4) \
    AG2("v_mfma_f32_32x32x16_bf16 v[" #Y ":" #Y "+15], v[" #BUF ":" #BUF "+3], " Q1 ", 0\n" R1, \
        "v_mfma_f32_32x32x16_bf16 v[" #Y ":" #Y "+15], v[" #BUF "+4:" #BUF "+7], " Q2 ", v[" #Y ":" #Y "+15]\n" R2, X, 0, PX, 0) \
    AG2("v_mfma_f32_32x32x16_bf16 " O ", v[" #BUF "+8:" #BUF "+11], v[" #PR ":" #PR "+3], " O "\n" R3, \
        "v_mfma_f32_32x32x16_bf16 " O ", v[" #BUF "+12:" #BUF "+15], v[" #PR "+4:" #PR "+7], " O "\n" R4, X, 8, PX, 4)
#define ARK1(NB, sl, tl) "ds_read_b128 v[" #NB ":" #NB "+3], %[k1a] offset:4096+(" #sl ")*12288+" #tl "*2624\n"
#define ARK2(NB, sl, tl) "ds_read_b64 v[" #NB "+4:" #NB "+5], %[k2a] offset:4096+(" #sl ")*12288+" #tl "*2624\n"
#define ARV0(NB, sl, tl) "ds_read_b128 v[" #NB "+8:" #NB "+11], %[va] offset:4096+(" #sl ")*12288+" #tl "*2624\n"
#define ARV1(NB, sl, tl) "ds_read_b128 v[" #NB "+12:" #NB "+15], %[va] offset:4096+(" #sl ")*12288+" #tl "*2624+672\n"
#define AH1(PREV, R1, R2, R3, R4) AHALF(128, 160, 144, "%[qb1]", "%[qb2]", "%[ob]", 168, PREV, R1, R2, R3, R4)
#define AH2(CUR, PREV, sl, tl) AHALF(144, 168, 128, "%[qa1]", "%[qa2]", "%[oa]", 160, CUR, ARK1(PREV, sl, tl), ARK2(PREV, sl, tl), ARV0(PREV, sl, tl), ARV1(PREV, sl, tl))
#define AWL "s_waitcnt lgkmcnt(0)\n"
#define ASYNC "s_waitcnt vmcnt(6) lgkmcnt(0)\ns_barrier\n"
#define ADMA0(fs) "s_mul_i32 %[of], %[ck], 10496\n s_add_u32 %[ck], %[ck], 1\n s_add_u32 m0, %[ldst], " #fs "*12288\n" \
                  "v_add_u32 v208, %[of], %[kvo]\n"
#define ADMA1 "global_load_lds_dwordx4 v208, %[img] sc1\n"
#define ADMA2 "global_load_lds_dwordx4 v208, %[img] offset:1024 sc1\n"
#define ADMA3 "global_load_lds_dwordx4 v208, %[img] offset:2048 sc1\n"
#define ACHUNK(sl, nsl) \
    AH1(192, "", "", "", "") AWL AH2(176, 192, sl, 2) \
    AH1(176, "", "", "", "") AWL AH2(192, 176, sl, 3) \
    AH1(192, "", "", "", "") ASYNC AH2(176, 192, nsl, 0) \
    AH1(176, ADMA0(sl), ADMA1, ADMA2, ADMA3) AWL AH2(192, 176, nsl, 1)
#define ACHUNKT(sl, nsl, W) \
    AH1(192, "", "", "", "") AWL AH2(176, 192, sl, 2) \
    AH1(176, "", "", "", "") AWL AH2(192, 176, sl, 3) \
    AH1(192, "", "", "", "") "s_waitcnt vmcnt(" #W ") lgkmcnt(0)\ns_barrier\n" AH2(176, 192, nsl, 0) \
    AH1(176, "", "", "", "") AWL AH2(192, 176, nsl, 1)
#define ASP1(NB) "ds_read_b128 v[" #NB "+8:" #NB "+11], %[va]\n"
#define ASP2(NB) "ds_read_b128 v[" #NB "+12:" #NB "+15], %[va] offset:672\n"
#define ACHUNKL(sl) \
    AH1(192, "", "", "", "") AWL AH2(176, 192, sl, 2) \
    AH1(176, "", "", "", "") AWL AH2(192, 176, sl, 3) \
    AH1(192, "", "", "", "") AWL AHALF(144, 168, 128, "%[qa1]", "%[qa2]", "%[oa]", 160, 176, ASP1(192), ASP2(192), "", "") \
    AH1(176, "", "", "", "") AWL AHALF(144, 168, 128, "%[qa1]", "%[qa2]", "%[oa]", 160, 192, "", "", "", "")
#define AMOV(d, s) "v_mov_b32 v" #d ", " s "\n"
#define ATTN_LOOP_ASM \
    "s_setprio 2\n s_mov_b32 %[m0s], m0\n s_mov_b32 %[it], 3\n s_mov_b32 %[ck], 4\n" \
    ARK1(176, 1, 1) ARK2(176, 1, 1) ARV0(176, 1, 1) ARV1(176, 1, 1) \
    "v_mov_b64 v[192:193], %[k0a]\n v_mov_b64 v[194:195], %[k0b]\n" \
    "v_mov_b64 v[196:197], %[k02]\n" AMOV(198, "%[kone]") AMOV(199, "0") AMOV(182, "%[kone]") AMOV(183, "0") \
    "v_mfma_f32_32x32x16_bf16 v[128:143], v[192:195], %[qa1], 0\n" \
    AMOV(200, "0") AMOV(201, "0") AMOV(202, "0") AMOV(203, "0") AMOV(204, "0") AMOV(205, "0") AMOV(206, "0") AMOV(207, "0") \
    AMOV(168, "0") AMOV(169, "0") AMOV(170, "0") AMOV(171, "0") AMOV(172, "0") AMOV(173, "0") AMOV(174, "0") AMOV(175, "0") \
    "v_mfma_f32_32x32x16_bf16 v[128:143], v[196:199], %[qa2], v[128:143]\n" \
    "s_nop 7\n s_nop 7\n" \
    "1:\n" \
    ACHUNK(1, 2) ACHUNK(2, 3) ACHUNK(3, 0) ACHUNK(0, 1) \
    "s_sub_u32 %[it], %[it], 1\n s_cmp_lg_u32 %[it], 0\n s_cbranch_scc1 1b\n" \
    ACHUNKT(1, 2, 6) ACHUNKT(2, 3, 3) ACHUNKT(3, 0, 0) ACHUNKL(0) "s_nop 1\n" \
    "v_mfma_f32_32x32x16_bf16 %[ob], v[200:203], v[168:171], %[ob]\n" \
    "v_mfma_f32_32x32x16_bf16 %[ob], v[204:207], v[172:175], %[ob]\n" \
    "s_setprio 3\n s_mov_b32 m0, %[m0s]\n s_nop 7\n s_nop 7\n"
#define ATTN_CLOBBERS "v128","v129","v130","v131","v132","v133","v134","v135","v136","v137","v138","v139","v140","v141","v142","v143", \
    "v144","v145","v146","v147","v148","v149","v150","v151","v152","v153","v154","v155","v156","v157","v158","v159", \
    "v160","v161","v162","v163","v164","v165","v166","v167","v168","v169","v170","v171","v172","v173","v174","v175", \
    "v176","v177","v178","v179","v180","v181","v182","v183","v184","v185","v186","v187","v188","v189","v190","v191", \
    "v192","v193","v194","v195","v196","v197","v198","v199","v200","v201","v202","v203","v204","v205","v206","v207","v208"

DI void attn_wg(const Params& p, int rb, int c, u64 ep, char* lds) {
    const int tid = threadIdx.x, lane = tid & 63, w = __builtin_amdgcn_readfirstlane(tid >> 6);
    const int r32 = lane & 31, h = lane >> 5;
    const unsigned lds0 = (unsigned)(uintptr_t)lds;
    const char* kvimg = p.ws + WS_KV;
    constexpr int RB = 32768;
    const unsigned kvoff = (unsigned)(w * 3072 + lane * 16);
#define KV_PIECE(chunk, j) glds16_dev(kvimg + (size_t)(chunk) * KV_CHUNK_B, kvoff + (j) * 1024, lds0 + RB + (((chunk) + 1) & 3) * KV_SLOT_B + w * 3072 + (j) * 1024)
    float ga, gb;
#define G_LOAD() asm volatile("global_load_dword %0, %2, %3 sc1\n\tglobal_load_dword %1, %2, %3 offset:128 sc1" : "=&v"(ga), "=&v"(gb) : "v"(r32 * 4), "s"(p.ws + WS_G + rb * 256) : "memory")
    constexpr int QS_B = 18432;
    unsigned qoff[5]; unsigned qdst[5];
#pragma unroll
    for (int j = 0; j < 5; ++j) {
        int i = w + 4 * j; if (i >= 18) i -= 2;
        if (j < 2) { const int row = 8 * i + (lane >> 3), pos = lane & 7, ch = pos ^ ((row >> 1) & 7); qoff[j] = row * (D_ * 2) + ch * 16; }
        else { const int idx = (i - 8) * 64 + lane, kr = idx / 10, ch = idx % 10; qoff[j] = kr * (D_ * 2) + ch * 16; }
        qdst[j] = lds0 + i * 1024;
    }
    const u16* xsrc = p.x + (size_t)rb * 64 * D_;
    const u16* wsrc = p.wq + c * 80;
#define Q_DMA(kt) do { const unsigned d_ = ((kt) & 3) * QS_B; const u16* xs_ = xsrc + (kt) * 64; const u16* ws_ = wsrc + (size_t)(kt) * 64 * D_; \
        glds16(xs_, qoff[0], d_ + qdst[0]); glds16(xs_, qoff[1], d_ + qdst[1]); glds16(ws_, qoff[2], d_ + qdst[2]); glds16(ws_, qoff[3], d_ + qdst[3]); glds16(ws_, qoff[4], d_ + qdst[4]); } while (0)
#define Q_PIECE(kt, j) glds16((j) < 2 ? xsrc + (kt) * 64 : wsrc + (size_t)(kt) * 64 * D_, qoff[j], ((kt) & 3) * QS_B + qdst[j])
    Q_DMA(0); Q_DMA(1); Q_DMA(2); Q_DMA(3);
    const u64 tag = epoch_tag(ep);
    f32x16 qa = zero16(), qb = zero16();
    {
        unsigned xa[4];
#pragma unroll
        for (int ks = 0; ks < 4; ++ks) xa[ks] = r32 * 128 + (((2 * ks + h) ^ ((r32 >> 1) & 7)) << 4);
        const unsigned wa = 8192 + (8 * h + ((lane & 15) >> 2)) * 160 + w * 40 + ((lane >> 4) & 1) * 32 + (lane & 3) * 8;
        bf16x8 wfA[4], x0A[4], x1A[4], wfB[4], x0B[4], x1B[4];
#define Q_RD(S, kt, ks) do { const lds_cptr sb_ = (lds_cptr)lds + ((kt) & 3) * QS_B; wf##S[ks] = cat8(trread(sb_ + wa + (ks) * 2560), trread(sb_ + wa + (ks) * 2560 + 640)); \
            x0##S[ks] = ldsread16(sb_ + xa[ks]); x1##S[ks] = ldsread16(sb_ + xa[ks] + 4096); } while (0)
#define Q_STEP(C, N, kt) do { \
            if ((kt) == 7 && w == 0) { flags_wait<2, 4>(p.ws + WS_KVF + (blockIdx.x & 15) * 1024, 128, tag, lane); }     \
            if ((kt) < 7) { WAIT_BAR(10); } else if ((kt) < 9) { WAIT_BAR(5); } \
            _Pragma("unroll") for (int ks = 0; ks < 4; ++ks) { qa = MFMA(wf##C[ks], x0##C[ks], qa); if ((kt) + 1 < 10) Q_RD(N, (kt) + 1, ks); SBAR(); qb = MFMA(wf##C[ks], x1##C[ks], qb); \
                if ((kt) + 4 < 10) { Q_PIECE((kt) + 4, ks); if (ks == 3) Q_PIECE((kt) + 4, 4); } \
                if ((kt) == 7) { if (ks < 3) KV_PIECE(0, ks); else G_LOAD(); } \
                if ((kt) == 8) KV_PIECE(1 + ks / 3, ks % 3); \
                if ((kt) == 9 && ks < 2) KV_PIECE(2, ks + 1); \
                SBAR(); } } while (0)
        WAIT_BAR(15);
        if (tid == 0) flag_set(p.ws, WS_SEEN + blockIdx.x * 8, tag);
#pragma unroll
        for (int ks = 0; ks < 4; ++ks) Q_RD(A, 0, ks);
#pragma unroll
        for (int kt = 0; kt < 10; kt += 2) { Q_STEP(A, B, kt); Q_STEP(B, A, kt + 1); }
    }
    constexpr float C2 = 0.22360679774997896f * 1.4426950408889634f;
    u32x4 qa1, qa2, qb1, qb2;
    qa1 = (u32x4){cvtpk(qa[0] * C2, qa[1] * C2), cvtpk(qa[2] * C2, qa[3] * C2), cvtpk(qa[4] * C2, qa[5] * C2), cvtpk(qa[6] * C2, qa[7] * C2)};
    qb1 = (u32x4){cvtpk(qb[0] * C2, qb[1] * C2), cvtpk(qb[2] * C2, qb[3] * C2), cvtpk(qb[4] * C2, qb[5] * C2), cvtpk(qb[6] * C2, qb[7] * C2)};
    qa2 = (u32x4){h ? 0u : cvtpk(qa[8] * C2, qa[9] * C2), h ? 0u : cvtpk(qa[10] * C2, qa[11] * C2), 0u, 0u};
    qb2 = (u32x4){h ? 0u : cvtpk(qb[8] * C2, qb[9] * C2), h ? 0u : cvtpk(qb[10] * C2, qb[11] * C2), 0u, 0u};
    f32x16 oa = zero16(), ob = zero16();
    const unsigned k1a = lane * 16, k2a = 1024 + r32 * 8;
    const unsigned va = 1280 + h * 336 + (r32 <= 20 ? r32 : (r32 < 28 ? 20 : 16)) * 16;
    const unsigned kone = h ? 0u : 0x3f80u;
    asm volatile("" : "+v"(oa), "+v"(ob), "+v"(qa1), "+v"(qa2), "+v"(qb1), "+v"(qb2));
    WAIT_BAR(8);
    const bf16x8 k01 = ldsread16((lds_cptr)lds + RB + KV_SLOT_B + k1a);
    const u32x2 k02 = *(const LAS u32x2*)((lds_cptr)lds + RB + KV_SLOT_B + k2a);
    glds16_dev(kvimg, (unsigned)(w * 1024 + lane * 16), lds0 + RB - 4096 + w * 1024);
    auto kfrag2 = [](u32x2 v, unsigned one) { return __builtin_bit_cast(bf16x8, (u32x4){v[0], v[1], one, 0u}); };
    {
        f32x16 sa = MFMA(k01, BC(qa1), zero16()); KV_PIECE(3, 0); SBAR();
        f32x16 sb = MFMA(k01, BC(qb1), zero16()); KV_PIECE(3, 1); SBAR();
        sa = MFMA(kfrag2(k02, kone), BC(qa2), sa); sb = MFMA(kfrag2(k02, kone), BC(qb2), sb); KV_PIECE(3, 2); SBAR();
        float ma = sa[0], mb = sb[0];
#pragma unroll
        for (int r = 1; r < 16; ++r) { ma = __builtin_fmaxf(ma, sa[r]); mb = __builtin_fmaxf(mb, sb[r]); }
        ma = xhalf_max(ma); mb = xhalf_max(mb);
        qa2[2] = h ? 0u : (cvtpk(-ma, 0.f) & 0xffffu);
        qb2[2] = h ? 0u : (cvtpk(-mb, 0.f) & 0xffffu);
    }
    {
        unsigned t_m0, t_it, t_ck, t_of;
        const u32x4 k0u = __builtin_bit_cast(u32x4, k01); const u32x2 k0a = {k0u[0], k0u[1]}, k0b = {k0u[2], k0u[3]};
        asm volatile(ATTN_LOOP_ASM
            : [oa] "+v"(oa), [ob] "+v"(ob), "+v"(ga), "+v"(gb), [m0s] "=&s"(t_m0), [it] "=&s"(t_it), [ck] "=&s"(t_ck), [of] "=&s"(t_of)
            : [qa1] "v"(qa1), [qa2] "v"(qa2), [qb1] "v"(qb1), [qb2] "v"(qb2), [k1a] "v"(lds0 + RB - 4096 + k1a), [k2a] "v"(lds0 + RB - 4096 + k2a), [va] "v"(lds0 + RB - 4096 + va),
              [kvo] "v"(kvoff), [img] "s"(kvimg), [ldst] "s"(lds0 + RB + w * 3072), [k0a] "v"(k0a), [k0b] "v"(k0b), [k02] "v"(k02), [kone] "v"(kone)
            : ATTN_CLOBBERS, "scc", "memory");
    }
    {
        float chk = 0.f;
#pragma unroll
        for (int r = 0; r < 8; ++r) chk += oa[r] * 0.f + ob[r] * 0.f;
        if (h == 0) { chk += (oa[8] + oa[9] + oa[10] + oa[11] + ob[8] + ob[9] + ob[10] + ob[11]) * 0.f; } else { chk += (oa[8] + ob[8]) * 0.f; }
        if (h == 1 && !(oa[8] > 1e-30f && ob[8] > 1e-30f && oa[8] < 1e30f && ob[8] < 1e30f)) chk = 1.f;
        if (__builtin_expect(__any(!(chk == 0.f)), 0)) {
            __builtin_amdgcn_fence(__ATOMIC_ACQUIRE, "agent");
            qa2[2] = 0u; qb2[2] = 0u;
            oa = zero16(); ob = zero16();
            float ma = -INFINITY, mb = -INFINITY;
            for (int t = 0; t < 64; ++t) {
                const char* tb = kvimg + (size_t)t * KV_TILE_B;
                const char* kb = kvimg + (size_t)((t + 63) & 63) * KV_TILE_B;
                const bf16x8 k1 = *(const bf16x8*)(kb + k1a), k2 = kfrag2(*(const u32x2*)(kb + k2a), kone);
                const bf16x8 v0 = *(const bf16x8*)(tb + va), v1 = *(const bf16x8*)(tb + va + 672);
                f32x16 sa = MFMA(k1, BC(qa1), zero16()); sa = MFMA(k2, BC(qa2), sa);
                f32x16 sb = MFMA(k1, BC(qb1), zero16()); sb = MFMA(k2, BC(qb2), sb);
                float ta = ma, tb_ = mb;
#pragma unroll
                for (int r = 0; r < 16; ++r) { ta = __builtin_fmaxf(ta, sa[r]); tb_ = __builtin_fmaxf(tb_, sb[r]); }
                ta = xhalf_max(ta); tb_ = xhalf_max(tb_);
                const float fa = __builtin_amdgcn_exp2f(ma - ta), fb = __builtin_amdgcn_exp2f(mb - tb_);
                ma = ta; mb = tb_;
#pragma unroll
                for (int r = 0; r < 16; ++r) { oa[r] *= fa; ob[r] *= fb; sa[r] = __builtin_amdgcn_exp2f(sa[r] - ma); sb[r] = __builtin_amdgcn_exp2f(sb[r] - mb); }
                u32x4 pa0, pa1, pb0, pb1;
#pragma unroll
                for (int j = 0; j < 4; ++j) { pa0[j] = cvtpk(sa[2 * j], sa[2 * j + 1]); pa1[j] = cvtpk(sa[8 + 2 * j], sa[9 + 2 * j]); pb0[j] = cvtpk(sb[2 * j], sb[2 * j + 1]); pb1[j] = cvtpk(sb[8 + 2 * j], sb[9 + 2 * j]); }
                oa = MFMA(v0, BC(pa0), oa); oa = MFMA(v1, BC(pa1), oa);
                ob = MFMA(v0, BC(pb0), ob); ob = MFMA(v1, BC(pb1), ob);
            }
        }
    }
    const float la = __uint_as_float(__builtin_amdgcn_permlane32_swap(__float_as_uint(oa[8]), __float_as_uint(oa[8]), false, false)[1]);
    const float lb = __uint_as_float(__builtin_amdgcn_permlane32_swap(__float_as_uint(ob[8]), __float_as_uint(ob[8]), false, false)[1]);
    const float fa = ga * __builtin_amdgcn_rcpf(la), fb = gb * __builtin_amdgcn_rcpf(lb);
    const int head = 4 * c + w;
    const __amdgpu_buffer_rsrc_t rs = __builtin_amdgcn_make_buffer_rsrc(p.ws, 0, (int)WS_END, 0x00020000);
    const unsigned ob0 = (unsigned)WS_O + (unsigned)(rb * 80 + (head >> 3) * 10) * 1024;
    st16_dev(rs, ob0 + (head & 7) * 1024 + lane * 16, (u32x4){cvtpk(oa[0] * fa, oa[1] * fa), cvtpk(oa[2] * fa, oa[3] * fa), cvtpk(oa[4] * fa, oa[5] * fa), cvtpk(oa[6] * fa, oa[7] * fa)});
    st16_dev(rs, ob0 + 40960 + (head & 7) * 1024 + lane * 16, (u32x4){cvtpk(ob[0] * fb, ob[1] * fb), cvtpk(ob[2] * fb, ob[3] * fb), cvtpk(ob[4] * fb, ob[5] * fb), cvtpk(ob[6] * fb, ob[7] * fb)});
    {
        const u32x2 ua = {cvtpk(oa[8] * fa, oa[9] * fa), cvtpk(oa[10] * fa, oa[11] * fa)}, ub = {cvtpk(ob[8] * fb, ob[9] * fb), cvtpk(ob[10] * fb, ob[11] * fb)};
        const u32x2 uv = {__builtin_amdgcn_permlane32_swap(ua[0], ub[0], false, false)[0], __builtin_amdgcn_permlane32_swap(ua[1], ub[1], false, false)[0]};
        st8_dev(p.ws + ob0 + h * 40960 + (8 + ((head >> 2) & 1)) * 1024 + ((head & 1) * 32 + r32) * 16 + ((head >> 1) & 1) * 8, uv);
    }
    WAIT_VM(0);
    if (lane == 0) flag_set(p.ws, WS_OF + (rb * 4 + (head >> 3)) * 128 + (head & 7) * 8, tag);
    if (blockIdx.x == 0 && w == 0) {
        flags_wait<8, 32>(p.ws + WS_SEEN, 512, tag, lane);
        if (lane == 0) flag_set(p.ws, WS_EPOCH, tag);
    }
}

DI u64 ffn_up_wg(const Params& p, int rb2, int fs, u64 ep, char* lds) {
    const int tid = threadIdx.x, lane = tid & 63, w = __builtin_amdgcn_readfirstlane(tid >> 6);
    const int r32 = lane & 31, h = lane >> 5;
    const unsigned lds0 = (unsigned)(uintptr_t)lds;
    constexpr int XS_B = 16384, WS_B = 12288, WS_0 = 2 * XS_B;
    const int xrow = 8 * w + (lane >> 3);
    const unsigned xo = xrow * (D_ * 2) + (((lane & 7) ^ ((xrow >> 1) & 7)) << 4);
    unsigned wo_[3];
#pragma unroll
    for (int j = 0; j < 3; ++j) { const int idx = (w + 4 * j) * 64 + lane, r = idx % 384; wo_[j] = (r / 12) * (F_ * 2) + ((r % 12) ^ (idx >= 384 ? 2 : 0)) * 16; }
    const u16* xsrc = p.x + (size_t)rb2 * 128 * D_;
    const u16* gsrc = p.wg + fs * 96;
    const u16* usrc = p.wu + fs * 96;
    const u16* w1src = (w < 2) ? gsrc : usrc;
    const unsigned dw = lds0 + w * 1024;
#define UP_X(kb, m) glds16(xsrc + (kb) * 64 + (m) * 32 * D_, xo, dw + ((kb) & 1) * XS_B + (m) * 4096)
#define UP_W0(s) glds16(gsrc + (size_t)(s) * 32 * F_, wo_[0], dw + WS_0 + ((s) & 3) * WS_B)
#define UP_W1(s) glds16(w1src + (size_t)(s) * 32 * F_, wo_[1], dw + WS_0 + ((s) & 3) * WS_B + 4096)
#define UP_W2(s) glds16(usrc + (size_t)(s) * 32 * F_, wo_[2], dw + WS_0 + ((s) & 3) * WS_B + 8192)
#define UP_XA(kb) do { UP_X(kb, 0); UP_X(kb, 1); UP_X(kb, 2); UP_X(kb, 3); } while (0)
#define UP_WA(s) do { UP_W0(s); UP_W1(s); UP_W2(s); } while (0)
    __builtin_amdgcn_s_setprio(3);
    UP_XA(0); UP_WA(0); UP_WA(1); UP_XA(1); UP_WA(2);
    __builtin_amdgcn_s_setprio(1);
    const u64 tag = epoch_tag(ep);
    f32x16 acc[6];
#pragma unroll
    for (int i = 0; i < 6; ++i) acc[i] = zero16();
    const int wr = w >> 1, wc = w & 1, row = 64 * wr + r32, blk = (lane >> 4) & 1;
    lds_cptr xa[4], wa[3];
#pragma unroll
    for (int kk = 0; kk < 4; ++kk) xa[kk] = (lds_cptr)lds + row * 128 + (((2 * kk + h) ^ ((row >> 1) & 7)) << 4);
#pragma unroll
    for (int i = 0; i < 3; ++i) wa[i] = (lds_cptr)lds + WS_0 + blk * 6144 + (8 * h + ((lane & 15) >> 2)) * 192 + ((3 * wc + i) ^ blk) * 32 + (lane & 3) * 8;
    bf16x8 xf0[2], xf1[2], wf0[3], wf1[3];
#define UP_RX(q, rt) ldsread16(xa[(q) & 3] + (((q) >> 2) & 1) * XS_B + (rt) * 4096)
#define UP_RW(q, i) cat8(trread(wa[i] + (((q) >> 1) & 3) * WS_B + ((q) & 1) * 3072), trread(wa[i] + (((q) >> 1) & 3) * WS_B + ((q) & 1) * 3072 + 768))
#define UP_STEP(C, N, q, D0, D1, D2, D3, D4, D5) do { \
        acc[0] = MFMA(wf##C[0], xf##C[0], acc[0]); xf##N[0] = UP_RX(q, 0); D0; SBAR(); \
        acc[3] = MFMA(wf##C[0], xf##C[1], acc[3]); wf##N[0] = UP_RW(q, 0); D1; SBAR(); \
        acc[1] = MFMA(wf##C[1], xf##C[0], acc[1]); xf##N[1] = UP_RX(q, 1); D2; SBAR(); \
        acc[4] = MFMA(wf##C[1], xf##C[1], acc[4]); wf##N[1] = UP_RW(q, 1); D3; SBAR(); \
        acc[2] = MFMA(wf##C[2], xf##C[0], acc[2]); wf##N[2] = UP_RW(q, 2); D4; SBAR(); \
        acc[5] = MFMA(wf##C[2], xf##C[1], acc[5]); D5; SBAR(); } while (0)
#define NOP_ do {} while (0)
    WAIT_BAR(10);
    if (tid == 0) flag_set(p.ws, WS_SEEN + blockIdx.x * 8, tag);
    xf0[0] = UP_RX(0, 0); xf0[1] = UP_RX(0, 1);
#pragma unroll
    for (int i = 0; i < 3; ++i) wf0[i] = UP_RW(0, i);
    for (int it = 0; it < 4; ++it) {
        const int s0 = 4 * it, kb = 2 * it;
        UP_STEP(0, 1, 1, NOP_, UP_W0(s0 + 3), NOP_, UP_W1(s0 + 3), NOP_, UP_W2(s0 + 3));
        WAIT_BAR(10);
        UP_STEP(1, 0, 2, UP_W0(s0 + 4), NOP_, NOP_, UP_W1(s0 + 4), NOP_, NOP_);
        UP_STEP(0, 1, 3, UP_W2(s0 + 4), NOP_, NOP_, NOP_, NOP_, NOP_);
        WAIT_BAR(6);
        UP_STEP(1, 0, 4, UP_X(kb + 2, 0), UP_X(kb + 2, 1), NOP_, UP_X(kb + 2, 2), NOP_, UP_X(kb + 2, 3));
        UP_STEP(0, 1, 5, NOP_, UP_W0(s0 + 5), NOP_, UP_W1(s0 + 5), NOP_, UP_W2(s0 + 5));
        WAIT_BAR(10);
        UP_STEP(1, 0, 6, UP_W0(s0 + 6), NOP_, NOP_, UP_W1(s0 + 6), NOP_, NOP_);
        UP_STEP(0, 1, 7, UP_W2(s0 + 6), NOP_, NOP_, NOP_, NOP_, NOP_);
        WAIT_BAR(6);
        UP_STEP(1, 0, 0, UP_X(kb + 3, 0), UP_X(kb + 3, 1), NOP_, UP_X(kb + 3, 2), NOP_, UP_X(kb + 3, 3));
    }
    UP_STEP(0, 1, 1, UP_W0(19), UP_W1(19), UP_W2(19), NOP_, NOP_, NOP_);
    WAIT_BAR(3);
    UP_STEP(1, 0, 2, NOP_, NOP_, NOP_, NOP_, NOP_, NOP_);
    UP_STEP(0, 1, 3, NOP_, NOP_, NOP_, NOP_, NOP_, NOP_);
    UP_STEP(1, 0, 4, NOP_, NOP_, NOP_, NOP_, NOP_, NOP_);
    UP_STEP(0, 1, 5, NOP_, NOP_, NOP_, NOP_, NOP_, NOP_);
    WAIT_BAR(0);
    UP_STEP(1, 0, 6, NOP_, NOP_, NOP_, NOP_, NOP_, NOP_);
    UP_STEP(0, 1, 7, NOP_, NOP_, NOP_, NOP_, NOP_, NOP_);
#pragma unroll
    for (int i = 0; i < 6; ++i) acc[i] = MFMA(wf1[i % 3], xf1[i / 3], acc[i]);
    __builtin_amdgcn_s_setprio(3);
    const __amdgpu_buffer_rsrc_t rs = __builtin_amdgcn_make_buffer_rsrc(p.ws, 0, (int)WS_END, 0x00020000);
    const unsigned ho = (unsigned)WS_H + (unsigned)((rb2 * 4 + 2 * wr) * 96 + fs * 6 + 3 * wc) * 1024 + lane * 16;
#pragma unroll
    for (int j = 0; j < 6; ++j) {
        float hv[8];
#pragma unroll
        for (int r = 0; r < 8; ++r) hv[r] = __builtin_fmaxf(acc[j][r], 0.f) * acc[j][8 + r];
        st16_dev(rs, ho + (j / 3) * 96 * 1024 + (j % 3) * 1024, (u32x4){cvtpk(hv[0], hv[1]), cvtpk(hv[2], hv[3]), cvtpk(hv[4], hv[5]), cvtpk(hv[6], hv[7])});
    }
    WAIT_BAR(0);
    if (tid == 0) flag_set(p.ws, WS_HF + (rb2 * 4 + (fs >> 2)) * 128 + (fs & 3) * 8, tag);
    return tag;
}

#define MFMA16(a, b, c) __builtin_amdgcn_mfma_f32_16x16x32_bf16((a), (b), (c), 0, 0, 0)
DI void ld16_dev(bf16x8& d0, bf16x8& d1, const char* sbase, unsigned voff) {
    asm volatile("global_load_dwordx4 %0, %2, %3 sc1\n\tglobal_load_dwordx4 %1, %2, %3 offset:256 sc1" : "=&v"(d0), "=&v"(d1) : "v"(voff), "s"(sbase) : "memory");
}
struct OutOffs { unsigned wo_[5], wm_[5], wl_[5]; };
DI OutOffs out_offsets() {
    const int lane = threadIdx.x & 63;
    OutOffs o;
#pragma unroll
    for (int j = 0; j < 5; ++j) {
        const int idx = j * 64 + lane, r = idx / 10, ch = (idx % 10) * 16;
        o.wo_[j] = r * (D_ * 2) + ch; o.wm_[j] = ((r >> 4) * 20 + (r & 15)) * (D_ * 2) + ch; o.wl_[j] = ((r >> 2) * 20 + 16 + (r & 3)) * (D_ * 2) + ch;
        asm volatile("" : "+v"(o.wo_[j]), "+v"(o.wm_[j]), "+v"(o.wl_[j]));
    }
    return o;
}
DI void out_wg(const Params& p, int rb, int cn, u64 tag, const OutOffs& oo, char* lds) {
    const int tid = threadIdx.x, lane = tid & 63, w = __builtin_amdgcn_readfirstlane(tid >> 6);
    const int r16 = lane & 15, g = lane >> 4;
    const unsigned lds0 = (unsigned)(uintptr_t)lds + w * 20480;
    const unsigned (&wo_)[5] = oo.wo_; const unsigned (&wm_)[5] = oo.wm_; const unsigned (&wl_)[5] = oo.wl_;
    const u16* dsrc = p.wd + (size_t)w * 384 * D_ + cn * 80;
    const u16* osrc = p.wo + (size_t)w * 160 * D_ + cn * 80;
    const char* hsrc = p.ws + WS_H + ((size_t)(rb * 2) * 48 + w * 12) * 2048;
    const char* asrc = p.ws + WS_O + ((size_t)(rb * 2) * 40 + w * 10) * 1024;
    const unsigned lo16 = (g >> 1) * 1024 + (g & 1) * 512 + r16 * 16;
#define W_PIECES(s_, o_, d_) do { glds16(s_, o_[0], d_); glds16(s_, o_[1], d_ + 1024); glds16(s_, o_[2], d_ + 2048); glds16(s_, o_[3], d_ + 3072); glds16(s_, o_[4], d_ + 4096); } while (0)
#define W_DMA(u) do { const unsigned d_ = lds0 + ((u) & 3) * 5120; \
        if ((u) < 12) W_PIECES(dsrc + (size_t)(u) * 32 * D_, wo_, d_); else if ((u) < 16) W_PIECES(osrc + (size_t)((u) - 12) * 40 * D_, wm_, d_); else W_PIECES(osrc, wl_, d_); } while (0)
    __builtin_amdgcn_s_setprio(1);
    W_DMA(0); W_DMA(1);
    f32x4 acc[4][5];
    const f32x4 z4 = {0.f, 0.f, 0.f, 0.f};
#define ACC(u, rg, i) ((u) == 0 ? z4 : acc[rg][i])
    const lds_cptr wbase = (lds_cptr)lds + w * 20480 + (16 * (g >> 1) + 4 * (g & 1) + (r16 >> 2)) * 160 + (lane & 3) * 8;
    bf16x8 wf[2][5];
    bf16x8 af[5][4];
#define W_RD(u, i) wf[(u) & 1][i] = cat8(trread(wbase + ((u) & 3) * 5120 + (i) * 32), trread(wbase + ((u) & 3) * 5120 + (i) * 32 + 1280))
#define H_LD(u) do { ld16_dev(af[(u) & 3][0], af[(u) & 3][1], hsrc + (u) * 2048, lo16); ld16_dev(af[(u) & 3][2], af[(u) & 3][3], hsrc + 48 * 2048 + (u) * 2048, lo16); } while (0)
#define W_PIECE(u, i) glds16((u) < 12 ? (const void*)(dsrc + (size_t)(u) * 32 * D_) : (const void*)(osrc + (size_t)((u) - 12) * 40 * D_), (u) < 12 ? wo_[i] : wm_[i], lds0 + ((u) & 3) * 5120 + (i) * 1024)
#define UNIT_D(u, A) do { _Pragma("unroll") for (int i = 0; i < 5; ++i) { \
        acc[0][i] = MFMA16(wf[(u) & 1][i], A[0], ACC(u, 0, i)); acc[1][i] = MFMA16(wf[(u) & 1][i], A[1], ACC(u, 1, i)); W_PIECE((u) + 4, i); SBAR(); \
        acc[2][i] = MFMA16(wf[(u) & 1][i], A[2], ACC(u, 2, i)); acc[3][i] = MFMA16(wf[(u) & 1][i], A[3], ACC(u, 3, i)); W_RD((u) + 1, i); \
        if ((u) + 3 < 12 && i == 1) ld16_dev(af[((u) + 3) & 3][0], af[((u) + 3) & 3][1], hsrc + ((u) + 3) * 2048, lo16); \
        if ((u) + 3 < 12 && i == 3) ld16_dev(af[((u) + 3) & 3][2], af[((u) + 3) & 3][3], hsrc + 48 * 2048 + ((u) + 3) * 2048, lo16); \
        SBAR(); } } while (0)
#define UNIT(u, A) do { _Pragma("unroll") for (int i = 0; i < 5; ++i) { \
        _Pragma("unroll") for (int rg = 0; rg < 4; ++rg) acc[rg][i] = MFMA16(wf[(u) & 1][i], A[rg], acc[rg][i]); \
        if ((u) + 1 < 17) W_RD((u) + 1, i); SBAR(); } } while (0)
#define PINA(A) "+v"(A[0]), "+v"(A[1]), "+v"(A[2]), "+v"(A[3])
    {
        const char* fp = p.ws + ((lane & 4) ? WS_KVF + (blockIdx.x & 15) * 1024 + (rb * 4) * 8 : WS_HF + ((rb >> 1) * 4 + w) * 128) + (lane & 3) * 8;
#pragma unroll 1
        for (unsigned spin = 0; spin < SPIN_CAP; ++spin) { if (__all(ld8_dev(fp) == tag)) break; __builtin_amdgcn_s_sleep(12); }
    }
    WAIT_VM(0);
    float gv[4];
    asm volatile("global_load_dword %0, %4, %5 sc1\n\tglobal_load_dword %1, %4, %5 offset:64 sc1\n\tglobal_load_dword %2, %4, %5 offset:128 sc1\n\tglobal_load_dword %3, %4, %5 offset:192 sc1"
                 : "=&v"(gv[0]), "=&v"(gv[1]), "=&v"(gv[2]), "=&v"(gv[3]) : "v"(r16 * 4), "s"(p.ws + WS_G + rb * 256) : "memory");
    H_LD(0); W_DMA(2); H_LD(1); W_DMA(3); H_LD(2);
#pragma unroll
    for (int i = 0; i < 5; ++i) W_RD(0, i);
    __builtin_amdgcn_s_setprio(1);
#pragma unroll
    for (int u = 0; u < 12; ++u) {
        if (u == 0) asm volatile("s_waitcnt vmcnt(18) lgkmcnt(0)" : PINA(af[0]), PINA(gv) :: "memory");
        else if (u <= 9) asm volatile("s_waitcnt vmcnt(18) lgkmcnt(0)" : PINA(af[u & 3]) :: "memory");
        else if (u == 10) asm volatile("s_waitcnt vmcnt(14) lgkmcnt(0)" : PINA(af[u & 3]) :: "memory");
        else asm volatile("s_waitcnt vmcnt(10) lgkmcnt(0)" : PINA(af[u & 3]) :: "memory");
        SBAR();
        UNIT_D(u, af[u & 3]);
    }
    __builtin_amdgcn_s_setprio(3);
    asm volatile("s_waitcnt lgkmcnt(0)" ::: "memory");
    W_DMA(16);
#pragma unroll
    for (int rg = 0; rg < 4; ++rg) {
        const float gm = __uint_as_float(cvtpk(1.f - gv[rg], 0.f) << 16);
#pragma unroll
        for (int i = 0; i < 5; ++i) acc[rg][i] *= gm;
    }
    flags_wait<1, 6>(p.ws + WS_OF + (rb * 4 + w) * 128, 8, tag, lane);
#pragma unroll
    for (int v = 0; v < 5; ++v) { ld16_dev(af[v][0], af[v][1], asrc + v * 2048, lo16); ld16_dev(af[v][2], af[v][3], asrc + 40 * 1024 + v * 2048, lo16); }
    asm volatile("s_waitcnt vmcnt(16)" : PINA(af[0]) :: "memory"); SBAR(); UNIT(12, af[0]);
    asm volatile("s_waitcnt vmcnt(12)" : PINA(af[1]) :: "memory"); SBAR(); UNIT(13, af[1]);
    asm volatile("s_waitcnt vmcnt(8)" : PINA(af[2]) :: "memory"); SBAR(); UNIT(14, af[2]);
    asm volatile("s_waitcnt vmcnt(4)" : PINA(af[3]) :: "memory"); SBAR(); UNIT(15, af[3]);
    asm volatile("s_waitcnt vmcnt(0)" : PINA(af[4]) :: "memory"); SBAR(); UNIT(16, af[4]);
    LAS f32x4* mine = (LAS f32x4*)((lds_ptr)lds + w * 20480);
#pragma unroll
    for (int rg = 0; rg < 4; ++rg)
#pragma unroll
        for (int i = 0; i < 5; ++i) mine[(rg * 5 + i) * 64 + lane] = acc[rg][i];
    WAIT_BAR(0);
    const LAS f32x4* all = (const LAS f32x4*)lds + w * 5 * 64 + lane;
    u16* orow = p.out + (size_t)(rb * 64 + w * 16 + r16) * D_ + cn * 80 + 4 * g;
#pragma unroll
    for (int i = 0; i < 5; ++i) {
        const f32x4 s = (all[i * 64] + all[1280 + i * 64]) + (all[2560 + i * 64] + all[3840 + i * 64]);
        st8_dev(orow + 16 * i, (u32x2){cvtpk(s[0], s[1]), cvtpk(s[2], s[3])});
    }
}

__global__ __launch_bounds__(256, 2) void k_fused(Params p) {
    extern __shared__ __attribute__((aligned(16))) char lds[];
    const int b = blockIdx.x, j = b & 7, i = (b >> 3) & 31;
    const u64 ep = epoch_load(p.ws + WS_EPOCH);
    if (b < 256) {
        __builtin_amdgcn_s_setprio(2);
        attn_wg(p, 16 * (j >> 2) + (i >> 1), 2 * (j & 3) + (i & 1), ep, lds);
    } else {
        if (i < 16) { __builtin_amdgcn_s_setprio(3); kv_job(p, 8 * j + (i >> 1), i & 1, ep, lds); __builtin_amdgcn_s_setprio(1); }
        const OutOffs oo = out_offsets();
        const u64 tag = ffn_up_wg(p, 8 * (j >> 2) + (i >> 2), 4 * (j & 3) + (i & 3), ep, lds);
        out_wg(p, 4 * j + (i >> 3), i & 7, tag, oo, lds);
    }
}

extern "C" void kernel_launch(void* const* d_in, const int* in_sizes, int n_in, void* d_out, int out_size, void* d_ws, size_t ws_size,
                              hipStream_t stream) {
    Params p;
    p.x = (const u16*)d_in[0]; p.wq = (const u16*)d_in[1]; p.wk = (const u16*)d_in[2]; p.wv = (const u16*)d_in[3]; p.wo = (const u16*)d_in[4];
    p.wg = (const u16*)d_in[6]; p.wu = (const u16*)d_in[7]; p.wd = (const u16*)d_in[8]; p.gate_w = (const u16*)d_in[9];
    p.out = (u16*)d_out; p.ws = (char*)d_ws;
    (void)hipFuncSetAttribute((const void*)k_fused, hipFuncAttributeMaxDynamicSharedMemorySize, LDS_BYTES);
    k_fused<<<512, 256, LDS_BYTES, stream>>>(p);
}
```
